```python
import torch

def _softmax(x, axis=-1):
    return torch.softmax(x, dim=axis)

def _gelu(x):
    return torch.nn.functional.gelu(x, approximate='tanh')

def _silu(x):
    return torch.nn.functional.silu(x)

def _sigmoid(x):
    return torch.sigmoid(x)

def _layernorm(x, axis=-1, eps=1e-05):
    m = x.mean(dim=axis, keepdim=True)
    v = x.var(dim=axis, keepdim=True, unbiased=False)
    return (x - m) / torch.sqrt(v + eps)

def _scatter_add(tgt, idx, src, axis=0):
    return tgt.clone().index_add_(axis, idx, src)

def _segment_sum(src, seg, n):
    out = torch.zeros((n,) + src.shape[1:], dtype=src.dtype)
    return out.index_add_(0, seg, src)

def _pad(x, pw):
    flat = tuple((v for pair in reversed(pw) for v in pair))
    return torch.nn.functional.pad(x, flat)
kernel_name = 'parallel_sum__linear_attn_block__mixer_gmlp__prompt'

def setup_inputs():

    _w = lambda a: (lambda t: t.to(torch.bfloat16) if t.is_floating_point() else t)(torch.as_tensor(a))

    def _A_setup():
        B, S, D = (1, 2048, 576)
        return {'x': _w((lambda *s: torch.randn(*s, dtype=torch.bfloat16) * 0.05)(B, S, D)), 'wqkv': _w((lambda *s: torch.randn(*s, dtype=torch.bfloat16) * 1.6)(D, 3 * D)), 'wo': _w((lambda *s: torch.randn(*s, dtype=torch.bfloat16) * 0.2)(D, D))}
    _A_in = _A_setup()

    def _B_setup():
        B, S, D, F = (1, 2048, 576, 4608)
        Fh = F // 2
        return {'x': _w((lambda *s: torch.randn(*s, dtype=torch.bfloat16) * 0.05)(B, S, D)), 'w_in': _w((lambda *s: torch.randn(*s, dtype=torch.bfloat16) * 0.4)(D, 2 * Fh)), 'w_sp': _w(0.01 * (lambda *s: torch.randn(*s, dtype=torch.bfloat16) * 0.4)(S, S)), 'b_sp': _w(1.0 + 0.01 * (lambda *s: torch.randn(*s, dtype=torch.bfloat16) * 0.05)(S)), 'w_out': _w((lambda *s: torch.randn(*s, dtype=torch.bfloat16) * 0.05)(Fh, D))}
    _B_in = _B_setup()
    return {'A_x': _A_in['x'], 'A_wqkv': _A_in['wqkv'], 'A_wo': _A_in['wo'], 'B_x': _B_in['x'], 'B_w_in': _B_in['w_in'], 'B_w_sp': _B_in['w_sp'], 'B_b_sp': _B_in['b_sp'], 'B_w_out': _B_in['w_out']}

def reference(A_x, A_wqkv, A_wo, B_x, B_w_in, B_w_sp, B_b_sp, B_w_out):

    def _A_ref(x, wqkv, wo):
        B, S, D = x.shape
        H = 32
        Dh = D // H
        qkv = (lambda x, *d: x.permute(*d))((x @ wqkv).reshape(B, S, 3, H, Dh), 2, 0, 3, 1, 4)
        q, k, v = (qkv[0], qkv[1], qkv[2])
        phi = lambda t: torch.where(t > 0, t + 1, torch.exp(t)) + 1e-06
        pq, pk = (phi(q), phi(k))
        kv = torch.einsum('bhsd,bhse->bhde', pk, v)
        z = torch.einsum('bhsd->bhd', pk)
        num = torch.einsum('bhqd,bhde->bhqe', pq, kv)
        den = torch.einsum('bhqd,bhd->bhq', pq, z)[..., None] + 1e-06
        o = num / den
        return (lambda x, *d: x.permute(*d))(o, 0, 2, 1, 3).reshape(B, S, D) @ wo

    def _B_ref(x, w_in, w_sp, b_sp, w_out):
        B, S, D = x.shape
        F = w_in.shape[-1]
        Fh = F // 2
        z = _gelu(torch.einsum('bsd,df->bsf', x, w_in))
        z1 = z[..., :Fh].clone()
        z2 = _layernorm(z[..., Fh:], axis=-1)
        g = torch.einsum('ts,bsf->btf', w_sp, z2) + b_sp[None, :, None]
        return torch.einsum('bsf,fd->bsd', z1 * g, w_out)
    x = A_x
    xn = _layernorm(x, axis=-1)
    return x + _A_ref(xn, A_wqkv, A_wo) + _B_ref(xn, B_w_in, B_w_sp, B_b_sp, B_w_out)
```

```cpp
#include <hip/hip_runtime.h>
#include <hip/hip_bf16.h>
typedef unsigned short u16;
__device__ __forceinline__ float bf2f(u16 b){ return __uint_as_float(((unsigned)b)<<16); }
__device__ __forceinline__ u16 f2bf(float f){ unsigned u=__float_as_uint(f); u += 0x7fffu + ((u>>16)&1u); return (u16)(u>>16); }
constexpr int S=2048, D=576, FH=2304, F=4608, H=32, DH=18;

__global__ void n_ln(const u16* x, int ld, int n, u16* o, int ldo){
  int r=blockIdx.x; __shared__ float red[256];
  float s=0; for(int i=threadIdx.x;i<n;i+=256) s+=bf2f(x[(size_t)r*ld+i]);
  red[threadIdx.x]=s; __syncthreads(); for(int k=128;k>0;k>>=1){ if(threadIdx.x<k) red[threadIdx.x]+=red[threadIdx.x+k]; __syncthreads(); }
  float m=red[0]/n; __syncthreads();
  float q=0; for(int i=threadIdx.x;i<n;i+=256){ float d=bf2f(x[(size_t)r*ld+i])-m; q+=d*d; }
  red[threadIdx.x]=q; __syncthreads(); for(int k=128;k>0;k>>=1){ if(threadIdx.x<k) red[threadIdx.x]+=red[threadIdx.x+k]; __syncthreads(); }
  float rs=rsqrtf(red[0]/n+1e-5f);
  for(int i=threadIdx.x;i<n;i+=256) o[(size_t)r*ldo+i]=f2bf((bf2f(x[(size_t)r*ld+i])-m)*rs);
}
__global__ void n_gemm(const u16* A,int lda,const u16* B,int ldb,float* C,int ldc,int M,int N,int K){
  __shared__ float As[16][17], Bs[16][17];
  int tx=threadIdx.x&15, ty=threadIdx.x>>4; int r=blockIdx.y*16+ty, c=blockIdx.x*16+tx; float acc=0;
  for(int k0=0;k0<K;k0+=16){ As[ty][tx]=bf2f(A[(size_t)r*lda+k0+tx]); Bs[ty][tx]=bf2f(B[(size_t)(k0+ty)*ldb+c]); __syncthreads();
    for(int k=0;k<16;k++) acc+=As[ty][k]*Bs[k][tx]; __syncthreads(); }
  C[(size_t)r*ldc+c]=acc;
}
__global__ void n_gelu(const float* z,u16* o,int n){ int i=blockIdx.x*256+threadIdx.x; if(i<n){ float x=bf2f(f2bf(z[i])); float u=0.7978845608f*(x+0.044715f*x*x*x); o[i]=f2bf(0.5f*x*(1.f+tanhf(u))); } }
__device__ float phi(float t){ return (t>0? t+1.f : __expf(t))+1e-6f; }
__global__ void n_kv(const float* qkv,float* kv,float* zz){
  int h=blockIdx.x, t=threadIdx.x; if(t>=DH*DH+DH) return;
  float acc=0;
  if(t<DH*DH){ int d=t/DH,e=t%DH; for(int s=0;s<S;s++) acc+=phi(qkv[(size_t)s*1728+576+h*DH+d])*qkv[(size_t)s*1728+1152+h*DH+e]; kv[h*DH*DH+t]=acc; }
  else { int d=t-DH*DH; for(int s=0;s<S;s++) acc+=phi(qkv[(size_t)s*1728+576+h*DH+d]); zz[h*DH+d]=acc; }
}
__global__ void n_o(const float* qkv,const float* kv,const float* zz,u16* o){ int i=blockIdx.x*256+threadIdx.x; if(i>=S*D) return; int s=i/D,c=i%D,h=c/DH,e=c%DH;
  float num=0,den=0; for(int d=0;d<DH;d++){ float p=phi(qkv[(size_t)s*1728+h*DH+d]); num+=p*kv[h*DH*DH+d*DH+e]; den+=p*zz[h*DH+d]; }
  o[i]=f2bf(num/(den+1e-6f)); }
__global__ void n_y(const float* g,const u16* z,const u16* b,u16* y){ int i=blockIdx.x*256+threadIdx.x; if(i>=S*FH) return; int t=i/FH,f=i%FH; y[i]=f2bf(bf2f(z[(size_t)t*F+f])*(g[i]+bf2f(b[t]))); }
__global__ void n_fin(const u16* x,const float* a,const float* b,u16* o){ int i=blockIdx.x*256+threadIdx.x; if(i<S*D) o[i]=f2bf(bf2f(x[i])+a[i]+b[i]); }

extern "C" void kernel_launch(void* const* d_in, const int* in_sizes, int n_in, void* d_out, int out_size, void* d_ws, size_t ws_size, hipStream_t st) {
  const u16* x=(const u16*)d_in[0]; const u16* wqkv=(const u16*)d_in[1]; const u16* wo=(const u16*)d_in[2];
  const u16* win=(const u16*)d_in[4]; const u16* wsp=(const u16*)d_in[5]; const u16* bsp=(const u16*)d_in[6]; const u16* wout=(const u16*)d_in[7];
  char* w=(char*)d_ws; size_t off=0; auto al=[&](size_t b){ void* p=w+off; off+=(b+255)&~(size_t)255; return p; };
  u16* xn=(u16*)al(S*D*2); float* qkv=(float*)al((size_t)S*1728*4); float* zf=(float*)al((size_t)S*F*4); u16* z=(u16*)al((size_t)S*F*2);
  u16* z2n=(u16*)al((size_t)S*FH*2); float* g=(float*)al((size_t)S*FH*4); u16* y=(u16*)al((size_t)S*FH*2); float* kv=(float*)al(H*DH*DH*4); float* zz=(float*)al(H*DH*4);
  u16* o=(u16*)al(S*D*2); float* ao=(float*)al(S*D*4); float* bo=(float*)al(S*D*4);
  n_ln<<<S,256,0,st>>>(x,D,D,xn,D);
  n_gemm<<<dim3(1728/16,S/16),256,0,st>>>(xn,D,wqkv,1728,qkv,1728,S,1728,D);
  n_gemm<<<dim3(F/16,S/16),256,0,st>>>(xn,D,win,F,zf,F,S,F,D);
  n_gelu<<<S*F/256,256,0,st>>>(zf,z,S*F);
  n_ln<<<S,256,0,st>>>(z+FH,F,FH,z2n,FH);
  n_gemm<<<dim3(FH/16,S/16),256,0,st>>>(wsp,S,z2n,FH,g,FH,S,FH,S);
  n_y<<<S*FH/256,256,0,st>>>(g,z,bsp,y);
  n_kv<<<H,384,0,st>>>(qkv,kv,zz);
  n_o<<<S*D/256,256,0,st>>>(qkv,kv,zz,o);
  n_gemm<<<dim3(D/16,S/16),256,0,st>>>(o,D,wo,D,ao,D,S,D,D);
  n_gemm<<<dim3(D/16,S/16),256,0,st>>>(y,FH,wout,D,bo,D,S,D,FH);
  n_fin<<<S*D/256,256,0,st>>>(x,ao,bo,(u16*)d_out);
}
```

```cpp
#include <hip/hip_runtime.h>
#include <hip/hip_bf16.h>
#include <type_traits>

namespace mk {
typedef unsigned short u16;
typedef short bf16x8 __attribute__((ext_vector_type(8)));
typedef short s16x4 __attribute__((ext_vector_type(4)));
typedef float f32x4 __attribute__((ext_vector_type(4)));
typedef float f32x2 __attribute__((ext_vector_type(2)));
typedef unsigned u32x4 __attribute__((ext_vector_type(4)));
typedef unsigned u32x2 __attribute__((ext_vector_type(2)));
typedef __bf16 bf16x2_t __attribute__((ext_vector_type(2)));
#define LAS __attribute__((address_space(3)))
#define GAS __attribute__((address_space(1)))
#define DI __device__ __forceinline__
template <int V> using IC = std::integral_constant<int, V>;
template <int B, int E, class F> __device__ __forceinline__ void sfor(F&& f) { if constexpr (B < E) { f(IC<B>{}); sfor<B + 1, E>(f); } }

constexpr int S = 2048, D = 576, FH = 2304;
constexpr int C_SZ = 18432, R_OFF = 73728, R_SZ = 16384, MISC = 139264;
constexpr int L_STASH = R_OFF, L_KVS = R_OFF + 36864;
constexpr int L_XSTAT = MISC + 8192, L_CS = L_XSTAT + 1024, L_ZPART = L_CS + 768, L_ZSTAT = L_ZPART + 2048, L_KVF = L_ZSTAT + 1024, L_BIAS = L_KVF + 6144, L_FLG = L_BIAS + 256, L_DUMMY = L_FLG + 256;
constexpr int L_CSP = L_DUMMY + 1024;
constexpr int L_YOWN = MISC;
static_assert(L_YOWN + 16384 <= L_BIAS, "lds");
constexpr int LDS_TOTAL = 163840;
static_assert(L_CSP + 2304 <= LDS_TOTAL, "lds");
constexpr int W_EPOCH = 0, W_TMO = 256, W_FLAGB = 4096, W_FLAGO = 8192, W_FLAGD = 16384, W_STAMP = 32768;
constexpr int W_STATS = 1 << 20, W_KVP = 2 << 20, W_CSG = 3 << 20, W_ZF = 4 << 20, W_YF = 16 << 20, W_PART = 32 << 20, W_END = 52 << 20;
constexpr int KVP_STRIDE = 2944, YF_GRP = 12 * 16384, PART_SRC = 9216, OOB = 0x40000000;
constexpr int SC1 = 16;
constexpr unsigned SPIN_MAX = 1u << 16;
#define FIN_SLEEP 16
#define FIN_POLL 4
#define ABL 0
#define FLAGB_STEP 20

template <int N> DI void waitv() { asm volatile("s_waitcnt vmcnt(%0)" ::"n"(N) : "memory"); }
DI void waitl0() { __builtin_amdgcn_s_waitcnt(0xC07F); asm volatile("" ::: "memory"); }
DI void bar() { asm volatile("" ::: "memory"); __builtin_amdgcn_s_barrier(); asm volatile("" ::: "memory"); }
#define SB() __builtin_amdgcn_sched_barrier(0)
DI f32x4 mfma(bf16x8 a, bf16x8 b, f32x4 c) { return __builtin_amdgcn_mfma_f32_16x16x32_bf16(a, b, c, 0, 0, 0); }
DI void mfa(f32x4& c, const bf16x8& a, const bf16x8& b) { asm volatile("v_mfma_f32_16x16x32_bf16 %0, %1, %2, %0" : "+v"(c) : "v"(a), "v"(b)); }
DI void mf0(f32x4& c, const bf16x8& a, const bf16x8& b) { asm volatile("v_mfma_f32_16x16x32_bf16 %0, %1, %2, 0" : "=&v"(c) : "v"(a), "v"(b)); }
DI int opq(int v) { asm volatile("" : "+v"(v)); return v; }
template <int N> DI void accfence(f32x4 (&a)[N][2]) {
  if constexpr (N == 5) asm volatile("s_nop 7\n\ts_nop 1" : "+v"(a[0][0]), "+v"(a[0][1]), "+v"(a[1][0]), "+v"(a[1][1]), "+v"(a[2][0]), "+v"(a[2][1]), "+v"(a[3][0]), "+v"(a[3][1]), "+v"(a[4][0]), "+v"(a[4][1]));
  else asm volatile("s_nop 7\n\ts_nop 1" : "+v"(a[0][0]), "+v"(a[0][1]), "+v"(a[1][0]), "+v"(a[1][1]), "+v"(a[2][0]), "+v"(a[2][1]), "+v"(a[3][0]), "+v"(a[3][1]));
}
DI unsigned pk2(float lo, float hi) { f32x2 v = {lo, hi}; return __builtin_bit_cast(unsigned, __builtin_convertvector(v, bf16x2_t)); }
DI float blo(unsigned u) { return __uint_as_float(u << 16); }
DI float bhi(unsigned u) { return __uint_as_float(u & 0xffff0000u); }
DI float dot2(unsigned a, unsigned b, float c) { return __builtin_amdgcn_fdot2_f32_bf16(__builtin_bit_cast(bf16x2_t, a), __builtin_bit_cast(bf16x2_t, b), c, false); }
DI float gelu(float v) {
  const float p = __builtin_fmaf(v * v, 0.10294324f, 2.3022082f);
  const float e = __builtin_amdgcn_exp2f(v * p);
  const float r = __builtin_amdgcn_rcpf(1.0f + e);
  return __builtin_fmaf(-v, r, v);
}
DI f32x2 gelu2(f32x2 v) {
  const f32x2 a = v * __builtin_elementwise_fma(v * v, (f32x2){0.10294324f, 0.10294324f}, (f32x2){2.3022082f, 2.3022082f});
  const f32x2 d = (f32x2){__builtin_amdgcn_exp2f(a.x), __builtin_amdgcn_exp2f(a.y)} + 1.0f;
  const f32x2 r = {__builtin_amdgcn_rcpf(d.x), __builtin_amdgcn_rcpf(d.y)};
  return __builtin_elementwise_fma(-v, r, v);
}
DI unsigned pk2v(f32x2 v) { return __builtin_bit_cast(unsigned, __builtin_convertvector(v, bf16x2_t)); }
DI f32x2 unpk(unsigned u) { return (f32x2){__uint_as_float(u << 16), __uint_as_float(u & 0xffff0000u)}; }
DI f32x2 lo2(f32x4 a) { return (f32x2){a.x, a.y}; }
DI f32x2 hi2(f32x4 a) { return (f32x2){a.z, a.w}; }
DI f32x2 fma2(f32x2 a, f32x2 b, f32x2 c) { return __builtin_elementwise_fma(a, b, c); }
DI f32x2 phi2(f32x2 t) {
  const f32x2 a = t * 1.44269504f, p = t + 1.0f;
  const f32x2 r = {t.x > 0.f ? p.x : __builtin_amdgcn_exp2f(a.x), t.y > 0.f ? p.y : __builtin_amdgcn_exp2f(a.y)};
  return r + 1e-6f;
}
DI float phi(float t) { return (t > 0.f ? t + 1.0f : __builtin_amdgcn_exp2f(t * 1.44269504f)) + 1e-6f; }
template <int CTRL> DI float dppadd(float v) { return v + __builtin_bit_cast(float, __builtin_amdgcn_update_dpp(0, __builtin_bit_cast(int, v), CTRL, 0xf, 0xf, true)); }
DI float row16sum(float v) { v = dppadd<0x128>(v); v = dppadd<0x124>(v); v = dppadd<0x122>(v); v = dppadd<0x121>(v); return v; }
DI float hsum32(float v) { const unsigned u = __float_as_uint(v); const auto r = __builtin_amdgcn_permlane32_swap(u, u, false, false); return __uint_as_float(r[0]) + __uint_as_float(r[1]); }
DI float hsum16(float v) { const unsigned u = __float_as_uint(v); const auto r = __builtin_amdgcn_permlane16_swap(u, u, false, false); return __uint_as_float(r[0]) + __uint_as_float(r[1]); }
DI float gsum(float v) { return hsum32(hsum16(v)); }
typedef __amdgpu_buffer_rsrc_t rsrc_t;
DI rsrc_t mkrs(const void* p, int bytes) { return __builtin_amdgcn_make_buffer_rsrc((void*)p, 0, bytes, 0x00020000); }
template <int AUX> DI void dma16(rsrc_t rs, LAS char* dst, int voff, int soff) {
  const unsigned m = (unsigned)(size_t)dst;
  if constexpr (AUX == 0) asm volatile("s_mov_b32 m0, %0\n\ts_nop 0\n\tbuffer_load_dwordx4 %1, %2, %3 offen lds" ::"s"(m), "v"(voff), "s"(rs), "s"(soff) : "memory");
  else asm volatile("s_mov_b32 m0, %0\n\ts_nop 0\n\tbuffer_load_dwordx4 %1, %2, %3 offen sc1 lds" ::"s"(m), "v"(voff), "s"(rs), "s"(soff) : "memory");
}
DI void dma4(rsrc_t rs, LAS char* dst, int voff, int soff) {
  const unsigned m = (unsigned)(size_t)dst;
  asm volatile("s_mov_b32 m0, %0\n\ts_nop 0\n\tbuffer_load_dword %1, %2, %3 offen sc1 lds" ::"s"(m), "v"(voff), "s"(rs), "s"(soff) : "memory");
}
template <int AUX> DI void st16(u32x4 v, rsrc_t rs, int voff, int soff) { __builtin_amdgcn_raw_buffer_store_b128(v, rs, voff, soff, AUX); asm volatile("s_nop 1" ::"v"(v) : "memory"); }
#define HLOAD(dst, voff, rs, soff) asm volatile("buffer_load_dwordx4 %0, %1, %2, %3 offen sc1 ; HIDDEN_LOAD" : "=v"(dst) : "v"(voff), "s"(rs), "s"(soff) : "memory")
DI bf16x8 trd(LAS char* a) {
  const s16x4 lo = __builtin_amdgcn_ds_read_tr16_b64_v4i16((LAS s16x4*)a);
  const s16x4 hi = __builtin_amdgcn_ds_read_tr16_b64_v4i16((LAS s16x4*)(a + 8 * 288));
  return __builtin_shufflevector(lo, hi, 0, 1, 2, 3, 4, 5, 6, 7);
}
DI bf16x8 cat(u32x2 a, u32x2 b) { u32x4 v = {a.x, a.y, b.x, b.y}; return __builtin_bit_cast(bf16x8, v); }

struct Ptrs { const u16 *x, *wqkv, *wo, *win, *wsp, *bsp, *wout; u16* out; char* ws; };

#define STAMP(k)

DI void poll_flags(char* ws, int off, bool active, unsigned tag) {
  GAS unsigned* p = (GAS unsigned*)(ws + off);
  _Pragma("unroll 1") for (unsigned sp = 0;; ++sp) {
    const unsigned v = __hip_atomic_load(p, __ATOMIC_RELAXED, __HIP_MEMORY_SCOPE_AGENT);
    if (__all(!active || v == tag)) break;
    if (sp > SPIN_MAX) { __hip_atomic_store((GAS unsigned*)(ws + W_TMO), 1u, __ATOMIC_RELAXED, __HIP_MEMORY_SCOPE_AGENT); break; }
    __builtin_amdgcn_s_sleep(2);
  }
  asm volatile("" ::: "memory");
}
DI void set_flag(char* ws, int off, unsigned tag) { __hip_atomic_store((GAS unsigned*)(ws + off), tag, __ATOMIC_RELAXED, __HIP_MEMORY_SCOPE_AGENT); }

template <int H>
DI void body(const Ptrs& P, LAS char* lds, const int wm, const int lane0, const int pi, const int pj, unsigned epoch) {
  constexpr int NF = H ? 4 : 5, NB = H ? 5 : 0;
  int lane = lane0, fr = lane & 15, g = lane >> 4;
  auto relane = [&]() { lane = opq(lane0); fr = lane & 15; g = lane >> 4; };
  const int gc = pj >> 2, gu = pj & 3, qo = 4 * (pj & 1);
  const int rot = (pi + 2 * pj) % 9;
  const rsrc_t rsX = mkrs(P.x + (size_t)pi * 128 * D, 128 * D * 2), rsWin = mkrs(P.win, D * 2 * FH * 2), rsQkv = mkrs(P.wqkv, D * 3 * D * 2);
  const rsrc_t rsSp = mkrs(P.wsp + (size_t)pi * 128 * S, 128 * S * 2), rsWs = mkrs(P.ws, W_END);
  const rsrc_t rsWout = mkrs(P.wout, FH * D * 2), rsWo = mkrs(P.wo, D * D * 2);
  const int yb = W_YF + (pi * 4 + gc) * YF_GRP;
  STAMP(0);

  auto rvoff = [&](int stride) { const int l = opq(lane0); return (8 * (l >> 5) + ((l >> 3) & 3)) * stride + ((l & 7) ^ (l >> 4)) * 16; };
  int vA[5], vB[5];
  auto cvoff = [&](int (&v)[5], int kind) {
    const int l = opq(lane0);
    const int t = kind == 0 ? pj : gu, b0 = 72 * pj - 8 * (pj & 1);
    const int a0 = 256 * t, a1 = (kind == 0 ? 4096 : 1024) + 32 * t - 128, a2 = 256 * t - 32;
#pragma unroll
    for (int idx = 0; idx < 5; ++idx) {
      const unsigned n = 64 * (wm + 4 * idx) + l, Pp = __umul24(n, 3641u) >> 16, cc = n - __umul24(Pp, 18u);
      unsigned k; int add;
      if (kind == 2) k = (Pp & ~24u) | ((Pp & 8u) << 1) | ((Pp & 16u) >> 1); else k = (Pp & ~12u) | ((Pp & 4u) << 1) | ((Pp & 8u) >> 1);
      if (kind != 1) { add = a0; add = cc >= 8 ? a1 : add; add = cc >= 10 ? a2 : add; }
      else { add = 1152; add = cc >= 4 ? 2304 - 64 : add; add = cc >= 8 ? 1152 - 64 : add; add = cc >= 9 ? 2304 - 80 : add; add = cc >= 10 ? -160 : add; add = cc >= 15 ? OOB : add; add += b0; }
      v[idx] = (int)(__umul24(k, kind == 0 ? 9216u : kind == 1 ? 3456u : 1152u) + 16u * cc) + add;
    }
  };
  auto ksl = [&](int s) { int k = rot + s; return k >= 9 ? k - 9 : k; };
  auto g3stage = [&](int q) { return q < 3 ? q : q == 11 ? 3 : 4 + ((2 * gu + (q - 3)) & 7); };
  auto ring = [&](int st, int idx, int dyn = 0) {
    const int slot = st & 3;
    if constexpr (H == 1) {
      if (idx == 4 && wm >= 2) return;
      LAS char* dst = lds + slot * C_SZ + (wm + 4 * idx) * 1024;
      if (st < 9) dma16<0>(rsWin, dst, vA[idx], 4608 + ksl(st) * (64 * 9216));
      else if (st < 18) dma16<0>(rsQkv, dst, vB[idx], ksl(st - 9) * (64 * 3456));
      else if (st < 27) dma16<0>(rsWin, dst, vA[idx], ksl(st - 18) * (64 * 9216));
      else if (st < 59) dma16<SC1>(rsWs, dst, vB[0], W_ZF + (pj * 32 + dyn) * C_SZ + (wm + 4 * idx) * 1024);
      else { const int q = st - 59;
        if (q < 3) dma16<0>(rsWo, dst, vA[idx], (144 * gc + 64 * q) * 1152);
        else if (q == 11) dma16<0>(rsWout, dst, vA[idx], (2048 + 64 * gc) * 1152);
        else dma16<0>(rsWout, dst, vA[idx], (512 * gc + 64 * (g3stage(q) - 4)) * 1152); }
    } else {
      if (idx == 4) return;
      LAS char* dst = lds + R_OFF + slot * R_SZ + (4 * wm + idx) * 1024;
      const int rb = 32 * wm + 16 * (idx & 1) + 4 * (idx >> 1);
      if (st < 9) dma16<0>(rsX, dst, vA[idx & 1], rb * 1152 + ksl(st) * 128);
      else if (st < 27) return;
      else if (st < 59) dma16<0>(rsSp, dst, vA[idx & 1], rb * 4096 + dyn * 128);
      else dma16<SC1>(rsWs, dst, vB[0], yb + g3stage(st - 59) * 16384 + (4 * wm + idx) * 1024);
    }
  };
  auto ring_all = [&](int st) { ring(st, 4); ring(st, 0); ring(st, 1); ring(st, 2); ring(st, 3); };
  auto ring_pos = [&](int st, int k, int nf) {
    const int dyn = st - 27;
    if (nf == 5) { ring(st, k == 0 ? 4 : k - 1, dyn); }
    else if (nf == 4) { if (k == 0) ring(st, 4, dyn); ring(st, k, dyn); }
    else { if (k == 0) { ring(st, 4, dyn); ring(st, 0, dyn); } else if (k == 1) { ring(st, 1, dyn); ring(st, 2, dyn); } else ring(st, 3, dyn); }
  };
  if constexpr (H == 1) cvoff(vA, 0); else { vA[0] = rvoff(1152); vA[1] = vA[0] ^ 64; }
  ring_all(0); ring_all(1);
  STAMP(17);
  if constexpr (H == 1) cvoff(vB, 1);
  else {
    *(LAS u32x4*)(lds + L_KVF + (wm * 64 + lane) * 16) = (u32x4){0, 0, 0, 0};
    if (wm < 2) *(LAS u32x4*)(lds + L_KVF + 4096 + (wm * 64 + lane) * 16) = (u32x4){0, 0, 0, 0};
  }
  LAS char *aR0, *aR1, *aC0, *aC1;
  auto mk_aR = [&](int par) { const int l = opq(lane0), r = l & 15, o = ((32 * wm + r) * 128 + (((l >> 4) ^ ((r >> 1) & 7)) * 16)) ^ (par * 64); aR0 = lds + R_OFF + o; aR1 = lds + R_OFF + (o ^ 64); };
  auto mk_aC = [&](int par) { const int l = opq(lane0); LAS char* a = lds + (16 * (l >> 5) + 4 * ((l >> 4) & 1) + ((l & 15) >> 2)) * 288 + (l & 3) * 8 + NB * 32;
    aC0 = a + par * (32 * 288); aC1 = a + (par ^ 1) * (32 * 288); };
  asm volatile("" : "+s"(epoch));
  const unsigned tag = epoch + 1u;
  unsigned ONES = 0x3f803f80u; asm volatile("" : "+s"(ONES));
  auto check_flags = [&](int n, int off) { const int l = opq(lane0); const unsigned v = *(LAS unsigned*)(lds + L_FLG + l * 4);
    if (!__all(l >= n || v == tag)) { STAMP(18); poll_flags(P.ws, off, l < n, tag); } };

  bf16x8 xf[9][2][2];
  f32x4 acc[NF][2];
  float xs1 = 0.f, xs2 = 0.f;
  u32x4 gl[16];

  auto pass = [&](auto PASSc) {
    constexpr int PASS = decltype(PASSc)::value;
    constexpr int NFP = (PASS == 1 && H == 1) ? 3 : NF, ST0 = 9 * PASS, NU = 9 * NFP;
    constexpr bool TR = (PASS == 2) || (PASS == 1 && H == 1);
    constexpr int L_LAND = PASS == 0 ? R_OFF + R_SZ : PASS == 1 ? L_KVS : R_OFF + 2 * R_SZ;
    const int csg = W_CSG + (PASS * 16 + pj) * (9 * 1152);
    bf16x8 bq[3][2];
    auto rdB = [&](int u) { const int s = u / NFP, n = u % NFP, o = ((ST0 + s) & 3) * C_SZ + n * 32; bq[u % 3][0] = trd(aC0 + o); bq[u % 3][1] = trd(aC1 + o); };
    auto rdX = [&](int s) {
#pragma unroll
      for (int it = 0; it < 2; ++it) { xf[s][it][0] = *(LAS bf16x8*)(aR0 + (s & 3) * R_SZ + it * 2048); xf[s][it][1] = *(LAS bf16x8*)(aR1 + (s & 3) * R_SZ + it * 2048); } };
#pragma unroll
    for (int n = NFP; n < NF; ++n) { acc[n][0] = (f32x4){0, 0, 0, 0}; acc[n][1] = (f32x4){0, 0, 0, 0}; }
    mk_aC(wm & 1);
    if (PASS == 0) { mk_aR(wm & 1); rdX(0); }
    rdB(0); rdB(1);
    if (PASS == 0) ring_all(2);
    LAS char* csw; { const int l = opq(lane0); csw = lds + L_CSP + (NB * 16 + (l & 15)) * 16 + ((wm & 1) * 2 + ((l >> 4) & 1)) * 4; }
    SB();
    sfor<0, NU>([&](auto Uc) __attribute__((always_inline)) {
      constexpr int u = decltype(Uc)::value, s = u / NFP, n = u % NFP, sg = ST0 + s;
      if (n == NFP - 2) {
        if constexpr (H == 1) {
          if (sg == 18 || sg == 19) waitv<13>();
          else if (sg == 16) asm volatile("s_waitcnt vmcnt(4) ; HIDDEN_WAIT" : "+v"(gl[0]), "+v"(gl[1]), "+v"(gl[2]), "+v"(gl[3]), "+v"(gl[4]), "+v"(gl[5]), "+v"(gl[6]), "+v"(gl[7]) :: "memory");
          else waitv<4>();
        } else {
          if (sg == 2) waitv<5>(); else if (sg <= 6) waitv<4>(); else if (sg == 7 || sg == 16) waitv<0>(); else if (sg == FLAGB_STEP) waitv<1>(); else if (sg == 25) waitv<20>();
          else if (sg == 26) asm volatile("s_waitcnt vmcnt(4) ; HIDDEN_WAIT" : "+v"(gl[0]), "+v"(gl[1]), "+v"(gl[2]), "+v"(gl[3]), "+v"(gl[4]), "+v"(gl[5]), "+v"(gl[6]), "+v"(gl[7]),
                                          "+v"(gl[8]), "+v"(gl[9]), "+v"(gl[10]), "+v"(gl[11]), "+v"(gl[12]), "+v"(gl[13]), "+v"(gl[14]), "+v"(gl[15]) :: "memory");
        }
        if (s == 1 || (s == 8 && H == 1) || (sg == 18 && H == 0)) waitl0();
        bar();
        if constexpr (H == 0) {
          if (s == 1) { const int t = wm * 64 + opq(lane0), tc = min(t, 143);
            const f32x4 c = *(LAS f32x4*)(lds + L_CSP + tc * 16);
            __builtin_amdgcn_raw_buffer_store_b64((u32x2){__float_as_uint((c.x + c.y) + (c.z + c.w)), tag}, rsWs, (t < 144 && pi < 9) ? t * 8 : OOB, csg + rot * 1152, SC1); }
          if (s == 5) { const int vo = opq(lane0) * 16;
#pragma unroll
            for (int k = 0; k < 3; ++k) dma16<SC1>(rsWs, lds + L_LAND + (wm + 4 * k) * 1024, vo, csg + (wm + 4 * k) * 1024); }
          if (sg == 9 && wm < 2) { const int r = wm * 64 + opq(lane0); const f32x2 a = *(LAS f32x2*)(lds + L_ZPART + r * 8) + *(LAS f32x2*)(lds + L_ZPART + 1024 + r * 8);
            st16<SC1>((u32x4){__float_as_uint(a.x), tag, __float_as_uint(a.y), tag}, rsWs, r * 16, W_STATS + (pi * 16 + pj) * 2048); }
          if (PASS == 1 && s >= 2 && s < 5) { const int vo = opq(lane0) * 16;
#pragma unroll
            for (int k = 0; k < 3; ++k) st16<SC1>((u32x4){~0u, ~0u, ~0u, ~0u}, rsWs, vo, W_PART + ((pi * 16 + pj) * 4 + wm) * PART_SRC + ((s - 2) * 3 + k) * 1024); }
          if (sg == 18) {
            const int t = wm * 64 + opq(lane0), rd = (t * 205) >> 10, e0 = 4 * (t - 5 * rd), hd = rd >= 18, cd = min(qo + rd, 39);
            LAS char* const a = lds + L_KVS + (cd >> 4) * 3072 + ((cd >> 2) & 3) * 256 + (cd & 3) * 4; u32x4 o;
#pragma unroll
            for (int k = 0; k < 4; ++k) { const int e = e0 + k, ce = qo + 18 * hd + e; int off = (ce >> 4) * 1024 + (ce & 15) * 16; off = ce >= 32 ? off + 128 : off; off = e >= 18 ? 2048 : off;
              o[k] = __float_as_uint((*(LAS float*)(a + off) + *(LAS float*)(a + off + 9216)) + (*(LAS float*)(a + off + 18432) + *(LAS float*)(a + off + 27648))); }
            st16<SC1>(o, rsWs, t < 180 ? t * 16 : OOB, W_KVP + (pj * 16 + pi) * KVP_STRIDE); }
          if (sg == FLAGB_STEP) { const int l = opq(lane0), li = (wm & 1) * 64 + l;
            if (wm == 0 && l == 0) set_flag(P.ws, W_FLAGB + pj * 128 + pi * 4, tag);
            if (wm < 2) dma4(rsWout, lds + L_DUMMY, li < 81 ? li * 128 : OOB, blockIdx.x * 81 * 128);
            else if (wm == 2) dma4(rsWo, lds + L_DUMMY, l < 21 ? l * 128 : OOB, blockIdx.x * 21 * 128); }
          if (sg == 23) { vA[0] = rvoff(4096); vA[1] = vA[0] ^ 64; }
        } else {
          if (s == 7) {
            const int t = min(wm * 64 + opq(lane0), 143); float c = 0.f; bool ok = true;
#pragma unroll
            for (int k = 0; k < 9; ++k) { const u32x2 v = *(LAS u32x2*)(lds + L_LAND + k * 1152 + t * 8); c += __uint_as_float(v.x); ok &= v.y == tag; }
            _Pragma("unroll 1") for (unsigned sp = 0; !__all(ok); ++sp) {
              STAMP(20);
              if (sp > SPIN_MAX) { set_flag(P.ws, W_TMO, 1u); break; }
              c = 0.f; ok = true;
#pragma unroll
              for (int k = 0; k < 9; ++k) { const u32x2 v = __builtin_amdgcn_raw_buffer_load_b64(rsWs, t * 8, csg + k * 1152, SC1); c += __uint_as_float(v.x); ok &= v.y == tag; }
            }
            *(LAS float*)(lds + L_CS + t * 4) = c; }
          if (sg == 15) { const int l = opq(lane0), vo = (l >> 5) * 16384 + (32 * wm + (l & 31)) * 16;
#pragma unroll
            for (int k = 0; k < 8; ++k) HLOAD(gl[k], vo, rsWs, W_STATS + (pi * 16 + k) * 2048); }
          if (sg == 16) {
            const int l = opq(lane0), vo = (l >> 5) * 16384 + (32 * wm + (l & 31)) * 16;
            float s1 = 0.f, s2 = 0.f; bool ok = true;
#pragma unroll
            for (int k = 0; k < 8; ++k) { ok &= (gl[k].y == tag) & (gl[k].w == tag); s1 += __uint_as_float(gl[k].x); s2 += __uint_as_float(gl[k].z); }
            _Pragma("unroll 1") for (unsigned sp = 0; !__all(ok); ++sp) {
              STAMP(19);
              if (sp > SPIN_MAX) { set_flag(P.ws, W_TMO, 1u); break; }
              s1 = 0.f; s2 = 0.f; ok = true;
#pragma unroll
              for (int k = 0; k < 8; ++k) { const u32x4 v = __builtin_amdgcn_raw_buffer_load_b128(rsWs, vo, W_STATS + (pi * 16 + k) * 2048, SC1);
                ok &= (v.y == tag) & (v.w == tag); s1 += __uint_as_float(v.x); s2 += __uint_as_float(v.z); }
            }
            s1 = hsum32(s1); s2 = hsum32(s2);
            const float mu = s1 * (1.0f / FH), var = fmaxf(s2 * (1.0f / FH) - mu * mu, 0.f), rs = __builtin_amdgcn_rsqf(var + 1e-5f);
            *(LAS float*)(lds + L_ZSTAT + (l >> 5) * 512 + (32 * wm + (l & 31)) * 4) = (l >> 5) ? rs : -mu * rs; }
          if (sg == 23) { vB[0] = opq(lane0) * 16; if (wm == 0) dma4(rsWs, lds + L_FLG, vB[0] >> 2, W_FLAGB + pj * 128); }
        }
        if (sg == 24) {
          check_flags(16, W_FLAGB + pj * 128 + (lane0 & 15) * 4);
          if constexpr (H == 0) { const int t = wm * 64 + opq(lane0), vo = t < 180 ? t * 16 : OOB;
            if (blockIdx.x == 0 && t == 0) set_flag(P.ws, W_EPOCH, tag);
#pragma unroll
            for (int k = 0; k < 16; ++k) HLOAD(gl[k], vo, rsWs, W_KVP + (pj * 16 + k) * KVP_STRIDE); }
        }
        SB();
      }
      if (u + 2 < NU) rdB(u + 2);
      if (PASS == 0 && n == NFP - 2 && s < 8) rdX(s + 1);
      { const int d = u - (NFP - 2); if (d >= 0) ring_pos(ST0 + d / NFP + 3, d % NFP, NFP); }
#pragma unroll
      for (int it = 0; it < 2; ++it)
#pragma unroll
        for (int hh = 0; hh < 2; ++hh) { const bf16x8 &oa = TR ? bq[u % 3][hh] : xf[s][it][hh], &ob = TR ? xf[s][it][hh] : bq[u % 3][hh];
          if (s == 0 && hh == 0) mf0(acc[n][it], oa, ob); else mfa(acc[n][it], oa, ob);
          if (PASS == 0 && n < 4) { const unsigned w = __builtin_bit_cast(u32x4, xf[s][H][n >> 1])[(n & 1) * 2 + it]; float t;
            if (hh == 0) asm volatile("v_lshlrev_b32 %0, 16, %3\n\tv_add_f32 %1, %1, %0\n\tv_fmac_f32 %2, %0, %0" : "=&v"(t), "+v"(xs1), "+v"(xs2) : "v"(w));
            else asm volatile("v_and_b32 %0, 0xffff0000, %3\n\tv_add_f32 %1, %1, %0\n\tv_fmac_f32 %2, %0, %0" : "=&v"(t), "+v"(xs1), "+v"(xs2) : "v"(w)); }
        }
      if (s == 0) { const u32x4 w = __builtin_bit_cast(u32x4, bq[u % 3][0]);
        *(LAS float*)(csw + n * 256) = hsum32(dot2(w.w, ONES, dot2(w.z, ONES, dot2(w.y, ONES, dot2(w.x, ONES, 0.f))))); }
      SB();
    });
#pragma unroll
    for (int k = 2; k < NFP; ++k) ring_pos(ST0 + 11, k, NFP);
    accfence(acc);
    relane();
  };
  auto csum1 = [&](int col) { return *(LAS float*)(lds + L_CS + col * 4); };
  auto csum4 = [&](int col) { return *(LAS f32x4*)(lds + L_CS + col * 4); };
  auto rowstat = [&](f32x2 (&nm)[4], f32x2 (&rs)[4]) {
#pragma unroll
    for (int it = 0; it < 2; ++it) { const f32x4 a = *(LAS f32x4*)(lds + L_XSTAT + (32 * wm + 16 * it + 4 * g) * 4), b = *(LAS f32x4*)(lds + L_XSTAT + 512 + (32 * wm + 16 * it + 4 * g) * 4);
      nm[2 * it] = lo2(a); nm[2 * it + 1] = hi2(a); rs[2 * it] = lo2(b); rs[2 * it + 1] = hi2(b); } };
  auto affN = [&](int n, float c, const f32x2 (&nm)[4], const f32x2 (&rs)[4], f32x2 (&v)[4]) { const f32x2 c2 = {c, c};
    v[0] = fma2(nm[0], c2, lo2(acc[n][0])) * rs[0]; v[1] = fma2(nm[1], c2, hi2(acc[n][0])) * rs[1]; v[2] = fma2(nm[2], c2, lo2(acc[n][1])) * rs[2]; v[3] = fma2(nm[3], c2, hi2(acc[n][1])) * rs[3]; };

  waitv<4>(); bar();
  STAMP(1);
  pass(IC<0>{});
  STAMP(2);
  if constexpr (H == 0) {
    dma4(mkrs(P.wsp, S * S * 2), lds + L_DUMMY, lane * 128, (blockIdx.x * 4 + wm) * 8192);
    if (wm == 0) dma4(mkrs(P.bsp, S * 2), lds + L_BIAS, lane * 4, pi * 256);
  }
  { const float s1 = gsum(xs1), s2 = gsum(xs2), mean = s1 * (1.0f / D), var = fmaxf(s2 * (1.0f / D) - mean * mean, 0.f);
    if (g < 2) *(LAS float*)(lds + L_XSTAT + g * 512 + (32 * wm + 16 * H + fr) * 4) = g ? __builtin_amdgcn_rsqf(var + 1e-5f) : -mean; }
  waitl0(); bar();
  {
    f32x2 nm[4], rs[4]; rowstat(nm, rs);
    LAS char* const sw = lds + L_STASH + (wm * 9 + NB) * 1024 + fr * 64 + (((g >> 1) ^ ((fr >> 2) & 1)) * 32) + (g & 1) * 16;
    LAS char* const sr = lds + L_STASH + (wm * 9 + NB) * 1024 + (4 * g + (fr >> 2)) * 64 + (g & 1) * 32 + (fr & 3) * 8;
    LAS char* const sr1 = lds + L_STASH + (wm * 9 + NB) * 1024 + (4 * g + (fr >> 2)) * 64 + ((g & 1) ^ 1) * 32 + (fr & 3) * 8;
    float cn[NF];
#pragma unroll
    for (int n = 0; n < NF; ++n) cn[n] = csum1((NB + n) * 16 + fr);
#pragma unroll
    for (int n = 0; n < NF; ++n) { f32x2 v[4]; affN(n, cn[n], nm, rs, v);
      *(LAS u32x4*)(sw + n * 1024) = (u32x4){pk2v(gelu2(v[0])), pk2v(gelu2(v[1])), pk2v(gelu2(v[2])), pk2v(gelu2(v[3]))}; }
    asm volatile("" ::: "memory");
    float s1[2] = {0.f, 0.f}, s2[2] = {0.f, 0.f};
#pragma unroll
    for (int n = 0; n < NF; ++n)
#pragma unroll
      for (int ih = 0; ih < 2; ++ih) { const u32x2 t = __builtin_bit_cast(u32x2, __builtin_amdgcn_ds_read_tr16_b64_v4i16((LAS s16x4*)((ih ? sr1 : sr) + n * 1024)));
        s1[ih] = dot2(t.x, ONES, s1[ih]); s2[ih] = dot2(t.x, t.x, s2[ih]); s1[ih] = dot2(t.y, ONES, s1[ih]); s2[ih] = dot2(t.y, t.y, s2[ih]); }
    const float a0 = gsum(s1[0]), b0 = gsum(s2[0]), a1 = gsum(s1[1]), b1 = gsum(s2[1]);
    if (g < 2) *(LAS f32x2*)(lds + L_ZPART + (H * 128 + 32 * wm + 16 * g + fr) * 8) = g ? (f32x2){a1, b1} : (f32x2){a0, b0};
    waitl0();
  }
  STAMP(3);
  pass(IC<1>{});
  STAMP(4);
  u32x2 pq[3][2];
  if constexpr (H == 0) {
    f32x2 nm[4], rs[4]; rowstat(nm, rs);
    float cn[5];
#pragma unroll
    for (int n = 0; n < 5; ++n) cn[n] = csum1(n * 16 + fr);
    bf16x8 pkf[3], vf[3];
#pragma unroll
    for (int n = 0; n < 5; ++n) { f32x2 v[4]; affN(n, cn[n], nm, rs, v);
      if (n >= 2) { u32x4 w = {pk2v(v[0]), pk2v(v[1]), pk2v(v[2]), pk2v(v[3])};
        if (n == 4 && fr == 0) w = (u32x4){ONES, ONES, ONES, ONES};
        vf[n - 2] = __builtin_bit_cast(bf16x8, w); }
      if (n < 2 || n == 4) pkf[n == 4 ? 2 : n] = __builtin_bit_cast(bf16x8, (u32x4){pk2v(phi2(v[0])), pk2v(phi2(v[1])), pk2v(phi2(v[2])), pk2v(phi2(v[3]))});
    }
#pragma unroll
    for (int td = 0; td < 3; ++td)
#pragma unroll
      for (int te = 0; te < 3; ++te) { if (td == 2 && te == 0) continue;
        *(LAS f32x4*)(lds + L_KVS + (wm * 9 + td * 3 + te) * 1024 + lane * 16) = mfma(pkf[td], vf[te], (f32x4){0, 0, 0, 0}); }
  } else {
    {
      const f32x4 m0 = *(LAS f32x4*)(lds + L_ZSTAT + (32 * wm + 8 * g) * 4), m1 = *(LAS f32x4*)(lds + L_ZSTAT + (32 * wm + 8 * g) * 4 + 16);
      const f32x4 r0 = *(LAS f32x4*)(lds + L_ZSTAT + 512 + (32 * wm + 8 * g) * 4), r1 = *(LAS f32x4*)(lds + L_ZSTAT + 512 + (32 * wm + 8 * g) * 4 + 16);
      LAS char* const sw = lds + L_STASH + wm * 9216 + fr * 64 + (((g >> 1) ^ ((fr >> 2) & 1)) * 32) + (g & 1) * 16;
      u32x4 z[9];
#pragma unroll
      for (int n = 0; n < 9; ++n) z[n] = *(LAS u32x4*)(sw + n * 1024);
#pragma unroll
      for (int n = 0; n < 9; ++n) {
        const u32x4 o = {pk2v(fma2(unpk(z[n].x), lo2(r0), lo2(m0))), pk2v(fma2(unpk(z[n].y), hi2(r0), hi2(m0))), pk2v(fma2(unpk(z[n].z), lo2(r1), lo2(m1))), pk2v(fma2(unpk(z[n].w), hi2(r1), hi2(m1)))};
        st16<SC1>(o, rsWs, lane * 16, W_ZF + (pj * 32 + 2 * pi + (wm >> 1)) * C_SZ + ((wm & 1) * 9 + n) * 1024); }
    }
    STAMP(5);
#pragma unroll
    for (int it = 0; it < 2; ++it) { const float nmean = *(LAS float*)(lds + L_XSTAT + (32 * wm + 16 * it + fr) * 4), rstd = *(LAS float*)(lds + L_XSTAT + 512 + (32 * wm + 16 * it + fr) * 4);
#pragma unroll
      for (int n = 0; n < 3; ++n) { const f32x4 v = __builtin_elementwise_fma(csum4((NB + n) * 16 + 4 * g), (f32x4){nmean, nmean, nmean, nmean}, acc[n][it]) * rstd;
        pq[n][it] = (u32x2){pk2v(phi2(lo2(v))), pk2v(phi2(hi2(v)))}; asm volatile("" : "+v"(pq[n][it])); } }
  }
  STAMP(6);
  pass(IC<2>{});
  STAMP(7);
  u32x2 z1[NF][2];
  f32x4 ag[NF][2];
  {
    constexpr int NR = NF + 2;
    LAS char* const aZ = lds + NB * 1024 + lane * 16;
    mk_aR(0);
    if constexpr (H == 0) vB[0] = lane * 16;
    f32x4 kvs;
    if constexpr (H == 0) { f32x4 a = {0, 0, 0, 0}, b = {0, 0, 0, 0};
#pragma unroll
      for (int k = 0; k < 8; ++k) { a += __builtin_bit_cast(f32x4, gl[k]); b += __builtin_bit_cast(f32x4, gl[8 + k]); }
      kvs = a + b; asm volatile("" : "+v"(kvs)); }
    bf16x8 fa[2][NR], fb[2][NR];
    auto rd1 = [&](bf16x8 (&f)[2][NR], int i, int slot) {
      const int h = i & 1; int ii = i >> 1; if (ii == 1) ii = 2; else if (ii == 2) ii = 1;
      if (ii < 2) f[h][ii] = *(LAS bf16x8*)((h ? aR1 : aR0) + slot * R_SZ + ii * 2048); else f[h][ii] = *(LAS bf16x8*)(aZ + slot * C_SZ + (h * 9 + ii - 2) * 1024);
    };
    float zn[2], zr[2], za, zb, ta, tb; f32x2 zc;
    LAS char* const aCs = lds + L_CS + (NB * 16 + 4 * g) * 4;
#pragma unroll
    for (int it = 0; it < 2; ++it) { zn[it] = *(LAS float*)(lds + L_XSTAT + (32 * wm + 16 * it + fr) * 4); zr[it] = *(LAS float*)(lds + L_XSTAT + 512 + (32 * wm + 16 * it + fr) * 4); }
    float GC1 = 2.3022082f; asm volatile("" : "+v"(GC1));
    auto zop = [&](int e, int k) { const int n = e >> 2, it = (e >> 1) & 1, h = e & 1;
      if (k == 0) zc = *(LAS f32x2*)(aCs + n * 64 + h * 8);
      if (k == 3) asm volatile("v_fma_f32 %0, %2, %4, %5\n\tv_fma_f32 %1, %3, %4, %6" : "=&v"(za), "=&v"(zb) : "v"(zc.x), "v"(zc.y), "v"(zn[it]), "v"(acc[n][it][2 * h]), "v"(acc[n][it][2 * h + 1]));
      if (k == 4) asm volatile("v_mul_f32 %0, %0, %2\n\tv_mul_f32 %1, %1, %2" : "+v"(za), "+v"(zb) : "v"(zr[it]));
      if (k == 5) asm volatile("v_mul_f32 %0, %2, %2\n\tv_mul_f32 %1, %3, %3" : "=&v"(ta), "=&v"(tb) : "v"(za), "v"(zb));
      if (k == 6) asm volatile("v_fmamk_f32 %0, %0, 0x3dd2d3e8, %2\n\tv_fmamk_f32 %1, %1, 0x3dd2d3e8, %2" : "+v"(ta), "+v"(tb) : "v"(GC1));
      if (k == 7) asm volatile("v_mul_f32 %0, %0, %2\n\tv_mul_f32 %1, %1, %3" : "+v"(ta), "+v"(tb) : "v"(za), "v"(zb));
      if (k == 8) asm volatile("v_exp_f32 %0, %0" : "+v"(ta));
      if (k == 9) asm volatile("v_exp_f32 %0, %0" : "+v"(tb));
      if (k == 10) asm volatile("v_add_f32 %0, 1.0, %0\n\tv_add_f32 %1, 1.0, %1" : "+v"(ta), "+v"(tb));
      if (k == 11) asm volatile("v_rcp_f32 %0, %0" : "+v"(ta));
      if (k == 12) asm volatile("v_rcp_f32 %0, %0" : "+v"(tb));
      if (k == 13) asm volatile("v_fma_f32 %0, -%0, %2, %0\n\tv_fma_f32 %1, -%1, %3, %1" : "+v"(za), "+v"(zb) : "v"(ta), "v"(tb));
      if (k == 14) { unsigned o; asm volatile("v_cvt_pk_bf16_f32 %0, %1, %2" : "=v"(o) : "v"(za), "v"(zb)); z1[n][it][h] = o; } };
    auto kvscatter = [&]() { const int t = wm * 64 + opq(lane0), rd = (t * 205) >> 10, e0 = 4 * (t - 5 * rd), hd = rd >= 18, dc = qo + rd;
      LAS char* const a = lds + L_KVF + (dc >> 5) * 1024 + ((dc >> 2) & 3) * 256 + ((dc >> 4) & 1) * 8 + (dc & 3) * 2;
      if (t < 180) {
#pragma unroll
        for (int k = 0; k < 4; ++k) { const int e = e0 + k; int ep = 18 * hd + e; ep = e == 18 ? 36 + hd : ep; ep = e == 19 ? 40 : ep;
          *(LAS u16*)(a + (ep >> 4) * 2048 + (ep & 15) * 16) = (u16)pk2(kvs[k], 0.f); } } };
    auto ostep = [&]() {
      const int l = opq(lane0), r = l & 15, gg = l >> 4; bf16x8 kf[3][2];
#pragma unroll
      for (int en = 0; en < 3; ++en) { kf[en][0] = *(LAS bf16x8*)(lds + L_KVF + (en * 2) * 1024 + l * 16); kf[en][1] = *(LAS bf16x8*)(lds + L_KVF + (en * 2 + 1) * 1024 + l * 16); }
#pragma unroll
      for (int it = 0; it < 2; ++it) { const bf16x8 b0 = cat(pq[0][it], pq[1][it]), b1 = cat(pq[2][it], (u32x2){0, 0}); f32x4 nm[3];
#pragma unroll
        for (int en = 0; en < 3; ++en) nm[en] = mfma(kf[en][1], b1, mfma(kf[en][0], b0, (f32x4){0, 0, 0, 0}));
        const float rA = __builtin_amdgcn_rcpf(__shfl(nm[2][0], 16 + r) + 1e-6f), rB = __builtin_amdgcn_rcpf(__shfl(nm[2][1], 16 + r) + 1e-6f);
        const float rM = gg == 0 ? rA : rB;
#pragma unroll
        for (int en = 0; en < 3; ++en) { const float r01 = en == 0 ? rA : en == 1 ? rM : rB, r23 = en == 0 ? rA : rB;
          const u32x2 o = {pk2v(lo2(nm[en]) * r01), pk2v(hi2(nm[en]) * r23)};
          const int oc4 = 9 * gu + 4 * en + gg;
          int vo = (oc4 >> 3) * 8192 + ((oc4 >> 2) & 1) * 512 + ((oc4 & 3) * 16 + r) * 8;
          if (en == 2) vo = gg != 0 ? OOB : vo;
          __builtin_amdgcn_raw_buffer_store_b64(o, rsWs, vo, yb + (2 * wm + it) * 1024, SC1); } } };
    auto step = [&](bf16x8 (&cur)[2][NR], bf16x8 (&nxt)[2][NR], auto Pc) {
      constexpr int p = decltype(Pc)::value, st = 30 + p, e = p - (H ? 3 : 1);
      if (H == 1 && p == 3) waitv<10>(); else waitv<4>();
      if (p == 31) waitl0();
      bar();
      SB();
      if constexpr (H == 0) {
        if (p == 0) kvscatter();
        if (p == 5 && wm == 0 && lane0 == 0) set_flag(P.ws, W_FLAGO + (pi * 4 + gc) * 128 + gu * 4, tag);
        if (p == 28 && wm == 0) { const int l = opq(lane0); dma4(rsWs, lds + L_FLG, l < 4 ? W_FLAGO + (pi * 4 + gc) * 128 + l * 4 : W_FLAGB + (4 * (l & 3) + gu) * 128 + pi * 4, 0); }
        if (p == 29) check_flags(8, lane0 < 4 ? W_FLAGO + (pi * 4 + gc) * 128 + lane0 * 4 : W_FLAGB + (4 * (lane0 & 3) + gu) * 128 + pi * 4);
      } else { if (p == 2) ostep(); if (p == 24) cvoff(vA, 2); }
      ring(st, 4, p + 3);
      SB();
      int ri = 0;
#pragma unroll
      for (int i = 0; i < 2 * NF; ++i) {
        if (e >= 0 && e < 4 * NF && i == 0) zop(e, 0);
        if (p != 31) { rd1(nxt, ri++, p & 3); if (ri < 2 * NR - (2 * NF - 1 - i)) rd1(nxt, ri++, p & 3); }
        const int n = i >> 1, m = i & 1;
        if (p == 0) mf0(ag[n][m], cur[0][2 + n], cur[0][m]); else mfa(ag[n][m], cur[0][2 + n], cur[0][m]);
        if (e >= 0 && e < 4 * NF && i > 0) zop(e, 2 * i);
        mfa(ag[n][m], cur[1][2 + n], cur[1][m]);
        if (e >= 0 && e < 4 * NF) zop(e, 2 * i + 1);
        if ((i & 1) && i < 8) ring(st, i >> 1, p + 3);
        SB();
      }
    };
#pragma unroll
    for (int i = 0; i < 2 * NR; ++i) rd1(fa, i, 3);
    STAMP(9);
    sfor<0, 16>([&](auto Kc) __attribute__((always_inline)) { constexpr int k = decltype(Kc)::value; step(fa, fb, IC<2 * k>{}); step(fb, fa, IC<2 * k + 1>{}); if (k == 5) STAMP(8); });
    accfence(ag);
  }
  STAMP(10);
  relane();
  {
    u32x2 y[NF][2];
#pragma unroll
    for (int it = 0; it < 2; ++it) { const float b = blo(*(LAS u16*)(lds + L_BIAS + (32 * wm + 8 * (fr >> 2) + 4 * it + (fr & 3)) * 2));
#pragma unroll
      for (int n = 0; n < NF; ++n) y[n][it] = (u32x2){pk2v(unpk(z1[n][it].x) * (lo2(ag[n][it]) + b)), pk2v(unpk(z1[n][it].y) * (hi2(ag[n][it]) + b))}; }
    waitv<0>();
#pragma unroll
    for (int hh = 0; hh < 2; ++hh)
#pragma unroll
      for (int it = 0; it < 2; ++it) { const u32x4 v = __builtin_bit_cast(u32x4, cat(y[2 * hh][it], y[2 * hh + 1][it]));
        st16<SC1>(v, rsWs, lane * 16, yb + (4 + 2 * gu + H) * 16384 + hh * 8192 + (2 * wm + it) * 1024);
        *(LAS u32x4*)(lds + (H ? L_YOWN : R_OFF + 2 * R_SZ) + hh * 8192 + (2 * wm + it) * 1024 + lane * 16) = v; }
    if constexpr (H == 0) {
#pragma unroll
      for (int it = 0; it < 2; ++it) __builtin_amdgcn_raw_buffer_store_b64(y[4][it], rsWs, lane * 8, yb + 3 * 16384 + (gu >> 1) * 8192 + (2 * wm + it) * 1024 + (gu & 1) * 512, SC1);
    }
  }
  STAMP(11);
  {
    LAS char* const aY = lds + R_OFF + 2 * wm * 1024;
    bf16x8 rf[2][2][2], bq[3][2];
    mk_aC(0);
    auto rdB = [&](int u) { const int q = u / NF, n = u % NF, o = ((59 + q) & 3) * C_SZ + n * 32; bq[u % 3][0] = trd(aC0 + o); if (q != 2) bq[u % 3][1] = trd(aC1 + o); };
    auto rdY = [&](int q) {
#pragma unroll
      for (int hh = 0; hh < 2; ++hh)
#pragma unroll
        for (int it = 0; it < 2; ++it) { LAS char* a = aY + (q == 4 ? L_YOWN - R_OFF : ((59 + q) & 3) * R_SZ) + hh * 8192 + it * 1024;
          if (q == 2) { if (hh == 0) rf[q & 1][it][0] = cat(*(LAS u32x2*)(a + lane * 8), (u32x2){0, 0}); }
          else if (q < 2 || q == 11) rf[q & 1][it][hh] = cat(*(LAS u32x2*)(a + lane * 8), *(LAS u32x2*)(a + 512 + lane * 8));
          else rf[q & 1][it][hh] = *(LAS bf16x8*)(a + lane * 16); } };
    rdY(0); rdB(0); rdB(1);
    SB();
    sfor<0, 12 * NF>([&](auto Uc) __attribute__((always_inline)) {
      constexpr int u = decltype(Uc)::value, q = u / NF, n = u % NF;
      if (n == NF - 2 && q < 11) {
        if constexpr (H == 1) { if (q == 10) waitv<0>(); else if (q >= 1) waitv<4>(); }
        else { if (q == 1 || q == 3 || q == 10) waitv<0>(); else if (q >= 4) waitv<4>(); }
        bar();
        if constexpr (H == 0) {
          if (q == 1 && wm == 0 && lane0 == 0) set_flag(P.ws, W_FLAGD + (pi * 4 + gc) * 128 + gu * 4, tag);
          if (q == 2 && wm == 0) dma4(rsWs, lds + L_FLG, vB[0] >> 2, W_FLAGD + (pi * 4 + gc) * 128);
          if (q == 3) { STAMP(12); check_flags(4, W_FLAGD + (pi * 4 + gc) * 128 + (lane0 & 3) * 4); STAMP(13); ring_all(64); }
          if (q == 4) STAMP(14);
        }
        SB();
      }
      if (u + 2 < 12 * NF) rdB(u + 2);
      if (n == NF - 2 && q < 11) rdY(q + 1);
      { const int d = u - (NF - 2); if (d >= 0) { const int qq = d / NF + 3; if (qq < 12 && (H == 1 || qq >= 6)) ring_pos(59 + qq, d % NF, NF); } }
#pragma unroll
      for (int it = 0; it < 2; ++it)
#pragma unroll
        for (int hh = 0; hh < 2; ++hh) { if (q == 0 && hh == 0) mf0(acc[n][it], bq[u % 3][hh], rf[q & 1][it][hh]); else if (q != 2 || hh == 0) mfa(acc[n][it], bq[u % 3][hh], rf[q & 1][it][hh]); }
      SB();
    });
    accfence(acc);
  }
  STAMP(15);
  relane();
  {
    const rsrc_t rsOut = mkrs(P.out + (size_t)pi * 128 * D, 128 * D * 2);
    auto packp = [&](const f32x4 (&a)[2]) { u32x4 v = {pk2v(lo2(a[0])), pk2v(hi2(a[0])), pk2v(lo2(a[1])), pk2v(hi2(a[1]))};
      v.x = min(v.x, 0xfffffffeu); v.y = min(v.y, 0xfffffffeu); v.z = min(v.z, 0xfffffffeu); v.w = min(v.w, 0xfffffffeu); return v; };
#pragma unroll
    for (int n = 0; n < NF; ++n) { const int dg = n < 4 ? n : wm, slot = n < 4 ? H * 4 + wm : 8;
      st16<SC1>(packp(acc[n]), rsWs, dg == gc ? OOB : lane * 16, W_PART + ((pi * 16 + 4 * dg + gu) * 4 + gc) * PART_SRC + slot * 1024); }
    constexpr int NK = H ? 1 : 2;
    f32x4 kp[NK][2]; int cb[NK];
#pragma unroll
    for (int it = 0; it < 2; ++it) { kp[0][it] = gc == 0 ? acc[0][it] : gc == 1 ? acc[1][it] : gc == 2 ? acc[2][it] : acc[3][it]; if constexpr (H == 0) kp[1][it] = acc[4][it]; }
    cb[0] = 256 * gu + 32 * (H * 4 + gc); if constexpr (H == 0) cb[1] = 1024 + 32 * gu;
    const int off1 = wm == gc ? 0 : OOB;
    int ro[2]; u32x2 xr[NK][2];
#pragma unroll
    for (int it = 0; it < 2; ++it) { ro[it] = (32 * wm + 8 * (fr >> 2) + 4 * it + (fr & 3)) * 1152 + 8 * g;
#pragma unroll
      for (int k = 0; k < NK; ++k) xr[k][it] = __builtin_amdgcn_raw_buffer_load_b64(rsX, ro[it] + (k ? off1 : 0), cb[k], 0); }
    const int pb = W_PART + (pi * 16 + pj) * 4 * PART_SRC;
    u32x4 v[NK][3];
    __builtin_amdgcn_s_sleep(FIN_SLEEP);
    _Pragma("unroll 1") for (unsigned sp = 0;; ++sp) {
      unsigned mx = 0;
#pragma unroll
      for (int k = 0; k < NK; ++k)
#pragma unroll
        for (int j = 0; j < 3; ++j) { const int so = pb + ((gc + 1 + j) & 3) * PART_SRC + (k ? 8 : H * 4 + wm) * 1024;
          v[k][j] = __builtin_amdgcn_raw_buffer_load_b128(rsWs, lane * 16 + (k ? off1 : 0), so, SC1);
          mx = max(max(mx, max(v[k][j].x, v[k][j].y)), max(v[k][j].z, v[k][j].w)); }
      if (__all(mx != ~0u)) break;
      if (sp > SPIN_MAX) { set_flag(P.ws, W_TMO, 1u); break; }
      __builtin_amdgcn_s_sleep(FIN_POLL);
    }
#pragma unroll
    for (int k = 0; k < NK; ++k)
#pragma unroll
      for (int it = 0; it < 2; ++it) { f32x2 a = lo2(kp[k][it]) + unpk(xr[k][it].x), b = hi2(kp[k][it]) + unpk(xr[k][it].y);
#pragma unroll
        for (int j = 0; j < 3; ++j) { a += unpk(v[k][j][2 * it]); b += unpk(v[k][j][2 * it + 1]); }
        __builtin_amdgcn_raw_buffer_store_b64((u32x2){pk2v(a), pk2v(b)}, rsOut, ro[it] + (k ? off1 : 0), cb[k], 0); }
  }
}

__global__ __launch_bounds__(512, 2) void fused(const u16* x, const u16* wqkv, const u16* wo, const u16* win, const u16* wsp, const u16* bsp, const u16* wout, u16* out, char* ws) {
  extern __shared__ __attribute__((aligned(16))) char smem[];
  LAS char* lds = (LAS char*)smem;
  const int lane = threadIdx.x & 63, wid = __builtin_amdgcn_readfirstlane(threadIdx.x >> 6);
  const int b = blockIdx.x, xc = b & 7, q = b >> 3;
  const int pi = 8 * (xc & 1) + (q & 7), pj = 4 * (xc >> 1) + (q >> 3);
  const unsigned epoch = *(const __attribute__((address_space(4))) unsigned*)(ws + W_EPOCH);
  const Ptrs P{x, wqkv, wo, win, wsp, bsp, wout, out, ws};
  if (wid < 4) body<0>(P, lds, wid, lane, pi, pj, epoch); else body<1>(P, lds, wid - 4, lane, pi, pj, epoch);
}
}

extern "C" void kernel_launch(void* const* d_in, const int* in_sizes, int n_in, void* d_out, int out_size, void* d_ws, size_t ws_size, hipStream_t stream) {
  using mk::u16;
  (void)hipFuncSetAttribute((const void*)mk::fused, hipFuncAttributeMaxDynamicSharedMemorySize, mk::LDS_TOTAL);
  mk::fused<<<256, 512, mk::LDS_TOTAL, stream>>>((const u16*)d_in[0], (const u16*)d_in[1], (const u16*)d_in[2], (const u16*)d_in[4], (const u16*)d_in[5], (const u16*)d_in[6],
                                                 (const u16*)d_in[7], (u16*)d_out, (char*)d_ws);
}
```

```cpp
#include <hip/hip_runtime.h>
#include <hip/hip_bf16.h>
#include <type_traits>

namespace mk {
typedef unsigned short u16;
typedef short bf16x8 __attribute__((ext_vector_type(8)));
typedef short s16x4 __attribute__((ext_vector_type(4)));
typedef float f32x4 __attribute__((ext_vector_type(4)));
typedef float f32x2 __attribute__((ext_vector_type(2)));
typedef unsigned u32x4 __attribute__((ext_vector_type(4)));
typedef unsigned u32x2 __attribute__((ext_vector_type(2)));
typedef __bf16 bf16x2_t __attribute__((ext_vector_type(2)));
#define LAS __attribute__((address_space(3)))
#define GAS __attribute__((address_space(1)))
#define DI __device__ __forceinline__
template <int V> using IC = std::integral_constant<int, V>;
template <int B, int E, class F> __device__ __forceinline__ void sfor(F&& f) { if constexpr (B < E) { f(IC<B>{}); sfor<B + 1, E>(f); } }

constexpr int S = 2048, D = 576, FH = 2304;
constexpr int C_SZ = 18432, R_OFF = 73728, R_SZ = 16384, MISC = 139264;
constexpr int L_STASH = R_OFF, L_KVS = R_OFF + 36864;
constexpr int L_XSTAT = MISC + 4096, L_CS = L_XSTAT + 1024, L_ZPART = L_CS + 1792, L_ZSTAT = L_ZPART + 2048, L_KVF = L_ZSTAT + 1024, L_TAB = L_KVF + 6144;
constexpr int L_BIAS = L_TAB + 3072, L_FLG = L_BIAS + 256, L_DUMMY = L_FLG + 1024, L_STAMP = L_DUMMY + 1024;
constexpr int L_YOWN = MISC;
static_assert(L_YOWN + 16384 <= L_BIAS, "lds");
constexpr int LDS_TOTAL = 163840;
static_assert(L_STAMP + 512 <= LDS_TOTAL, "lds");
constexpr int W_EPOCH = 0, W_TMO = 256, W_STAMP = 32768, W_COL = 2 << 20, W_ROW = 18 << 20, W_END = 50 << 20;
constexpr int COL_SZ = 1 << 20, C_ZF = 0, C_KVP = 0x90000, C_CSG = 0xA0000, C_FLAGB = 0xB0000;
constexpr int ROW_SZ = 2 << 20, C_YF = 0, C_PART = 0xC0000, C_STATS = 0x150000, C_FLAGO = 0x158000, C_FLAGD = 0x158100;
constexpr int KVP_STRIDE = 2944, YF_GRP = 12 * 16384, PART_SRC = 9216, OOB = 0x40000000;
constexpr int SC1 = 16;
constexpr unsigned SPIN_MAX = 1u << 16;
#define FIN_SLEEP 16
#define FIN_POLL 4
#define STAT_SLOT 13
constexpr int vpre1(int sg) { return sg == 10 ? 1 : 0; }
constexpr int vpost1(int sg) { return sg == 24 ? 2 : sg == 1 || sg == 10 || (sg >= 20 && sg <= 26) ? 1 : sg == STAT_SLOT ? 8 : sg == STAT_SLOT + 3 ? 3 : sg == 18 ? 6 : sg == 19 ? 2 : sg == 27 + 14 ? 6 : 0; }
constexpr int allow1(int sg) { return 5 + (sg == 25 ? 0 : vpost1(sg - 1)) + vpre1(sg) + vpost1(sg); }

template <int N> DI void waitv() { asm volatile("s_waitcnt vmcnt(%0)" ::"n"(N) : "memory"); }
DI void waitl0() { __builtin_amdgcn_s_waitcnt(0xC07F); asm volatile("" ::: "memory"); }
DI void bar() { asm volatile("" ::: "memory"); __builtin_amdgcn_s_barrier(); asm volatile("" ::: "memory"); }
#define SB() __builtin_amdgcn_sched_barrier(0)
DI f32x4 mfma(bf16x8 a, bf16x8 b, f32x4 c) { return __builtin_amdgcn_mfma_f32_16x16x32_bf16(a, b, c, 0, 0, 0); }
DI void mfa(f32x4& c, const bf16x8& a, const bf16x8& b) { asm volatile("v_mfma_f32_16x16x32_bf16 %0, %1, %2, %0" : "+v"(c) : "v"(a), "v"(b)); }
DI void mf0(f32x4& c, const bf16x8& a, const bf16x8& b) { asm volatile("v_mfma_f32_16x16x32_bf16 %0, %1, %2, 0" : "=&v"(c) : "v"(a), "v"(b)); }
DI int opq(int v) { asm volatile("" : "+v"(v)); return v; }
template <int N> DI void accfence(f32x4 (&a)[N][2]) {
  if constexpr (N == 5) asm volatile("s_nop 7\n\ts_nop 1" : "+v"(a[0][0]), "+v"(a[0][1]), "+v"(a[1][0]), "+v"(a[1][1]), "+v"(a[2][0]), "+v"(a[2][1]), "+v"(a[3][0]), "+v"(a[3][1]), "+v"(a[4][0]), "+v"(a[4][1]));
  else asm volatile("s_nop 7\n\ts_nop 1" : "+v"(a[0][0]), "+v"(a[0][1]), "+v"(a[1][0]), "+v"(a[1][1]), "+v"(a[2][0]), "+v"(a[2][1]), "+v"(a[3][0]), "+v"(a[3][1]));
}
DI unsigned pk2(float lo, float hi) { f32x2 v = {lo, hi}; return __builtin_bit_cast(unsigned, __builtin_convertvector(v, bf16x2_t)); }
DI float blo(unsigned u) { return __uint_as_float(u << 16); }
DI float dot2(unsigned a, unsigned b, float c) { return __builtin_amdgcn_fdot2_f32_bf16(__builtin_bit_cast(bf16x2_t, a), __builtin_bit_cast(bf16x2_t, b), c, false); }
DI f32x2 gelu2(f32x2 v) {
  const f32x2 a = v * __builtin_elementwise_fma(v * v, (f32x2){0.10294324f, 0.10294324f}, (f32x2){2.3022082f, 2.3022082f});
  const f32x2 d = (f32x2){__builtin_amdgcn_exp2f(a.x), __builtin_amdgcn_exp2f(a.y)} + 1.0f;
  const f32x2 r = {__builtin_amdgcn_rcpf(d.x), __builtin_amdgcn_rcpf(d.y)};
  return __builtin_elementwise_fma(-v, r, v);
}
DI unsigned pk2v(f32x2 v) { return __builtin_bit_cast(unsigned, __builtin_convertvector(v, bf16x2_t)); }
DI f32x2 unpk(unsigned u) { return (f32x2){__uint_as_float(u << 16), __uint_as_float(u & 0xffff0000u)}; }
DI f32x2 lo2(f32x4 a) { return (f32x2){a.x, a.y}; }
DI f32x2 hi2(f32x4 a) { return (f32x2){a.z, a.w}; }
DI f32x2 fma2(f32x2 a, f32x2 b, f32x2 c) { return __builtin_elementwise_fma(a, b, c); }
DI f32x2 phi2(f32x2 t) {
  const f32x2 a = t * 1.44269504f, p = t + 1.0f;
  const f32x2 r = {t.x > 0.f ? p.x : __builtin_amdgcn_exp2f(a.x), t.y > 0.f ? p.y : __builtin_amdgcn_exp2f(a.y)};
  return r + 1e-6f;
}
DI float hsum32(float v) { const unsigned u = __float_as_uint(v); const auto r = __builtin_amdgcn_permlane32_swap(u, u, false, false); return __uint_as_float(r[0]) + __uint_as_float(r[1]); }
DI float hsum16(float v) { const unsigned u = __float_as_uint(v); const auto r = __builtin_amdgcn_permlane16_swap(u, u, false, false); return __uint_as_float(r[0]) + __uint_as_float(r[1]); }
DI float gsum(float v) { return hsum32(hsum16(v)); }
typedef __amdgpu_buffer_rsrc_t rsrc_t;
DI rsrc_t mkrs(const void* p, int bytes) { return __builtin_amdgcn_make_buffer_rsrc((void*)p, 0, bytes, 0x00020000); }
template <int AUX> DI void dma16(rsrc_t rs, LAS char* dst, int voff, int soff) {
  const unsigned m = (unsigned)(size_t)dst;
  if constexpr (AUX == 0) asm volatile("s_mov_b32 m0, %0\n\ts_nop 0\n\tbuffer_load_dwordx4 %1, %2, %3 offen lds" ::"s"(m), "v"(voff), "s"(rs), "s"(soff) : "memory");
  else asm volatile("s_mov_b32 m0, %0\n\ts_nop 0\n\tbuffer_load_dwordx4 %1, %2, %3 offen sc1 lds" ::"s"(m), "v"(voff), "s"(rs), "s"(soff) : "memory");
}
DI void dma4(rsrc_t rs, LAS char* dst, int voff, int soff) {
  const unsigned m = (unsigned)(size_t)dst;
  asm volatile("s_mov_b32 m0, %0\n\ts_nop 3\n\tbuffer_load_dword %1, %2, %3 offen sc1 lds" ::"s"(m), "v"(voff), "s"(rs), "s"(soff) : "memory");
}
template <int AUX> DI void st16(u32x4 v, rsrc_t rs, int voff, int soff) { asm volatile("" : "+v"(v)); __builtin_amdgcn_raw_buffer_store_b128(v, rs, voff, soff, AUX); asm volatile("s_nop 1" ::"v"(v) : "memory"); }
#define LIKELY(x) __builtin_expect(!!(x), 1)
#define UNLIKELY(x) __builtin_expect(!!(x), 0)
#define HLOAD(dst, voff, rs, soff) asm volatile("buffer_load_dwordx4 %0, %1, %2, %3 offen sc1 ; HIDDEN_LOAD" : "=v"(dst) : "v"(voff), "s"(rs), "s"(soff) : "memory")
DI s16x4 tr1(LAS char* a) { return __builtin_amdgcn_ds_read_tr16_b64_v4i16((LAS s16x4*)a); }
DI bf16x8 cat4(s16x4 a, s16x4 b) { return __builtin_shufflevector(a, b, 0, 1, 2, 3, 4, 5, 6, 7); }
DI bf16x8 cat(u32x2 a, u32x2 b) { u32x4 v = {a.x, a.y, b.x, b.y}; return __builtin_bit_cast(bf16x8, v); }

struct Ptrs { const u16 *x, *wqkv, *wo, *win, *wsp, *bsp, *wout; u16* out; char* ws; };

#define STAMP(k)

DI void poll_flags(char* ws, int off, bool active, unsigned tag) {
  GAS unsigned* p = (GAS unsigned*)(ws + off);
  _Pragma("unroll 1") for (unsigned sp = 0;; ++sp) {
    const unsigned v = __hip_atomic_load(p, __ATOMIC_RELAXED, __HIP_MEMORY_SCOPE_AGENT);
    if (__all(!active || v == tag)) break;
    if (sp > SPIN_MAX) { __hip_atomic_store((GAS unsigned*)(ws + W_TMO), 1u, __ATOMIC_RELAXED, __HIP_MEMORY_SCOPE_AGENT); break; }
    __builtin_amdgcn_s_sleep(2);
  }
  asm volatile("" ::: "memory");
}
DI void set_flag(char* ws, int off, unsigned tag) { __hip_atomic_store((GAS unsigned*)(ws + off), tag, __ATOMIC_RELAXED, __HIP_MEMORY_SCOPE_AGENT); }

template <int H>
DI void body(const Ptrs& P, LAS char* lds, const int wm, const int lane0, const int pi, const int pj, unsigned epoch, const rsrc_t rsCode) {
  constexpr int NF = H ? 4 : 5, NB = H ? 5 : 0;
  int lane = lane0, fr = lane & 15, g = lane >> 4;
  auto relane = [&]() { lane = opq(lane0); fr = lane & 15; g = lane >> 4; };
  const int gc = pj >> 2, gu = pj & 3, qo = 4 * (pj & 1);
  const int rot = (pi + 2 * pj) % 9;
  const rsrc_t rsX = mkrs(P.x + (size_t)pi * 128 * D, 128 * D * 2), rsWin = mkrs(P.win, D * 2 * FH * 2), rsQkv = mkrs(P.wqkv, D * 3 * D * 2);
  const rsrc_t rsSp = mkrs(P.wsp + (size_t)pi * 128 * S, 128 * S * 2), rsWs = mkrs(P.ws, W_END);
  const rsrc_t rsWout = mkrs(P.wout, FH * D * 2), rsWo = mkrs(P.wo, D * D * 2);
  const int colb = W_COL + pj * COL_SZ, rowb = W_ROW + pi * ROW_SZ, yb = rowb + C_YF + gc * YF_GRP;
  STAMP(0);

  auto rvoff = [&](int stride) { const int l = opq(lane0); return (8 * (l >> 5) + ((l >> 3) & 3)) * stride + ((l & 7) ^ (l >> 4)) * 16; };
  int vA[5], vB[5];
  auto cvoff = [&](int (&v)[5], int kind) {
    const int l = opq(lane0);
    const int t = kind == 0 ? pj : gu, b0 = 72 * pj - 8 * (pj & 1);
    const int a0 = 256 * t, a1 = (kind == 0 ? 4096 : 1024) + 32 * t - 128, a2 = 256 * t - 32;
#pragma unroll
    for (int idx = 0; idx < 5; ++idx) {
      const unsigned n = 64 * (wm + 4 * idx) + l, Pp = __umul24(n, 3641u) >> 16, cc = n - __umul24(Pp, 18u);
      unsigned k; int add;
      if (kind == 2) k = (Pp & ~24u) | ((Pp & 8u) << 1) | ((Pp & 16u) >> 1); else k = (Pp & ~12u) | ((Pp & 4u) << 1) | ((Pp & 8u) >> 1);
      if (kind != 1) { add = a0; add = cc >= 8 ? a1 : add; add = cc >= 10 ? a2 : add; }
      else { add = 1152; add = cc >= 4 ? 2304 - 64 : add; add = cc >= 8 ? 1152 - 64 : add; add = cc >= 9 ? 2304 - 80 : add; add = cc >= 10 ? -160 : add; add = cc >= 15 ? OOB : add; add += b0; }
      v[idx] = (int)(__umul24(k, kind == 0 ? 9216u : kind == 1 ? 3456u : 1152u) + 16u * cc) + add;
      if (idx == 4) v[idx] = wm >= 2 ? OOB : v[idx];
      asm volatile("" : "+v"(v[idx]));
    }
  };
  auto ksl = [&](int s) { int k = rot + s; return k >= 9 ? k - 9 : k; };
  auto kord = [&](int p) { return p < 2 ? 2 * pi + p : (p - 2 < 2 * pi ? p - 2 : p); };
  auto g3stage = [&](int q) { return q < 3 ? q : q == 11 ? 3 : 4 + ((2 * gu + (q - 3)) & 7); };
  auto ring = [&](int st, int idx, int dyn = 0) {
    const int slot = st & 3;
    if constexpr (H == 1) {
      LAS char* dst = lds + ((idx == 4 && wm >= 2) ? L_DUMMY : slot * C_SZ + (wm + 4 * idx) * 1024);
      if (st < 9) dma16<0>(rsWin, dst, vA[idx], 4608 + ksl(st) * (64 * 9216));
      else if (st < 18) dma16<0>(rsQkv, dst, vB[idx], ksl(st - 9) * (64 * 3456));
      else if (st < 27) dma16<0>(rsWin, dst, vA[idx], ksl(st - 18) * (64 * 9216));
      else if (st < 59) dma16<SC1>(rsWs, dst, vB[idx == 4], colb + C_ZF + kord(dyn) * C_SZ + (wm + 4 * idx) * 1024);
      else { const int q = st - 59;
        if (q < 3) dma16<0>(rsWo, dst, vA[idx], (144 * gc + 64 * q) * 1152);
        else if (q == 11) dma16<0>(rsWout, dst, vA[idx], (2048 + 64 * gc) * 1152);
        else dma16<0>(rsWout, dst, vA[idx], (512 * gc + 64 * (g3stage(q) - 4)) * 1152); }
    } else {
      if (idx == 4) return;
      LAS char* dst = lds + R_OFF + slot * R_SZ + (4 * wm + idx) * 1024;
      const int rb = 32 * wm + 16 * (idx & 1) + 4 * (idx >> 1);
      if (st < 9) dma16<0>(rsX, dst, vA[idx & 1], rb * 1152 + ksl(st) * 128);
      else if (st < 27) return;
      else if (st < 59) dma16<0>(rsSp, dst, vA[idx & 1], rb * 4096 + kord(dyn) * 128);
      else dma16<SC1>(rsWs, dst, vB[0], yb + g3stage(st - 59) * 16384 + (4 * wm + idx) * 1024);
    }
  };
  auto ring_all = [&](int st) { ring(st, 4); ring(st, 0); ring(st, 1); ring(st, 2); ring(st, 3); };
  auto ring_pos = [&](int st, int k, int nf) {
    const int dyn = st - 27;
    if (nf == 5) { ring(st, k == 0 ? 4 : k - 1, dyn); }
    else if (nf == 4) { if (k == 0) ring(st, 4, dyn); ring(st, k, dyn); }
    else { if (k == 0) { ring(st, 4, dyn); ring(st, 0, dyn); } else if (k == 1) { ring(st, 1, dyn); ring(st, 2, dyn); } else ring(st, 3, dyn); }
  };
  if constexpr (H == 1) cvoff(vA, 0); else { vA[0] = rvoff(1152); vA[1] = vA[0] ^ 64; }
  auto codewarm = [&]() { if (wm == 3) dma4(rsCode, lds + L_DUMMY, opq(lane0) * 128, ((blockIdx.x >> 3) & 15) * 8192); };
  if constexpr (H == 0) codewarm();
  ring_all(0); ring_all(1);
  STAMP(17);
  if constexpr (H == 1) cvoff(vB, 1);
  else {
    const int t = wm * 64 + lane;
    *(LAS u32x4*)(lds + L_KVF + t * 16) = (u32x4){0, 0, 0, 0};
    *(LAS u32x2*)(lds + L_KVF + 4096 + t * 8) = (u32x2){0, 0};
    const int rd = (t * 205) >> 10, e0 = 4 * (t - 5 * rd), hd = rd >= 18, dc = qo + rd;
    const int gb = (dc >> 4) * 3072 - (dc >> 5) * 1024 + ((dc >> 2) & 3) * 256 + (dc & 3) * 4, sb = (dc >> 5) * 1024 + ((dc >> 2) & 3) * 256 + ((dc >> 4) & 1) * 8 + (dc & 3) * 2;
    unsigned go[4], so[4];
#pragma unroll
    for (int k = 0; k < 4; ++k) { const int e = e0 + k, ce = qo + 18 * hd + e; int off = (ce >> 4) * 1024 + (ce & 15) * 16; off = ce >= 32 ? off + 128 : off; off = e >= 18 ? 2048 : off;
      int ep = 18 * hd + e; ep = e == 18 ? 36 + hd : ep; ep = e == 19 ? 40 : ep;
      go[k] = gb + off; so[k] = sb + (ep >> 4) * 2048 + (ep & 15) * 16; }
    if (t < 180) *(LAS u32x4*)(lds + L_TAB + t * 16) = (u32x4){go[0] | (go[1] << 16), go[2] | (go[3] << 16), so[0] | (so[1] << 16), so[2] | (so[3] << 16)};
  }
  LAS char *aR0, *aR1, *aC0, *aC1;
  auto mk_aR = [&]() { const int l = opq(lane0), r = l & 15, o = (32 * wm + r) * 128 + (((l >> 4) ^ ((r >> 1) & 7)) * 16); aR0 = lds + R_OFF + o; aR1 = lds + R_OFF + (o ^ 64); };
  auto mk_aC = [&]() { const int l = opq(lane0); aC0 = lds + (16 * (l >> 5) + 4 * ((l >> 4) & 1) + ((l & 15) >> 2)) * 288 + (l & 3) * 8 + NB * 32; aC1 = aC0 + 32 * 288; asm volatile("" : "+v"(aC1)); };
  asm volatile("" : "+s"(epoch));
  const unsigned tag = epoch + 1u;
  unsigned ONES = 0x3f803f80u; asm volatile("" : "+s"(ONES));
  auto raise = [&](int off, bool on = true) { __builtin_amdgcn_raw_buffer_store_b32(tag, rsWs, (opq(lane0) == 0 && wm == 0 && on) ? 0 : OOB, off, SC1); };
  auto flags_dma = [&](int voff, int soff) { dma4(rsWs, lds + L_FLG + wm * 256, voff, soff); };
  auto check_flags = [&](int off) { const unsigned v = *(LAS unsigned*)(lds + L_FLG + wm * 256 + opq(lane0) * 4);
    if (UNLIKELY(!__all(v == tag))) { STAMP(18); poll_flags(P.ws, off, true, tag); } };

  bf16x8 xf[9][2][2];
  f32x4 acc[NF][2];
  f32x4 zm[2], zr2[2];
  auto z2n_tile = [&](int n) { const int l = opq(lane0), r = l & 15, q = l >> 4;
    const u32x4 z = *(LAS u32x4*)(lds + L_STASH + wm * 9216 + n * 1024 + r * 64 + (((q >> 1) ^ ((r >> 2) & 1)) * 32) + (q & 1) * 16);
    const u32x4 o = {pk2v(fma2(unpk(z.x), lo2(zr2[0]), lo2(zm[0]))), pk2v(fma2(unpk(z.y), hi2(zr2[0]), hi2(zm[0]))), pk2v(fma2(unpk(z.z), lo2(zr2[1]), lo2(zm[1]))), pk2v(fma2(unpk(z.w), hi2(zr2[1]), hi2(zm[1])))};
    st16<SC1>(o, rsWs, l * 16, colb + C_ZF + (2 * pi + (wm >> 1)) * C_SZ + ((wm & 1) * 9 + n) * 1024); };

  auto pass = [&](auto PASSc) {
    constexpr int PASS = decltype(PASSc)::value;
    constexpr int NFP = (PASS == 1 && H == 1) ? 3 : NF, ST0 = 9 * PASS, NU = 9 * NFP;
    constexpr bool TR = (PASS == 2) || (PASS == 1 && H == 1);
    constexpr int L_LAND = PASS == 0 ? R_OFF + R_SZ : PASS == 1 ? L_KVS : R_OFF + 2 * R_SZ;
    const int csg = colb + C_CSG + PASS * 12288;
    s16x4 bq[3][4];
    u32x4 gl[8]; u32x2 zp[2]; f32x4 ct, xs1[2], xs2[2]; float csv = 0.f, cs5 = 0.f;
    auto rdB = [&](int u, int j) { const int s = u / NFP, n = u % NFP; if (u < NU) bq[u % 3][j] = tr1(((j & 2) ? aC1 : aC0) + ((ST0 + s) & 3) * C_SZ + n * 32 + (j & 1) * (8 * 288)); };
    auto rdX = [&](int s, int j) { xf[s][j >> 1][j & 1] = *(LAS bf16x8*)(((j & 1) ? aR1 : aR0) + (s & 3) * R_SZ + (j >> 1) * 2048); };
    auto slot = [&](auto SGc) __attribute__((always_inline)) {
      constexpr int sg = decltype(SGc)::value, s = sg - ST0;
      if (s == 1) { const int l = opq(lane0); const bool on = wm == 0 && pi < 9;
        __builtin_amdgcn_raw_buffer_store_b64((u32x2){__float_as_uint(csv), tag}, rsWs, on ? (NB * 16 + l) * 8 : OOB, csg + rot * 1152, SC1);
        if constexpr (H == 0) __builtin_amdgcn_raw_buffer_store_b64((u32x2){__float_as_uint(cs5), tag}, rsWs, (on && l < 16) ? (64 + l) * 8 : OOB, csg + rot * 1152, SC1); }
      if constexpr (H == 1) {
        if (sg >= 20 && sg <= 26) { const int k = (sg - 20) * 4 + wm, src = k / 9;
          st16<SC1>((u32x4){~0u, ~0u, ~0u, ~0u}, rsWs, k < 27 ? opq(lane0) * 16 : OOB, rowb + C_PART + (pj * 4 + ((gc + 1 + src) & 3)) * PART_SRC + (k - 9 * src) * 1024); }
        if (sg == 8) {
          asm volatile("s_nop 7" : "+v"(xs1[0]), "+v"(xs2[0]), "+v"(xs1[1]), "+v"(xs2[1]));
          const int l = opq(lane0), r = l & 15, j = r & 3;
          LAS char* const o = lds + ((r >> 2) == (l >> 4) ? L_XSTAT + (32 * wm + r) * 4 : L_DUMMY);
#pragma unroll
          for (int it = 0; it < 2; ++it) { float s1 = xs1[it].x, s2 = xs2[it].x;
            s1 = j == 1 ? xs1[it].y : s1; s2 = j == 1 ? xs2[it].y : s2; s1 = j == 2 ? xs1[it].z : s1; s2 = j == 2 ? xs2[it].z : s2; s1 = j == 3 ? xs1[it].w : s1; s2 = j == 3 ? xs2[it].w : s2;
            const float mean = s1 * (1.0f / D), var = fmaxf(s2 * (1.0f / D) - mean * mean, 0.f);
            *(LAS float*)(o + it * 64) = -mean; *(LAS float*)(o + it * 64 + 512) = __builtin_amdgcn_rsqf(var + 1e-5f); } }
        if (s == 8) { const int t = min(wm * 64 + opq(lane0), 143); float c = 0.f; unsigned bad = 0;
#pragma unroll
          for (int j = 0; j < 9; ++j) { const u32x2 v = *(LAS u32x2*)(lds + L_LAND + j * 1152 + t * 8); c += __uint_as_float(v.x); bad |= v.y ^ tag; }
          if (UNLIKELY(!__all(bad == 0))) {
            _Pragma("unroll 1") for (unsigned sp = 0; !__all(bad == 0); ++sp) {
              STAMP(20);
              if (sp > SPIN_MAX) { set_flag(P.ws, W_TMO, 1u); break; }
              c = 0.f; bad = 0;
#pragma unroll
              for (int j = 0; j < 9; ++j) { const u32x2 v = __builtin_amdgcn_raw_buffer_load_b64(rsWs, t * 8, csg + j * 1152, SC1); c += __uint_as_float(v.x); bad |= v.y ^ tag; }
            } }
          *(LAS float*)(lds + L_CS + PASS * 576 + t * 4) = c; }
        if (sg == STAT_SLOT) { const int l = opq(lane0), vo = (l >> 5) * 16384 + (32 * wm + (l & 31)) * 16;
#pragma unroll
          for (int j = 0; j < 8; ++j) HLOAD(gl[j], vo, rsWs, rowb + C_STATS + j * 2048); }
        if (sg == STAT_SLOT + 2) {
          asm volatile("s_waitcnt vmcnt(10) ; HIDDEN_WAIT %0 %1 %2 %3 %4 %5 %6 %7" : "+v"(gl[0]), "+v"(gl[1]), "+v"(gl[2]), "+v"(gl[3]), "+v"(gl[4]), "+v"(gl[5]), "+v"(gl[6]), "+v"(gl[7]) :: "memory");
          const int l = opq(lane0); float s1 = 0.f, s2 = 0.f; unsigned bad = 0;
#pragma unroll
          for (int j = 0; j < 8; ++j) { bad |= gl[j].y ^ tag; bad |= gl[j].w ^ tag; s1 += __uint_as_float(gl[j].x); s2 += __uint_as_float(gl[j].z); }
          if (UNLIKELY(!__all(bad == 0))) {
            _Pragma("unroll 1") for (unsigned sp = 0; !__all(bad == 0); ++sp) {
              STAMP(19);
              if (sp > SPIN_MAX) { set_flag(P.ws, W_TMO, 1u); break; }
              s1 = 0.f; s2 = 0.f; bad = 0;
#pragma unroll
              for (int j = 0; j < 8; ++j) { const u32x4 v = __builtin_amdgcn_raw_buffer_load_b128(rsWs, (l >> 5) * 16384 + (32 * wm + (l & 31)) * 16, rowb + C_STATS + j * 2048, SC1);
                bad |= v.y ^ tag; bad |= v.w ^ tag; s1 += __uint_as_float(v.x); s2 += __uint_as_float(v.z); }
            } }
          s1 = hsum32(s1); s2 = hsum32(s2);
          const float mu = s1 * (1.0f / FH), var = fmaxf(s2 * (1.0f / FH) - mu * mu, 0.f), rs = __builtin_amdgcn_rsqf(var + 1e-5f);
          *(LAS float*)(lds + L_ZSTAT + (l >> 5) * 512 + (32 * wm + (l & 31)) * 4) = (l >> 5) ? rs : -mu * rs;
          asm volatile("" ::: "memory");
          LAS char* const a = lds + L_ZSTAT + (32 * wm + 8 * (l >> 4)) * 4;
          zm[0] = *(LAS f32x4*)a; zm[1] = *(LAS f32x4*)(a + 16); zr2[0] = *(LAS f32x4*)(a + 512); zr2[1] = *(LAS f32x4*)(a + 528); }
        if (sg == STAT_SLOT + 3) { z2n_tile(0); z2n_tile(1); z2n_tile(2); }
        if (sg == 19) {
          const int t = wm * 64 + opq(lane0); const u32x2 tb = *(LAS u32x2*)(lds + L_TAB + min(t, 179) * 16); u32x4 o;
#pragma unroll
          for (int k = 0; k < 4; ++k) { LAS char* const a = lds + L_KVS + ((k & 1) ? tb[k >> 1] >> 16 : tb[k >> 1] & 0xffffu);
            o[k] = __float_as_uint((*(LAS float*)a + *(LAS float*)(a + 8192)) + (*(LAS float*)(a + 16384) + *(LAS float*)(a + 24576))); }
          st16<SC1>(o, rsWs, t < 180 ? t * 16 : OOB, colb + C_KVP + pi * KVP_STRIDE); }
        if (sg == 23) { vB[0] = opq(lane0) * 16; vB[1] = wm >= 2 ? OOB : vB[0]; }
        if (sg == 24) flags_dma((opq(lane0) & 15) * 4, colb + C_FLAGB);
        if (sg == 26) check_flags(colb + C_FLAGB + (lane0 & 15) * 4);
      }
    };
    auto front = [&](auto SGc) __attribute__((always_inline)) {
      constexpr int sg = decltype(SGc)::value, s = sg - ST0;
      if constexpr (H == 0) {
        if (s == 5) { const int vo = opq(lane0) * 16;
#pragma unroll
          for (int k = 0; k < 3; ++k) dma16<SC1>(rsWs, lds + L_LAND + (wm + 4 * k) * 1024, vo, csg + (wm + 4 * k) * 1024); }
        if (sg == 21) raise(colb + C_FLAGB + pi * 4);
        if (sg == 19) { const int l = opq(lane0), li = (wm & 1) * 64 + l;
          if (wm < 2) dma4(rsWout, lds + L_DUMMY, li < 81 ? li * 128 : OOB, blockIdx.x * 81 * 128);
          else if (wm == 2) dma4(rsWo, lds + L_DUMMY, l < 21 ? l * 128 : OOB, blockIdx.x * 21 * 128);
          codewarm(); }
        if (sg == 22) { vA[0] = rvoff(4096); vA[1] = vA[0] ^ 64; }
      } else {
        if (sg == 9) { const int r = wm * 64 + opq(lane0); const f32x2 a = __builtin_bit_cast(f32x2, zp[0]) + __builtin_bit_cast(f32x2, zp[1]);
          st16<SC1>((u32x4){__float_as_uint(a.x), tag, __float_as_uint(a.y), tag}, rsWs, r < 128 ? r * 16 : OOB, rowb + C_STATS + pj * 2048); }
      }
    };
#pragma unroll
    for (int n = NFP; n < NF; ++n) { acc[n][0] = (f32x4){0, 0, 0, 0}; acc[n][1] = (f32x4){0, 0, 0, 0}; }
    mk_aC();
    if (PASS == 0) { mk_aR(); rdX(0, 0); rdX(0, 1); rdX(0, 2); rdX(0, 3); }
#pragma unroll
    for (int j = 0; j < 8; ++j) rdB(j >> 2, j & 3);
    if (PASS == 0) ring_all(2);
    bf16x8 ones8 = __builtin_bit_cast(bf16x8, (u32x4){ONES, ONES, ONES, ONES}); asm volatile("" : "+v"(ones8));
    const int gq = opq(lane0) >> 4;
    SB();
    sfor<0, NU>([&](auto Uc) __attribute__((always_inline)) {
      constexpr int u = decltype(Uc)::value, s = u / NFP, n = u % NFP, sg = ST0 + s, d = u - (NFP - 2);
      if (n == NFP - 2) {
        slot(IC<sg>{});
        if constexpr (H == 1) waitv<allow1(sg)>();
        else { if (sg == 1 || sg == 2) waitv<6>(); else if (sg == 6) waitv<7>(); else if (sg < 6 || sg >= 25) waitv<4>(); else if (sg == 7 || sg == 16) waitv<0>(); }
        if ((s == 8 && H == 1) || (sg == 18 && H == 0)) waitl0();
        bar();
        if (H == 1 && sg == 9) { const int r = (wm * 64 + opq(lane0)) & 127; zp[0] = *(LAS u32x2*)(lds + L_ZPART + r * 8); zp[1] = *(LAS u32x2*)(lds + L_ZPART + 1024 + r * 8); }
        SB();
      }
      const bf16x8 b0 = cat4(bq[u % 3][0], bq[u % 3][1]), b1 = cat4(bq[u % 3][2], bq[u % 3][3]);
#pragma unroll
      for (int j = 0; j < 4; ++j) { const int it = j >> 1, hh = j & 1; const bf16x8 &bb = hh ? b1 : b0, &oa = TR ? bb : xf[s][it][hh], &ob = TR ? xf[s][it][hh] : bb;
        if (s == 0 && hh == 0) mf0(acc[n][it], oa, ob); else mfa(acc[n][it], oa, ob);
        SB();
        rdB(u + 2, j);
        if (PASS == 0 && n == NFP - 2 && s < 8) rdX(s + 1, j);
        if (j == 3) {
          if (u >= 1 && u <= NFP) { asm volatile("s_nop 0" : "+v"(ct)); if (u - 1 < 4) csv = gq == u - 1 ? ct.x : csv; else cs5 = ct.x; asm volatile("" : "+v"(csv), "+v"(cs5)); }
          if (n == NFP - 2) front(IC<sg>{});
          if constexpr (d >= 0) ring_pos(ST0 + d / NFP + 3, d % NFP, NFP); }
        SB(); }
      if (s == 0) { mf0(ct, ones8, b0); mfa(ct, ones8, b1); }
      if (PASS == 0 && H == 1 && n < 2) {
#pragma unroll
        for (int it = 0; it < 2; ++it) { f32x4& xs = n ? xs2[it] : xs1[it]; const bf16x8 &o0 = n ? xf[s][it][0] : ones8, &o1 = n ? xf[s][it][1] : ones8;
          if (s == 0) mf0(xs, xf[s][it][0], o0); else mfa(xs, xf[s][it][0], o0); mfa(xs, xf[s][it][1], o1); } }
      SB();
    });
#pragma unroll
    for (int k = 2; k < NFP; ++k) ring_pos(ST0 + 11, k, NFP);
    accfence(acc);
    relane();
  };
  auto csum1 = [&](int ps, int col) { return *(LAS float*)(lds + L_CS + ps * 576 + col * 4); };
  auto rowstat = [&](f32x2 (&nm)[4], f32x2 (&rs)[4]) {
#pragma unroll
    for (int it = 0; it < 2; ++it) { const f32x4 a = *(LAS f32x4*)(lds + L_XSTAT + (32 * wm + 16 * it + 4 * g) * 4), b = *(LAS f32x4*)(lds + L_XSTAT + 512 + (32 * wm + 16 * it + 4 * g) * 4);
      nm[2 * it] = lo2(a); nm[2 * it + 1] = hi2(a); rs[2 * it] = lo2(b); rs[2 * it + 1] = hi2(b); } };
  auto affN = [&](int n, float c, const f32x2 (&nm)[4], const f32x2 (&rs)[4], f32x2 (&v)[4]) { const f32x2 c2 = {c, c};
    v[0] = fma2(nm[0], c2, lo2(acc[n][0])) * rs[0]; v[1] = fma2(nm[1], c2, hi2(acc[n][0])) * rs[1]; v[2] = fma2(nm[2], c2, lo2(acc[n][1])) * rs[2]; v[3] = fma2(nm[3], c2, hi2(acc[n][1])) * rs[3]; };

  if constexpr (H == 1) { __builtin_amdgcn_s_setprio(1); waitv<5>(); } else waitv<4>();
  bar();
  STAMP(1);
  pass(IC<0>{});
  STAMP(2);
  if constexpr (H == 0) {
    dma4(mkrs(P.wsp, S * S * 2), lds + L_DUMMY, lane * 128, (blockIdx.x * 4 + wm) * 8192);
    if (wm == 0) dma4(mkrs(P.bsp, S * 2), lds + L_BIAS, lane * 4, pi * 256);
  }
  {
    f32x2 nm[4], rs[4]; rowstat(nm, rs);
    LAS char* const sw = lds + L_STASH + (wm * 9 + NB) * 1024 + fr * 64 + (((g >> 1) ^ ((fr >> 2) & 1)) * 32) + (g & 1) * 16;
    LAS char* const sr = lds + L_STASH + (wm * 9 + NB) * 1024 + (4 * g + (fr >> 2)) * 64 + (g & 1) * 32 + (fr & 3) * 8;
    LAS char* const sr1 = lds + L_STASH + (wm * 9 + NB) * 1024 + (4 * g + (fr >> 2)) * 64 + ((g & 1) ^ 1) * 32 + (fr & 3) * 8;
    float cn[NF];
#pragma unroll
    for (int n = 0; n < NF; ++n) cn[n] = csum1(0, (NB + n) * 16 + fr);
#pragma unroll
    for (int n = 0; n < NF; ++n) { f32x2 v[4]; affN(n, cn[n], nm, rs, v);
      *(LAS u32x4*)(sw + n * 1024) = (u32x4){pk2v(gelu2(v[0])), pk2v(gelu2(v[1])), pk2v(gelu2(v[2])), pk2v(gelu2(v[3]))}; }
    asm volatile("" ::: "memory");
    float s1[2] = {0.f, 0.f}, s2[2] = {0.f, 0.f};
#pragma unroll
    for (int n = 0; n < NF; ++n)
#pragma unroll
      for (int ih = 0; ih < 2; ++ih) { const u32x2 t = __builtin_bit_cast(u32x2, __builtin_amdgcn_ds_read_tr16_b64_v4i16((LAS s16x4*)((ih ? sr1 : sr) + n * 1024)));
        s1[ih] = dot2(t.x, ONES, s1[ih]); s2[ih] = dot2(t.x, t.x, s2[ih]); s1[ih] = dot2(t.y, ONES, s1[ih]); s2[ih] = dot2(t.y, t.y, s2[ih]); }
    const float a0 = gsum(s1[0]), b0 = gsum(s2[0]), a1 = gsum(s1[1]), b1 = gsum(s2[1]);
    if (g < 2) *(LAS f32x2*)(lds + L_ZPART + (H * 128 + 32 * wm + 16 * g + fr) * 8) = g ? (f32x2){a1, b1} : (f32x2){a0, b0};
    waitl0();
  }
  STAMP(3);
  pass(IC<1>{});
  STAMP(4);
  u32x2 pq[3][2];
  f32x4 aq[3][2];
  if constexpr (H == 0) {
    f32x2 nm[4], rs[4]; rowstat(nm, rs);
    float cn[5];
#pragma unroll
    for (int n = 0; n < 5; ++n) cn[n] = csum1(1, n * 16 + fr);
    bf16x8 pkf[3], vf[3];
#pragma unroll
    for (int n = 0; n < 5; ++n) { f32x2 v[4]; affN(n, cn[n], nm, rs, v);
      if (n >= 2) { u32x4 w = {pk2v(v[0]), pk2v(v[1]), pk2v(v[2]), pk2v(v[3])};
        if (n == 4 && fr == 0) w = (u32x4){ONES, ONES, ONES, ONES};
        vf[n - 2] = __builtin_bit_cast(bf16x8, w); }
      if (n < 2 || n == 4) pkf[n == 4 ? 2 : n] = __builtin_bit_cast(bf16x8, (u32x4){pk2v(phi2(v[0])), pk2v(phi2(v[1])), pk2v(phi2(v[2])), pk2v(phi2(v[3]))});
    }
#pragma unroll
    for (int td = 0; td < 3; ++td)
#pragma unroll
      for (int te = 0; te < 3; ++te) { if (td == 2 && te == 0) continue;
        *(LAS f32x4*)(lds + L_KVS + (wm * 8 + td * 3 + te - (td == 2)) * 1024 + lane * 16) = mfma(pkf[td], vf[te], (f32x4){0, 0, 0, 0}); }
  } else {
#pragma unroll
    for (int n = 3; n < 9; ++n) z2n_tile(n);
#pragma unroll
    for (int n = 0; n < 3; ++n) { aq[n][0] = acc[n][0]; aq[n][1] = acc[n][1]; }
  }
  STAMP(6);
  pass(IC<2>{});
  STAMP(7);
  u32x2 z1[NF][2];
  f32x4 ag[NF][2];
  {
    constexpr int NR = NF + 2;
    LAS char* const aZ = lds + NB * 1024 + lane * 16;
    mk_aR();
    if constexpr (H == 0) vB[0] = lane * 16;
    f32x4 kvs = {0, 0, 0, 0}; u32x4 kl[2][2]; int kvo;
    if constexpr (H == 0) { const int t = wm * 64 + lane; kvo = t < 180 ? t * 16 : OOB; }
    bf16x8 fa[2][NR], fb[2][NR];
    auto rd1 = [&](bf16x8 (&f)[2][NR], int i, int slot) {
      const int h = i & 1; int ii = i >> 1; if (ii == 1) ii = 2; else if (ii == 2) ii = 1;
      if (ii < 2) f[h][ii] = *(LAS bf16x8*)((h ? aR1 : aR0) + slot * R_SZ + ii * 2048); else f[h][ii] = *(LAS bf16x8*)(aZ + slot * C_SZ + (h * 9 + ii - 2) * 1024);
    };
    float zn[2], zr[2], za, zb, ta, tb; f32x2 zc;
    LAS char* const aCs = lds + L_CS + 576 + (NB * 16 + 4 * g) * 4;
#pragma unroll
    for (int it = 0; it < 2; ++it) { zn[it] = *(LAS float*)(lds + L_XSTAT + (32 * wm + 16 * it + fr) * 4); zr[it] = *(LAS float*)(lds + L_XSTAT + 512 + (32 * wm + 16 * it + fr) * 4); }
    float GC1 = 2.3022082f; asm volatile("" : "+v"(GC1));
    auto zop = [&](int e, int k) { const int n = e >> 2, it = (e >> 1) & 1, h = e & 1;
      if (k == 0) zc = *(LAS f32x2*)(aCs + 576 + n * 64 + h * 8);
      if (k == 3) asm volatile("v_fma_f32 %0, %2, %4, %5\n\tv_fma_f32 %1, %3, %4, %6" : "=&v"(za), "=&v"(zb) : "v"(zc.x), "v"(zc.y), "v"(zn[it]), "v"(acc[n][it][2 * h]), "v"(acc[n][it][2 * h + 1]));
      if (k == 4) asm volatile("v_mul_f32 %0, %0, %2\n\tv_mul_f32 %1, %1, %2" : "+v"(za), "+v"(zb) : "v"(zr[it]));
      if (k == 5) asm volatile("v_mul_f32 %0, %2, %2\n\tv_mul_f32 %1, %3, %3" : "=&v"(ta), "=&v"(tb) : "v"(za), "v"(zb));
      if (k == 6) asm volatile("v_fmamk_f32 %0, %0, 0x3dd2d3e8, %2\n\tv_fmamk_f32 %1, %1, 0x3dd2d3e8, %2" : "+v"(ta), "+v"(tb) : "v"(GC1));
      if (k == 7) asm volatile("v_mul_f32 %0, %0, %2\n\tv_mul_f32 %1, %1, %3" : "+v"(ta), "+v"(tb) : "v"(za), "v"(zb));
      if (k == 8) asm volatile("v_exp_f32 %0, %0" : "+v"(ta));
      if (k == 9) asm volatile("v_exp_f32 %0, %0" : "+v"(tb));
      if (k == 10) asm volatile("v_add_f32 %0, 1.0, %0\n\tv_add_f32 %1, 1.0, %1" : "+v"(ta), "+v"(tb));
      if (k == 11) asm volatile("v_rcp_f32 %0, %0" : "+v"(ta));
      if (k == 12) asm volatile("v_rcp_f32 %0, %0" : "+v"(tb));
      if (k == 13) asm volatile("v_fma_f32 %0, -%0, %2, %0\n\tv_fma_f32 %1, -%1, %3, %1" : "+v"(za), "+v"(zb) : "v"(ta), "v"(tb));
      if (k == 14) { unsigned o; asm volatile("v_cvt_pk_bf16_f32 %0, %1, %2" : "=v"(o) : "v"(za), "v"(zb)); z1[n][it][h] = o; } };
    auto qop = [&](int e, int k) { const int n = e >> 2, it = (e >> 1) & 1, h = e & 1;
      if (k == 0) zc = *(LAS f32x2*)(aCs + n * 64 + h * 8);
      if (k == 3) asm volatile("v_fma_f32 %0, %2, %4, %5\n\tv_fma_f32 %1, %3, %4, %6" : "=&v"(za), "=&v"(zb) : "v"(zc.x), "v"(zc.y), "v"(zn[it]), "v"(aq[n][it][2 * h]), "v"(aq[n][it][2 * h + 1]));
      if (k == 4) asm volatile("v_mul_f32 %0, %0, %2\n\tv_mul_f32 %1, %1, %2" : "+v"(za), "+v"(zb) : "v"(zr[it]));
      if (k == 5) asm volatile("v_min_f32 %0, 0, %2\n\tv_min_f32 %1, 0, %3" : "=&v"(ta), "=&v"(tb) : "v"(za), "v"(zb));
      if (k == 6) asm volatile("v_mul_f32 %0, 0x3fb8aa3b, %0\n\tv_mul_f32 %1, 0x3fb8aa3b, %1" : "+v"(ta), "+v"(tb));
      if (k == 7) asm volatile("v_exp_f32 %0, %0" : "+v"(ta));
      if (k == 8) asm volatile("v_exp_f32 %0, %0" : "+v"(tb));
      if (k == 9) asm volatile("v_max_f32 %0, 0, %0\n\tv_max_f32 %1, 0, %1" : "+v"(za), "+v"(zb));
      if (k == 10) asm volatile("v_add_f32 %0, %0, %2\n\tv_add_f32 %1, %1, %3" : "+v"(za), "+v"(zb) : "v"(ta), "v"(tb));
      if (k == 11) asm volatile("v_add_f32 %0, 0x358637bd, %0\n\tv_add_f32 %1, 0x358637bd, %1" : "+v"(za), "+v"(zb));
      if (k == 12) { unsigned o; asm volatile("v_cvt_pk_bf16_f32 %0, %1, %2" : "=v"(o) : "v"(za), "v"(zb)); pq[n][it][h] = o; } };
    auto kvscatter = [&]() { const int t = wm * 64 + opq(lane0); const u32x2 tb = *(LAS u32x2*)(lds + L_TAB + min(t, 179) * 16 + 8);
      const unsigned k01 = pk2v(lo2(kvs)), k23 = pk2v(hi2(kvs)), msk = t < 180 ? 0xffffu : 0u; LAS char* const a = lds + (t < 180 ? L_KVF : L_DUMMY);
      *(LAS u16*)(a + (tb.x & msk)) = (u16)k01; *(LAS u16*)(a + ((tb.x >> 16) & msk)) = (u16)(k01 >> 16); *(LAS u16*)(a + (tb.y & msk)) = (u16)k23; *(LAS u16*)(a + ((tb.y >> 16) & msk)) = (u16)(k23 >> 16); };
    auto ostep = [&]() {
      const int l = opq(lane0), r = l & 15, gg = l >> 4; bf16x8 kf[3][2];
#pragma unroll
      for (int en = 0; en < 3; ++en) { kf[en][0] = *(LAS bf16x8*)(lds + L_KVF + (en * 2) * 1024 + l * 16); kf[en][1] = *(LAS bf16x8*)(lds + L_KVF + (en * 2 + 1) * 1024 + l * 16); }
#pragma unroll
      for (int it = 0; it < 2; ++it) { const bf16x8 b0 = cat(pq[0][it], pq[1][it]), b1 = cat(pq[2][it], (u32x2){0, 0}); f32x4 nm[3];
#pragma unroll
        for (int en = 0; en < 3; ++en) nm[en] = mfma(kf[en][1], b1, mfma(kf[en][0], b0, (f32x4){0, 0, 0, 0}));
        const float rA = __builtin_amdgcn_rcpf(__shfl(nm[2][0], 16 + r) + 1e-6f), rB = __builtin_amdgcn_rcpf(__shfl(nm[2][1], 16 + r) + 1e-6f);
        const float rM = gg == 0 ? rA : rB;
#pragma unroll
        for (int en = 0; en < 3; ++en) { const float r01 = en == 0 ? rA : en == 1 ? rM : rB, r23 = en == 0 ? rA : rB;
          const u32x2 o = {pk2v(lo2(nm[en]) * r01), pk2v(hi2(nm[en]) * r23)};
          const int oc4 = 9 * gu + 4 * en + gg;
          int vo = (oc4 >> 3) * 8192 + ((oc4 >> 2) & 1) * 512 + ((oc4 & 3) * 16 + r) * 8;
          if (en == 2) vo = gg != 0 ? OOB : vo;
          __builtin_amdgcn_raw_buffer_store_b64(o, rsWs, vo, yb + (2 * wm + it) * 1024, SC1); } } };
    auto step = [&](bf16x8 (&cur)[2][NR], bf16x8 (&nxt)[2][NR], auto Pc) {
      constexpr int p = decltype(Pc)::value, st = 30 + p, e = p - (H ? 12 : 1);
      auto vop = [&](int k) { if (H == 1 && p < 12) qop(p, k); else if (e >= 0 && e < 4 * NF) zop(e, k); };
      if constexpr (H == 1) { if (p == 14) ostep(); if (p == 22) cvoff(vA, 2); waitv<allow1(27 + p)>(); }
      else if (H == 0 && p >= 2 && p <= 9) { if (p <= 8) asm volatile("s_waitcnt vmcnt(6) ; HIDDEN_WAIT %0 %1" : "+v"(kl[p & 1][0]), "+v"(kl[p & 1][1]) :: "memory");
                                             else asm volatile("s_waitcnt vmcnt(4) ; HIDDEN_WAIT %0 %1" : "+v"(kl[p & 1][0]), "+v"(kl[p & 1][1]) :: "memory"); }
      else if (H == 0 && p == 1) waitv<6>();
      else if (H == 0 && (p == 18 || p == 20)) waitv<5>();
      else waitv<4>();
      if (p == 31) waitl0();
      bar();
      SB();
      if constexpr (H == 0) {
        if (p >= 2 && p <= 9) { kvs += __builtin_bit_cast(f32x4, kl[p & 1][0]) + __builtin_bit_cast(f32x4, kl[p & 1][1]); asm volatile("" : "+v"(kvs)); }
        if (p < 8) { HLOAD(kl[p & 1][0], kvo, rsWs, colb + C_KVP + 2 * p * KVP_STRIDE); HLOAD(kl[p & 1][1], kvo, rsWs, colb + C_KVP + (2 * p + 1) * KVP_STRIDE); }
        if (p == 10) kvscatter();
        if (p == 17) raise(rowb + C_FLAGO + pj * 4);
        if (p == 19) raise(W_EPOCH, blockIdx.x == 0);
        if (p == 22) { codewarm();
          if (wm == 2) dma4(mkrs(P.out + (size_t)pi * 128 * D, 128 * D * 2), lds + L_DUMMY, opq(lane0) * 2304, 0);
        }
        if (p == 27) flags_dma((opq(lane0) & 15) * 4, rowb + C_FLAGO);
        if (p == 28) check_flags(rowb + C_FLAGO + (lane0 & 15) * 4);
      }
      ring(st, 4, p + 3);
      SB();
      vop(0);
#pragma unroll
      for (int i = 0; i < 4 * NF; ++i) {
        const int n = i >> 2, m = (i >> 1) & 1, h = i & 1;
        if (p == 0 && h == 0) mf0(ag[n][m], cur[0][2 + n], cur[0][m]); else mfa(ag[n][m], cur[h][2 + n], cur[h][m]);
        SB();
        if (p != 31 && i < 2 * NR) rd1(nxt, i, p & 3);
        if (i > 0) vop(i);
        if ((i & 3) == 3 && i < 16) ring(st, i >> 2, p + 3);
        SB();
      }
    };
#pragma unroll
    for (int i = 0; i < 2 * NR; ++i) rd1(fa, i, 3);
    STAMP(9);
    sfor<0, 16>([&](auto Kc) __attribute__((always_inline)) { constexpr int k = decltype(Kc)::value; step(fa, fb, IC<2 * k>{}); step(fb, fa, IC<2 * k + 1>{}); if (k == 5) STAMP(8); });
    accfence(ag);
  }
  STAMP(10);
  relane();
  {
    u32x2 y[NF][2];
#pragma unroll
    for (int it = 0; it < 2; ++it) { const float b = blo(*(LAS u16*)(lds + L_BIAS + (32 * wm + 8 * (fr >> 2) + 4 * it + (fr & 3)) * 2));
#pragma unroll
      for (int n = 0; n < NF; ++n) y[n][it] = (u32x2){pk2v(unpk(z1[n][it].x) * (lo2(ag[n][it]) + b)), pk2v(unpk(z1[n][it].y) * (hi2(ag[n][it]) + b))}; }
    waitv<0>();
#pragma unroll
    for (int hh = 0; hh < 2; ++hh)
#pragma unroll
      for (int it = 0; it < 2; ++it) { const u32x4 v = __builtin_bit_cast(u32x4, cat(y[2 * hh][it], y[2 * hh + 1][it]));
        st16<SC1>(v, rsWs, lane * 16, yb + (4 + 2 * gu + H) * 16384 + hh * 8192 + (2 * wm + it) * 1024);
        *(LAS u32x4*)(lds + (H ? L_YOWN : R_OFF + 2 * R_SZ) + hh * 8192 + (2 * wm + it) * 1024 + lane * 16) = v; }
    if constexpr (H == 0) {
#pragma unroll
      for (int it = 0; it < 2; ++it) __builtin_amdgcn_raw_buffer_store_b64(y[4][it], rsWs, lane * 8, yb + 3 * 16384 + (gu >> 1) * 8192 + (2 * wm + it) * 1024 + (gu & 1) * 512, SC1);
    }
  }
  STAMP(11);
  {
    LAS char* const aY = lds + R_OFF + 2 * wm * 1024;
    bf16x8 rf[2][2][2]; s16x4 bq[3][4];
    mk_aC();
    auto rdB = [&](int u, int j) { const int q = u / NF, n = u % NF; if (u < 12 * NF && (q != 2 || j < 2)) bq[u % 3][j] = tr1(((j & 2) ? aC1 : aC0) + ((59 + q) & 3) * C_SZ + n * 32 + (j & 1) * (8 * 288)); };
    auto rdY = [&](int q, int j) { const int it = j >> 1, hh = j & 1; LAS char* a = aY + (q == 4 ? L_YOWN - R_OFF : ((59 + q) & 3) * R_SZ) + hh * 8192 + it * 1024;
      if (q == 2) { if (hh == 0) rf[q & 1][it][0] = cat(*(LAS u32x2*)(a + lane * 8), (u32x2){0, 0}); }
      else if (q < 2 || q == 11) rf[q & 1][it][hh] = cat(*(LAS u32x2*)(a + lane * 8), *(LAS u32x2*)(a + 512 + lane * 8));
      else rf[q & 1][it][hh] = *(LAS bf16x8*)(a + lane * 16); };
    rdY(0, 0); rdY(0, 1); rdY(0, 2); rdY(0, 3);
#pragma unroll
    for (int j = 0; j < 8; ++j) rdB(j >> 2, j & 3);
    SB();
    sfor<0, 12 * NF>([&](auto Uc) __attribute__((always_inline)) {
      constexpr int u = decltype(Uc)::value, q = u / NF, n = u % NF;
      if (n == NF - 2 && q < 11) {
        if constexpr (H == 1) { if (q == 10) waitv<0>(); else if (q >= 1) waitv<5>(); }
        else { if (q == 1 || q == 3 || q == 10) waitv<0>(); else if (q >= 4) waitv<4>(); }
        bar();
        if constexpr (H == 0) {
          if (q == 1) raise(rowb + C_FLAGD + gc * 128 + gu * 4);
          if (q == 2) flags_dma((opq(lane0) & 3) * 4, rowb + C_FLAGD + gc * 128);
          if (q == 3) { STAMP(12); check_flags(rowb + C_FLAGD + gc * 128 + (lane0 & 3) * 4); STAMP(13); ring_all(64); }
          if (q == 4) STAMP(14);
        }
        SB();
      }
      const bf16x8 b0 = cat4(bq[u % 3][0], bq[u % 3][1]), b1 = q == 2 ? b0 : cat4(bq[u % 3][2], bq[u % 3][3]);
#pragma unroll
      for (int j = 0; j < 4; ++j) { const int it = j >> 1, hh = j & 1;
        if (q == 0 && hh == 0) mf0(acc[n][it], b0, rf[q & 1][it][hh]); else if (q != 2 || hh == 0) mfa(acc[n][it], hh ? b1 : b0, rf[q & 1][it][hh]);
        SB();
        rdB(u + 2, j);
        if (n == NF - 2 && q < 11) rdY(q + 1, j);
        if (j == 3) { const int d = u - (NF - 2); if (d >= 0) { const int qq = d / NF + 3; if (qq < 12 && (H == 1 || qq >= 6)) ring_pos(59 + qq, d % NF, NF); } }
        SB(); }
    });
    accfence(acc);
  }
  STAMP(15);
  relane();
  {
    const rsrc_t rsOut = mkrs(P.out + (size_t)pi * 128 * D, 128 * D * 2);
    auto packp = [&](const f32x4 (&a)[2]) { u32x4 v = {pk2v(lo2(a[0])), pk2v(hi2(a[0])), pk2v(lo2(a[1])), pk2v(hi2(a[1]))};
      v.x = min(v.x, 0xfffffffeu); v.y = min(v.y, 0xfffffffeu); v.z = min(v.z, 0xfffffffeu); v.w = min(v.w, 0xfffffffeu); return v; };
    constexpr int NK = H ? 1 : 2;
    f32x4 kp[NK][2]; int cb[NK];
#pragma unroll
    for (int it = 0; it < 2; ++it) { kp[0][it] = gc == 0 ? acc[0][it] : gc == 1 ? acc[1][it] : gc == 2 ? acc[2][it] : acc[3][it]; if constexpr (H == 0) kp[1][it] = acc[4][it]; }
    cb[0] = 256 * gu + 32 * (H * 4 + gc); if constexpr (H == 0) cb[1] = 1024 + 32 * gu;
    const int off1 = wm == gc ? 0 : OOB;
    int ro[2]; u32x2 xr[NK][2];
#pragma unroll
    for (int it = 0; it < 2; ++it) { ro[it] = (32 * wm + 8 * (fr >> 2) + 4 * it + (fr & 3)) * 1152 + 8 * g;
#pragma unroll
      for (int k = 0; k < NK; ++k) xr[k][it] = __builtin_amdgcn_raw_buffer_load_b64(rsX, ro[it] + (k ? off1 : 0), cb[k], 0); }
#pragma unroll
    for (int n = 0; n < NF; ++n) { const int dg = n < 4 ? n : wm, slot = n < 4 ? H * 4 + wm : 8;
      st16<SC1>(packp(acc[n]), rsWs, dg == gc ? OOB : lane * 16, rowb + C_PART + ((4 * dg + gu) * 4 + gc) * PART_SRC + slot * 1024); }
    const int pb = rowb + C_PART + pj * 4 * PART_SRC;
    u32x4 v[NK][3];
    __builtin_amdgcn_s_sleep(FIN_SLEEP);
    _Pragma("unroll 1") for (unsigned sp = 0;; ++sp) {
      unsigned mx = 0;
#pragma unroll
      for (int k = 0; k < NK; ++k)
#pragma unroll
        for (int j = 0; j < 3; ++j) { const int so = pb + ((gc + 1 + j) & 3) * PART_SRC + (k ? 8 : H * 4 + wm) * 1024;
          v[k][j] = __builtin_amdgcn_raw_buffer_load_b128(rsWs, lane * 16 + (k ? off1 : 0), so, SC1);
          mx = max(max(mx, max(v[k][j].x, v[k][j].y)), max(v[k][j].z, v[k][j].w)); }
      if (LIKELY(__all(mx != ~0u))) break;
      if (sp > SPIN_MAX) { set_flag(P.ws, W_TMO, 1u); break; }
      __builtin_amdgcn_s_sleep(FIN_POLL);
    }
#pragma unroll
    for (int k = 0; k < NK; ++k)
#pragma unroll
      for (int it = 0; it < 2; ++it) { f32x2 a = lo2(kp[k][it]) + unpk(xr[k][it].x), b = hi2(kp[k][it]) + unpk(xr[k][it].y);
#pragma unroll
        for (int j = 0; j < 3; ++j) { a += unpk(v[k][j][2 * it]); b += unpk(v[k][j][2 * it + 1]); }
        __builtin_amdgcn_raw_buffer_store_b64((u32x2){pk2v(a), pk2v(b)}, rsOut, ro[it] + (k ? off1 : 0), cb[k], 0); }
  }
  if constexpr (H == 0) asm volatile(".Lmk_end:");
}

__global__ __launch_bounds__(512, 2) void fused(const u16* x, const u16* wqkv, const u16* wo, const u16* win, const u16* wsp, const u16* bsp, const u16* wout, u16* out, char* ws) {
  extern __shared__ __attribute__((aligned(16))) char smem[];
  LAS char* lds = (LAS char*)smem;
  const int lane = threadIdx.x & 63, wid = __builtin_amdgcn_readfirstlane(threadIdx.x >> 6);
  const int b = blockIdx.x, xc = b & 7, q = b >> 3;
  const int pi = 8 * (xc & 1) + (q & 7), pj = 4 * (xc >> 1) + (q >> 3);
  const unsigned epoch = *(const __attribute__((address_space(4))) unsigned*)(ws + W_EPOCH);
  const Ptrs P{x, wqkv, wo, win, wsp, bsp, wout, out, ws};
  unsigned long long pc; int clen;
  asm volatile("s_getpc_b64 %0\n.Lmk_pc:\n\ts_mov_b32 %1, .Lmk_end-.Lmk_pc" : "=s"(pc), "=s"(clen));
  const rsrc_t rsCode = mkrs((const void*)pc, clen);
  if (wid < 4) body<0>(P, lds, wid, lane, pi, pj, epoch, rsCode); else body<1>(P, lds, wid - 4, lane, pi, pj, epoch, rsCode);
}
}

extern "C" void kernel_launch(void* const* d_in, const int* in_sizes, int n_in, void* d_out, int out_size, void* d_ws, size_t ws_size, hipStream_t stream) {
  using mk::u16;
  (void)hipFuncSetAttribute((const void*)mk::fused, hipFuncAttributeMaxDynamicSharedMemorySize, mk::LDS_TOTAL);
  mk::fused<<<256, 512, mk::LDS_TOTAL, stream>>>((const u16*)d_in[0], (const u16*)d_in[1], (const u16*)d_in[2], (const u16*)d_in[4], (const u16*)d_in[5], (const u16*)d_in[6],
                                                 (const u16*)d_in[7], (u16*)d_out, (char*)d_ws);
}
```

```cpp
#include <hip/hip_runtime.h>
#include <hip/hip_bf16.h>
#include <type_traits>

namespace mk {
typedef unsigned short u16;
typedef short bf16x8 __attribute__((ext_vector_type(8)));
typedef short s16x4 __attribute__((ext_vector_type(4)));
typedef float f32x4 __attribute__((ext_vector_type(4)));
typedef float f32x2 __attribute__((ext_vector_type(2)));
typedef unsigned u32x4 __attribute__((ext_vector_type(4)));
typedef unsigned u32x2 __attribute__((ext_vector_type(2)));
typedef __bf16 bf16x2_t __attribute__((ext_vector_type(2)));
#define LAS __attribute__((address_space(3)))
#define GAS __attribute__((address_space(1)))
#define DI __device__ __forceinline__
template <int V> using IC = std::integral_constant<int, V>;
template <int B, int E, class F> __device__ __forceinline__ void sfor(F&& f) { if constexpr (B < E) { f(IC<B>{}); sfor<B + 1, E>(f); } }

constexpr int S = 2048, D = 576, FH = 2304;
constexpr int C_SZ = 18432, R_OFF = 73728, R_SZ = 16384, MISC = 139264;
constexpr int L_STASH = R_OFF, L_KVS = R_OFF + 36864;
constexpr int L_ZPART = MISC + 4096, L_KVF = L_ZPART + 2048, L_XSTAT = L_KVF + 6144, L_CS = L_XSTAT + 1024, L_TAB = L_CS + 1792;
constexpr int L_LAND0 = MISC;
constexpr int L_BIAS = L_TAB + 3072, L_FLG = L_BIAS + 256, L_DUMMY = L_FLG + 1024, L_STAMP = L_DUMMY + 1024;
constexpr int L_YOWN = MISC;
static_assert(L_YOWN + 16384 <= L_BIAS, "lds");
constexpr int LDS_TOTAL = 163840;
static_assert(L_STAMP + 512 <= LDS_TOTAL, "lds");
constexpr int W_EPOCH = 0, W_TMO = 256, W_STAMP = 32768, W_COL = 2 << 20, W_ROW = 18 << 20, W_END = 50 << 20;
constexpr int COL_SZ = 1 << 20, C_ZF = 0, C_KVP = 0x90000, C_CSG = 0xA0000, C_FLAGB = 0xB0000;
constexpr int ROW_SZ = 2 << 20, C_YF = 0, C_PART = 0xC0000, C_STATS = 0x150000, C_FLAGO = 0x158000, C_FLAGD = 0x158100;
constexpr int KVP_STRIDE = 2944, YF_GRP = 12 * 16384, PART_SRC = 9216, OOB = 0x40000000;
constexpr int SC1 = 16;
constexpr unsigned SPIN_MAX = 1u << 16;
#define FIN_SLEEP 16
#define FIN_POLL 4
#define ABL 0
constexpr int vpre1(int sg) { return sg == 10 ? 1 : 0; }
constexpr int vpost1(int sg) { return sg == 1 ? 2 : (sg >= 2 && sg <= 7) || sg == 10 || sg == 24 ? 1 : sg == 12 || sg == 13 ? 4 : sg == 17 ? 3 : sg == 18 ? 6 : sg == 19 ? 2 : sg == 27 + 14 || sg == 27 + 15 ? 3 : 0; }
constexpr int allow1(int sg) { return 5 + (sg == 25 ? 0 : vpost1(sg - 1)) + vpre1(sg) + vpost1(sg); }

template <int N> DI void waitv() { asm volatile("s_waitcnt vmcnt(%0)" ::"n"(N) : "memory"); }
DI void waitl0() { __builtin_amdgcn_s_waitcnt(0xC07F); asm volatile("" ::: "memory"); }
DI void bar() { asm volatile("" ::: "memory"); __builtin_amdgcn_s_barrier(); asm volatile("" ::: "memory"); }
#define SB() __builtin_amdgcn_sched_barrier(0)
DI f32x4 mfma(bf16x8 a, bf16x8 b, f32x4 c) { return __builtin_amdgcn_mfma_f32_16x16x32_bf16(a, b, c, 0, 0, 0); }
DI void mfa(f32x4& c, const bf16x8& a, const bf16x8& b) { asm volatile("v_mfma_f32_16x16x32_bf16 %0, %1, %2, %0" : "+v"(c) : "v"(a), "v"(b)); }
DI void mf0(f32x4& c, const bf16x8& a, const bf16x8& b) { asm volatile("v_mfma_f32_16x16x32_bf16 %0, %1, %2, 0" : "=&v"(c) : "v"(a), "v"(b)); }
DI int opq(int v) { asm volatile("" : "+v"(v)); return v; }
template <int N> DI void accfence(f32x4 (&a)[N][2]) {
  if constexpr (N == 5) asm volatile("s_nop 7\n\ts_nop 1" : "+v"(a[0][0]), "+v"(a[0][1]), "+v"(a[1][0]), "+v"(a[1][1]), "+v"(a[2][0]), "+v"(a[2][1]), "+v"(a[3][0]), "+v"(a[3][1]), "+v"(a[4][0]), "+v"(a[4][1]));
  else asm volatile("s_nop 7\n\ts_nop 1" : "+v"(a[0][0]), "+v"(a[0][1]), "+v"(a[1][0]), "+v"(a[1][1]), "+v"(a[2][0]), "+v"(a[2][1]), "+v"(a[3][0]), "+v"(a[3][1]));
}
DI unsigned pk2(float lo, float hi) { f32x2 v = {lo, hi}; return __builtin_bit_cast(unsigned, __builtin_convertvector(v, bf16x2_t)); }
DI float blo(unsigned u) { return __uint_as_float(u << 16); }
DI float dot2(unsigned a, unsigned b, float c) { return __builtin_amdgcn_fdot2_f32_bf16(__builtin_bit_cast(bf16x2_t, a), __builtin_bit_cast(bf16x2_t, b), c, false); }
DI f32x2 gelu2(f32x2 v) {
  const f32x2 a = v * __builtin_elementwise_fma(v * v, (f32x2){0.10294324f, 0.10294324f}, (f32x2){2.3022082f, 2.3022082f});
  const f32x2 d = (f32x2){__builtin_amdgcn_exp2f(a.x), __builtin_amdgcn_exp2f(a.y)} + 1.0f;
  const f32x2 r = {__builtin_amdgcn_rcpf(d.x), __builtin_amdgcn_rcpf(d.y)};
  return __builtin_elementwise_fma(-v, r, v);
}
DI unsigned pk2v(f32x2 v) { return __builtin_bit_cast(unsigned, __builtin_convertvector(v, bf16x2_t)); }
DI f32x2 unpk(unsigned u) { return (f32x2){__uint_as_float(u << 16), __uint_as_float(u & 0xffff0000u)}; }
DI f32x2 lo2(f32x4 a) { return (f32x2){a.x, a.y}; }
DI f32x2 hi2(f32x4 a) { return (f32x2){a.z, a.w}; }
DI f32x2 fma2(f32x2 a, f32x2 b, f32x2 c) { return __builtin_elementwise_fma(a, b, c); }
DI f32x2 phi2(f32x2 t) {
  const f32x2 a = t * 1.44269504f, p = t + 1.0f;
  const f32x2 r = {t.x > 0.f ? p.x : __builtin_amdgcn_exp2f(a.x), t.y > 0.f ? p.y : __builtin_amdgcn_exp2f(a.y)};
  return r + 1e-6f;
}
DI float hsum32(float v) { const unsigned u = __float_as_uint(v); const auto r = __builtin_amdgcn_permlane32_swap(u, u, false, false); return __uint_as_float(r[0]) + __uint_as_float(r[1]); }
DI float hsum16(float v) { const unsigned u = __float_as_uint(v); const auto r = __builtin_amdgcn_permlane16_swap(u, u, false, false); return __uint_as_float(r[0]) + __uint_as_float(r[1]); }
template <int N> DI float bcast16(float v) { return __builtin_bit_cast(float, __builtin_amdgcn_update_dpp(0, __builtin_bit_cast(int, v), 0x150 + N, 0xf, 0xf, true)); }
DI float ror8sum(float v) { return v + __builtin_bit_cast(float, __builtin_amdgcn_update_dpp(0, __builtin_bit_cast(int, v), 0x128, 0xf, 0xf, true)); }
DI float gsum(float v) { return hsum32(hsum16(v)); }
typedef __amdgpu_buffer_rsrc_t rsrc_t;
DI rsrc_t mkrs(const void* p, int bytes) { return __builtin_amdgcn_make_buffer_rsrc((void*)p, 0, bytes, 0x00020000); }
template <int AUX> DI void dma16(rsrc_t rs, LAS char* dst, int voff, int soff) {
  const unsigned m = (unsigned)(size_t)dst;
  if constexpr (AUX == 0) asm volatile("s_mov_b32 m0, %0\n\ts_nop 0\n\tbuffer_load_dwordx4 %1, %2, %3 offen lds" ::"s"(m), "v"(voff), "s"(rs), "s"(soff) : "memory");
  else asm volatile("s_mov_b32 m0, %0\n\ts_nop 0\n\tbuffer_load_dwordx4 %1, %2, %3 offen sc1 lds" ::"s"(m), "v"(voff), "s"(rs), "s"(soff) : "memory");
}
DI void dma4(rsrc_t rs, LAS char* dst, int voff, int soff) {
  const unsigned m = (unsigned)(size_t)dst;
  asm volatile("s_mov_b32 m0, %0\n\ts_nop 3\n\tbuffer_load_dword %1, %2, %3 offen sc1 lds" ::"s"(m), "v"(voff), "s"(rs), "s"(soff) : "memory");
}
template <int AUX> DI void st16(u32x4 v, rsrc_t rs, int voff, int soff) { asm volatile("" : "+v"(v)); __builtin_amdgcn_raw_buffer_store_b128(v, rs, voff, soff, AUX); asm volatile("s_nop 1" ::"v"(v) : "memory"); }
#define LIKELY(x) __builtin_expect(!!(x), 1)
#define UNLIKELY(x) __builtin_expect(!!(x), 0)
#define HLOAD(dst, voff, rs, soff) asm volatile("buffer_load_dwordx4 %0, %1, %2, %3 offen sc1 ; HIDDEN_LOAD" : "=v"(dst) : "v"(voff), "s"(rs), "s"(soff) : "memory")
DI s16x4 tr1(LAS char* a) { return __builtin_amdgcn_ds_read_tr16_b64_v4i16((LAS s16x4*)a); }
DI bf16x8 cat4(s16x4 a, s16x4 b) { return __builtin_shufflevector(a, b, 0, 1, 2, 3, 4, 5, 6, 7); }
DI bf16x8 cat(u32x2 a, u32x2 b) { u32x4 v = {a.x, a.y, b.x, b.y}; return __builtin_bit_cast(bf16x8, v); }

struct Ptrs { const u16 *x, *wqkv, *wo, *win, *wsp, *bsp, *wout; u16* out; char* ws; };

#define STAMP(k)

DI void poll_flags(char* ws, int off, bool active, unsigned tag) {
  GAS unsigned* p = (GAS unsigned*)(ws + off);
  _Pragma("unroll 1") for (unsigned sp = 0;; ++sp) {
    const unsigned v = __hip_atomic_load(p, __ATOMIC_RELAXED, __HIP_MEMORY_SCOPE_AGENT);
    if (__all(!active || v == tag)) break;
    if (sp > SPIN_MAX) { __hip_atomic_store((GAS unsigned*)(ws + W_TMO), 1u, __ATOMIC_RELAXED, __HIP_MEMORY_SCOPE_AGENT); break; }
    __builtin_amdgcn_s_sleep(2);
  }
  asm volatile("" ::: "memory");
}
DI void set_flag(char* ws, int off, unsigned tag) { __hip_atomic_store((GAS unsigned*)(ws + off), tag, __ATOMIC_RELAXED, __HIP_MEMORY_SCOPE_AGENT); }

template <int H>
DI void body(const Ptrs& P, LAS char* lds, const int wm, const int lane0, const int pi, const int pj, unsigned epoch, const rsrc_t rsCode) {
  constexpr int NF = H ? 4 : 5, NB = H ? 5 : 0;
  int lane = lane0, fr = lane & 15, g = lane >> 4;
  auto relane = [&]() { lane = opq(lane0); fr = lane & 15; g = lane >> 4; };
  const int gc = pj >> 2, gu = pj & 3, qo = 4 * (pj & 1);
  const int rot = (pi + 2 * pj) % 9;
  const rsrc_t rsX = mkrs(P.x + (size_t)pi * 128 * D, 128 * D * 2), rsWin = mkrs(P.win, D * 2 * FH * 2), rsQkv = mkrs(P.wqkv, D * 3 * D * 2);
  const rsrc_t rsSp = mkrs(P.wsp + (size_t)pi * 128 * S, 128 * S * 2), rsWs = mkrs(P.ws, W_END);
  const rsrc_t rsWout = mkrs(P.wout, FH * D * 2), rsWo = mkrs(P.wo, D * D * 2);
  const int colb = W_COL + pj * COL_SZ, rowb = W_ROW + pi * ROW_SZ, yb = rowb + C_YF + gc * YF_GRP;
  STAMP(0);

  auto rvoff = [&](int stride) { const int l = opq(lane0); return (8 * (l >> 5) + ((l >> 3) & 3)) * stride + ((l & 7) ^ (l >> 4)) * 16; };
  int vA[5], vB[5];
  auto cvoff = [&](int (&v)[5], int kind, int i0 = 0, int i1 = 5) {
    const int l = opq(lane0);
    const int t = kind == 0 ? pj : gu, b0 = 72 * pj - 8 * (pj & 1);
    const int a0 = 256 * t, a1 = (kind == 0 ? 4096 : 1024) + 32 * t - 128, a2 = 256 * t - 32;
#pragma unroll
    for (int idx = i0; idx < i1; ++idx) {
      const unsigned n = 64 * (wm + 4 * idx) + l, Pp = __umul24(n, 3641u) >> 16, cc = n - __umul24(Pp, 18u);
      unsigned k; int add;
      if (kind == 2) k = (Pp & ~24u) | ((Pp & 8u) << 1) | ((Pp & 16u) >> 1); else k = (Pp & ~12u) | ((Pp & 4u) << 1) | ((Pp & 8u) >> 1);
      if (kind != 1) { add = a0; add = cc >= 8 ? a1 : add; add = cc >= 10 ? a2 : add; }
      else { add = 1152; add = cc >= 4 ? 2304 - 64 : add; add = cc >= 8 ? 1152 - 64 : add; add = cc >= 9 ? 2304 - 80 : add; add = cc >= 10 ? -160 : add; add = cc >= 15 ? OOB : add; add += b0; }
      v[idx] = (int)(__umul24(k, kind == 0 ? 9216u : kind == 1 ? 3456u : 1152u) + 16u * cc) + add;
      if (idx == 4) v[idx] = wm >= 2 ? OOB : v[idx];
      asm volatile("" : "+v"(v[idx]));
    }
  };
  auto ksl = [&](int s) { int k = rot + s; return k >= 9 ? k - 9 : k; };
  auto kord = [&](int p) { return p < 2 ? 2 * pi + p : (p - 2 < 2 * pi ? p - 2 : p); };
  auto g3stage = [&](int q) { return q < 3 ? q : q == 11 ? 3 : 4 + ((2 * gu + (q - 3)) & 7); };
  auto ring = [&](int st, int idx, int dyn = 0) {
    const int slot = st & 3;
    if constexpr (H == 1) {
      LAS char* dst = lds + ((idx == 4 && wm >= 2) ? L_DUMMY : slot * C_SZ + (wm + 4 * idx) * 1024);
      if (st < 9) dma16<0>(rsWin, dst, vA[idx], 4608 + ksl(st) * (64 * 9216));
      else if (st < 18) dma16<0>(rsQkv, dst, vB[idx], ksl(st - 9) * (64 * 3456));
      else if (st < 27) dma16<0>(rsWin, dst, vA[idx], ksl(st - 18) * (64 * 9216));
      else if (st < 59) dma16<SC1>(rsWs, dst, vB[idx == 4], colb + C_ZF + kord(dyn) * C_SZ + (wm + 4 * idx) * 1024);
      else { const int q = st - 59;
        if (q < 3) dma16<0>(rsWo, dst, vA[idx], (144 * gc + 64 * q) * 1152);
        else if (q == 11) dma16<0>(rsWout, dst, vA[idx], (2048 + 64 * gc) * 1152);
        else dma16<0>(rsWout, dst, vA[idx], (512 * gc + 64 * (g3stage(q) - 4)) * 1152); }
    } else {
      if (idx == 4) return;
      LAS char* dst = lds + R_OFF + slot * R_SZ + (4 * wm + idx) * 1024;
      const int rb = 32 * wm + 16 * (idx & 1) + 4 * (idx >> 1);
      if (st < 9) dma16<0>(rsX, dst, vA[idx & 1], rb * 1152 + ksl(st) * 128);
      else if (st < 27) return;
      else if (st < 59) dma16<0>(rsSp, dst, vA[idx & 1], rb * 4096 + kord(dyn) * 128);
      else dma16<SC1>(rsWs, dst, vB[0], yb + g3stage(st - 59) * 16384 + (4 * wm + idx) * 1024);
    }
  };
  auto ring_all = [&](int st) { ring(st, 4); ring(st, 0); ring(st, 1); ring(st, 2); ring(st, 3); };
  auto ring_pos = [&](int st, int k, int nf) {
    const int dyn = st - 27;
    if (nf == 5) { ring(st, k == 0 ? 4 : k - 1, dyn); }
    else if (nf == 4) { if (k == 0) ring(st, 4, dyn); ring(st, k, dyn); }
    else { if (k == 0) { ring(st, 4, dyn); ring(st, 0, dyn); } else if (k == 1) { ring(st, 1, dyn); ring(st, 2, dyn); } else ring(st, 3, dyn); }
  };
  if constexpr (H == 1) cvoff(vA, 0); else { vA[0] = rvoff(1152); vA[1] = vA[0] ^ 64; }
  auto codewarm = [&]() { if (wm == 3) dma4(rsCode, lds + L_DUMMY, opq(lane0) * 128, ((blockIdx.x >> 3) & 15) * 8192); };
  if constexpr (H == 0) codewarm();
  ring_all(0); ring_all(1);
  STAMP(17);
  if constexpr (H == 1) cvoff(vB, 1);
  else {
    const int t = wm * 64 + lane;
    const int rd = (t * 205) >> 10, e0 = 4 * (t - 5 * rd), hd = rd >= 18, dc = qo + rd;
    const int gb = (dc >> 4) * 3072 - (dc >> 5) * 1024 + ((dc >> 2) & 3) * 256 + (dc & 3) * 4, sb = (dc >> 5) * 1024 + ((dc >> 2) & 3) * 256 + ((dc >> 4) & 1) * 8 + (dc & 3) * 2;
    unsigned go[4], so[4];
#pragma unroll
    for (int k = 0; k < 4; ++k) { const int e = e0 + k, ce = qo + 18 * hd + e; int off = (ce >> 4) * 1024 + (ce & 15) * 16; off = ce >= 32 ? off + 128 : off; off = e >= 18 ? 2048 : off;
      int ep = 18 * hd + e; ep = e == 18 ? 36 + hd : ep; ep = e == 19 ? 40 : ep;
      go[k] = gb + off; so[k] = sb + (ep >> 4) * 2048 + (ep & 15) * 16; }
    if (t < 180) *(LAS u32x4*)(lds + L_TAB + t * 16) = (u32x4){go[0] | (go[1] << 16), go[2] | (go[3] << 16), so[0] | (so[1] << 16), so[2] | (so[3] << 16)};
  }
  LAS char *aR0, *aR1, *aC0, *aC1;
  auto mk_aR = [&]() { const int l = opq(lane0), r = l & 15, o = (32 * wm + r) * 128 + (((l >> 4) ^ ((r >> 1) & 7)) * 16); aR0 = lds + R_OFF + o; aR1 = lds + R_OFF + (o ^ 64); };
  auto mk_aC = [&]() { const int l = opq(lane0); aC0 = lds + (16 * (l >> 5) + 4 * ((l >> 4) & 1) + ((l & 15) >> 2)) * 288 + (l & 3) * 8 + NB * 32; aC1 = aC0 + 32 * 288; asm volatile("" : "+v"(aC1)); };
  asm volatile("" : "+s"(epoch));
  const unsigned tag = epoch + 1u;
  unsigned ONES = 0x3f803f80u; asm volatile("" : "+s"(ONES));
  auto raise = [&](int off, bool on = true) { __builtin_amdgcn_raw_buffer_store_b32(tag, rsWs, (opq(lane0) == 0 && wm == 0 && on) ? 0 : OOB, off, SC1); };
  auto flags_dma = [&](int voff, int soff) { dma4(rsWs, lds + L_FLG + wm * 256, voff, soff); };
  auto check_flags = [&](int off) { const unsigned v = *(LAS unsigned*)(lds + L_FLG + wm * 256 + opq(lane0) * 4);
    if (UNLIKELY(!__all(v == tag))) { STAMP(18); poll_flags(P.ws, off, true, tag); } };

  bf16x8 xf[9][2][2];
  f32x4 acc[NF][2];
  f32x4 zm[2], zr2[2];
  auto z2_read = [&](int n) { const int l = opq(lane0), r = l & 15, q = l >> 4;
    return *(LAS u32x4*)(lds + L_STASH + wm * 9216 + n * 1024 + r * 64 + (((q >> 1) ^ ((r >> 2) & 1)) * 32) + (q & 1) * 16); };
  auto z2n_tile = [&](int n, u32x4 z) {
    const u32x4 o = {pk2v(fma2(unpk(z.x), lo2(zr2[0]), lo2(zm[0]))), pk2v(fma2(unpk(z.y), hi2(zr2[0]), hi2(zm[0]))), pk2v(fma2(unpk(z.z), lo2(zr2[1]), lo2(zm[1]))), pk2v(fma2(unpk(z.w), hi2(zr2[1]), hi2(zm[1])))};
    st16<SC1>(o, rsWs, opq(lane0) * 16, colb + C_ZF + (2 * pi + (wm >> 1)) * C_SZ + ((wm & 1) * 9 + n) * 1024); };

  auto pass = [&](auto PASSc) {
    constexpr int PASS = decltype(PASSc)::value;
    constexpr int NFP = (PASS == 1 && H == 1) ? 3 : NF, ST0 = 9 * PASS, NU = 9 * NFP;
    constexpr bool TR = (PASS == 2) || (PASS == 1 && H == 1);
    constexpr int L_LAND = PASS == 0 ? L_LAND0 : PASS == 1 ? L_KVS : R_OFF + 2 * R_SZ;
    const int csg = colb + C_CSG + PASS * 12288;
    s16x4 bq[3][4];
    u32x4 gl[8]; u32x2 zp[2], t2[9]; float tf[16]; f32x4 ct, xs1[2], xs2[2]; float csv = 0.f, cs5 = 0.f;
    auto rdB = [&](int u, int j) { const int s = u / NFP, n = u % NFP; if (u < NU) bq[u % 3][j] = tr1(((j & 2) ? aC1 : aC0) + ((ST0 + s) & 3) * C_SZ + n * 32 + (j & 1) * (8 * 288)); };
    auto rdX = [&](int s, int j) { xf[s][j >> 1][j & 1] = *(LAS bf16x8*)(((j & 1) ? aR1 : aR0) + (s & 3) * R_SZ + (j >> 1) * 2048); };
    auto slot = [&](auto SGc) __attribute__((always_inline)) {
      constexpr int sg = decltype(SGc)::value, s = sg - ST0;
      if (s == 1) { const int l = opq(lane0); const bool on = wm == 0 && pi < 9;
        __builtin_amdgcn_raw_buffer_store_b64((u32x2){__float_as_uint(csv), tag}, rsWs, on ? (NB * 16 + l) * 8 : OOB, csg + rot * 1152, SC1);
        if constexpr (H == 0) __builtin_amdgcn_raw_buffer_store_b64((u32x2){__float_as_uint(cs5), tag}, rsWs, (on && l < 16) ? (64 + l) * 8 : OOB, csg + rot * 1152, SC1); }
      if constexpr (H == 1) {
        if (sg >= 1 && sg <= 7) { const int k = (sg - 1) * 4 + wm, src = k / 9;
          st16<SC1>((u32x4){~0u, ~0u, ~0u, ~0u}, rsWs, (k < 27 && !(ABL & 1)) ? opq(lane0) * 16 : OOB, rowb + C_PART + (pj * 4 + ((gc + 1 + src) & 3)) * PART_SRC + (k - 9 * src) * 1024); }
        if (sg == 8) {
          asm volatile("s_nop 7" : "+v"(xs1[0]), "+v"(xs2[0]), "+v"(xs1[1]), "+v"(xs2[1]));
          const int l = opq(lane0), r = l & 15, j = r & 3;
          LAS char* const o = lds + ((r >> 2) == (l >> 4) ? L_XSTAT + (32 * wm + r) * 4 : L_DUMMY);
#pragma unroll
          for (int it = 0; it < 2; ++it) { float s1 = xs1[it].x, s2 = xs2[it].x;
            s1 = j == 1 ? xs1[it].y : s1; s2 = j == 1 ? xs2[it].y : s2; s1 = j == 2 ? xs1[it].z : s1; s2 = j == 2 ? xs2[it].z : s2; s1 = j == 3 ? xs1[it].w : s1; s2 = j == 3 ? xs2[it].w : s2;
            const float mean = s1 * (1.0f / D), var = fmaxf(s2 * (1.0f / D) - mean * mean, 0.f);
            *(LAS float*)(o + it * 64) = -mean; *(LAS float*)(o + it * 64 + 512) = __builtin_amdgcn_rsqf(var + 1e-5f); } }
        if (s == 7) { const int t = min(wm * 64 + opq(lane0), 143); unsigned bad = 0;
          auto f = [&](int j) { return __uint_as_float(t2[j].x); };
          float c = (((f(0) + f(1)) + (f(2) + f(3))) + ((f(4) + f(5)) + (f(6) + f(7)))) + f(8);
#pragma unroll
          for (int j = 0; j < 9; ++j) bad |= t2[j].y ^ tag;
          if (UNLIKELY(!__all(bad == 0))) {
            _Pragma("unroll 1") for (unsigned sp = 0; !__all(bad == 0); ++sp) {
              STAMP(20);
              if (sp > SPIN_MAX) { set_flag(P.ws, W_TMO, 1u); break; }
              bad = 0;
#pragma unroll
              for (int j = 0; j < 9; ++j) { t2[j] = __builtin_amdgcn_raw_buffer_load_b64(rsWs, t * 8, csg + j * 1152, SC1); bad |= t2[j].y ^ tag; }
              c = (((f(0) + f(1)) + (f(2) + f(3))) + ((f(4) + f(5)) + (f(6) + f(7)))) + f(8);
            } }
          *(LAS float*)(lds + L_CS + PASS * 576 + t * 4) = c; }
        auto svo = [&]() { const int l = opq(lane0); return ((l >> 3) & 1) * 16384 + (32 * wm + 8 * (l >> 4) + (l & 7)) * 16; };
        if (sg == 12 || sg == 13) { const int vo = svo();
#pragma unroll
          for (int j = 4 * (sg - 12); j < 4 * (sg - 11); ++j) HLOAD(gl[j], vo, rsWs, rowb + C_STATS + j * 2048); }
        if (sg == 15) {
          asm volatile("s_waitcnt vmcnt(10) ; HIDDEN_WAIT %0 %1 %2 %3 %4 %5 %6 %7" : "+v"(gl[0]), "+v"(gl[1]), "+v"(gl[2]), "+v"(gl[3]), "+v"(gl[4]), "+v"(gl[5]), "+v"(gl[6]), "+v"(gl[7]) :: "memory");
          float s1, s2; unsigned bad;
          auto red = [&]() { auto a = [&](int j) { return __uint_as_float(gl[j].x); }; auto b = [&](int j) { return __uint_as_float(gl[j].z); }; bad = 0;
            s1 = ((a(0) + a(1)) + (a(2) + a(3))) + ((a(4) + a(5)) + (a(6) + a(7))); s2 = ((b(0) + b(1)) + (b(2) + b(3))) + ((b(4) + b(5)) + (b(6) + b(7)));
#pragma unroll
            for (int j = 0; j < 8; ++j) { bad |= gl[j].y ^ tag; bad |= gl[j].w ^ tag; } };
          red();
          if (UNLIKELY(!__all(bad == 0))) {
            _Pragma("unroll 1") for (unsigned sp = 0; !__all(bad == 0); ++sp) {
              STAMP(19);
              if (sp > SPIN_MAX) { set_flag(P.ws, W_TMO, 1u); break; }
              const int vo = svo();
#pragma unroll
              for (int j = 0; j < 8; ++j) gl[j] = __builtin_amdgcn_raw_buffer_load_b128(rsWs, vo, rowb + C_STATS + j * 2048, SC1);
              red();
            } }
          s1 = ror8sum(s1); s2 = ror8sum(s2);
          const float mu = s1 * (1.0f / FH), var = fmaxf(s2 * (1.0f / FH) - mu * mu, 0.f);
          tf[1] = __builtin_amdgcn_rsqf(var + 1e-5f); tf[0] = -mu * tf[1]; asm volatile("" : "+v"(tf[0]), "+v"(tf[1])); }
        if (sg == 16) {
          zm[0] = (f32x4){bcast16<0>(tf[0]), bcast16<1>(tf[0]), bcast16<2>(tf[0]), bcast16<3>(tf[0])}; zm[1] = (f32x4){bcast16<4>(tf[0]), bcast16<5>(tf[0]), bcast16<6>(tf[0]), bcast16<7>(tf[0])};
          zr2[0] = (f32x4){bcast16<0>(tf[1]), bcast16<1>(tf[1]), bcast16<2>(tf[1]), bcast16<3>(tf[1])}; zr2[1] = (f32x4){bcast16<4>(tf[1]), bcast16<5>(tf[1]), bcast16<6>(tf[1]), bcast16<7>(tf[1])};
          asm volatile("" : "+v"(zm[0]), "+v"(zm[1]), "+v"(zr2[0]), "+v"(zr2[1])); }
        if (sg == 17) { z2n_tile(0, gl[0]); z2n_tile(1, gl[1]); z2n_tile(2, gl[2]); }
        if (sg == 19) { const int t = wm * 64 + opq(lane0); u32x4 o;
#pragma unroll
          for (int k = 0; k < 4; ++k) o[k] = __float_as_uint((tf[4 * k] + tf[4 * k + 1]) + (tf[4 * k + 2] + tf[4 * k + 3]));
          st16<SC1>(o, rsWs, t < 180 ? t * 16 : OOB, colb + C_KVP + pi * KVP_STRIDE); }
        if (sg == 23) { vB[0] = opq(lane0) * 16; vB[1] = wm >= 2 ? OOB : vB[0]; }
        if (sg == 24) flags_dma((opq(lane0) & 15) * 4, colb + C_FLAGB);
        if (sg == 26) check_flags(colb + C_FLAGB + (lane0 & 15) * 4);
      }
    };
    auto front = [&](auto SGc) __attribute__((always_inline)) {
      constexpr int sg = decltype(SGc)::value, s = sg - ST0;
      if constexpr (H == 0) {
        if (s == 4) { const int vo = opq(lane0) * 16;
#pragma unroll
          for (int k = 0; k < 3; ++k) dma16<SC1>(rsWs, lds + L_LAND + (wm + 4 * k) * 1024, vo, csg + (wm + 4 * k) * 1024); }
        if (sg == 21) raise(colb + C_FLAGB + pi * 4);
        if (sg == 22) { vA[0] = rvoff(4096); vA[1] = vA[0] ^ 64; }
      } else {
        if (sg == 9) { const int r = wm * 64 + opq(lane0); const f32x2 a = __builtin_bit_cast(f32x2, zp[0]) + __builtin_bit_cast(f32x2, zp[1]);
          st16<SC1>((u32x4){__float_as_uint(a.x), tag, __float_as_uint(a.y), tag}, rsWs, r < 128 ? r * 16 : OOB, rowb + C_STATS + pj * 2048); }
        if (s == 6) { const int t = min(wm * 64 + opq(lane0), 143);
#pragma unroll
          for (int j = 0; j < 9; ++j) t2[j] = *(LAS u32x2*)(lds + L_LAND + j * 1152 + t * 8); }
        if (sg == 16) { gl[0] = z2_read(0); gl[1] = z2_read(1); gl[2] = z2_read(2); }
      }
    };
#pragma unroll
    for (int n = NFP; n < NF; ++n) { acc[n][0] = (f32x4){0, 0, 0, 0}; acc[n][1] = (f32x4){0, 0, 0, 0}; }
    mk_aC();
    if (PASS == 0) { mk_aR(); rdX(0, 0); rdX(0, 1); rdX(0, 2); rdX(0, 3); }
#pragma unroll
    for (int j = 0; j < 8; ++j) rdB(j >> 2, j & 3);
    if (PASS == 0) ring_all(2);
    bf16x8 ones8 = __builtin_bit_cast(bf16x8, (u32x4){ONES, ONES, ONES, ONES}); asm volatile("" : "+v"(ones8));
    const int gq = opq(lane0) >> 4;
    SB();
    sfor<0, NU>([&](auto Uc) __attribute__((always_inline)) {
      constexpr int u = decltype(Uc)::value, s = u / NFP, n = u % NFP, sg = ST0 + s, d = u - (NFP - 2);
      if (n == NFP - 2) {
        slot(IC<sg>{});
        if constexpr (H == 1) waitv<allow1(sg)>();
        else { if (sg == 1 || sg == 2) waitv<6>(); else if (sg == 5) waitv<7>(); else if (sg < 7 || sg == 26) waitv<4>(); else if (sg == 7 || sg == 15 || sg == 24) waitv<0>(); }
        if ((sg == 8 && H == 1) || (sg == 18 && H == 0)) waitl0();
        bar();
        if (H == 1 && sg == 9) { const int r = (wm * 64 + opq(lane0)) & 127; zp[0] = *(LAS u32x2*)(lds + L_ZPART + r * 8); zp[1] = *(LAS u32x2*)(lds + L_ZPART + 1024 + r * 8); }
        if (H == 1 && sg == 18) zp[0] = *(LAS u32x2*)(lds + L_TAB + min(wm * 64 + opq(lane0), 179) * 16);
        SB();
      }
      const bf16x8 b0 = cat4(bq[u % 3][0], bq[u % 3][1]), b1 = cat4(bq[u % 3][2], bq[u % 3][3]);
#pragma unroll
      for (int j = 0; j < 4; ++j) { const int it = j >> 1, hh = j & 1; const bf16x8 &bb = hh ? b1 : b0, &oa = TR ? bb : xf[s][it][hh], &ob = TR ? xf[s][it][hh] : bb;
        if (s == 0 && hh == 0) mf0(acc[n][it], oa, ob); else mfa(acc[n][it], oa, ob);
        SB();
        rdB(u + 2, j);
        if (PASS == 0 && n == NFP - 2 && s < 8) rdX(s + 1, j);
        if (j == 3) {
          if (u >= 1 && u <= NFP) { asm volatile("s_nop 0" : "+v"(ct)); if (u - 1 < 4) csv = gq == u - 1 ? ct.x : csv; else cs5 = ct.x; asm volatile("" : "+v"(csv), "+v"(cs5)); }
          if (n == NFP - 2) front(IC<sg>{});
          if (H == 1 && sg == 19 && n == 0) {
#pragma unroll
            for (int k = 0; k < 4; ++k) { LAS char* const a = lds + L_KVS + ((k & 1) ? zp[0][k >> 1] >> 16 : zp[0][k >> 1] & 0xffffu);
#pragma unroll
              for (int w = 0; w < 4; ++w) tf[4 * k + w] = *(LAS float*)(a + w * 8192); } }
          if constexpr (d >= 0) ring_pos(ST0 + d / NFP + 3, d % NFP, NFP); }
        SB(); }
      if (s == 0) { mf0(ct, ones8, b0); mfa(ct, ones8, b1); }
      if (PASS == 0 && H == 1 && n < 2 && (s == 0 || !(ABL & 2))) {
#pragma unroll
        for (int it = 0; it < 2; ++it) { f32x4& xs = n ? xs2[it] : xs1[it]; const bf16x8 &o0 = n ? xf[s][it][0] : ones8, &o1 = n ? xf[s][it][1] : ones8;
          if (s == 0) mf0(xs, xf[s][it][0], o0); else mfa(xs, xf[s][it][0], o0); mfa(xs, xf[s][it][1], o1); } }
      SB();
    });
#pragma unroll
    for (int k = 2; k < NFP; ++k) ring_pos(ST0 + 11, k, NFP);
    accfence(acc);
    relane();
  };
  auto csum1 = [&](int ps, int col) { return *(LAS float*)(lds + L_CS + ps * 576 + col * 4); };
  auto rowstat = [&](f32x2 (&nm)[4], f32x2 (&rs)[4]) {
#pragma unroll
    for (int it = 0; it < 2; ++it) { const f32x4 a = *(LAS f32x4*)(lds + L_XSTAT + (32 * wm + 16 * it + 4 * g) * 4), b = *(LAS f32x4*)(lds + L_XSTAT + 512 + (32 * wm + 16 * it + 4 * g) * 4);
      nm[2 * it] = lo2(a); nm[2 * it + 1] = hi2(a); rs[2 * it] = lo2(b); rs[2 * it + 1] = hi2(b); } };
  auto affN = [&](int n, float c, const f32x2 (&nm)[4], const f32x2 (&rs)[4], f32x2 (&v)[4]) { const f32x2 c2 = {c, c};
    v[0] = fma2(nm[0], c2, lo2(acc[n][0])) * rs[0]; v[1] = fma2(nm[1], c2, hi2(acc[n][0])) * rs[1]; v[2] = fma2(nm[2], c2, lo2(acc[n][1])) * rs[2]; v[3] = fma2(nm[3], c2, hi2(acc[n][1])) * rs[3]; };

  if constexpr (H == 1) { if (!(ABL & 4)) __builtin_amdgcn_s_setprio(1); waitv<5>(); } else waitv<4>();
  bar();
  STAMP(1);
  pass(IC<0>{});
  STAMP(2);
  if constexpr (H == 0) {
    dma4(mkrs(P.wsp, S * S * 2), lds + L_DUMMY, lane * 128, (blockIdx.x * 4 + wm) * 8192);
    if (wm == 0) dma4(mkrs(P.bsp, S * 2), lds + L_BIAS, lane * 4, pi * 256);
  }
  {
    f32x2 nm[4], rs[4]; rowstat(nm, rs);
    LAS char* const sw = lds + L_STASH + (wm * 9 + NB) * 1024 + fr * 64 + (((g >> 1) ^ ((fr >> 2) & 1)) * 32) + (g & 1) * 16;
    LAS char* const sr = lds + L_STASH + (wm * 9 + NB) * 1024 + (4 * g + (fr >> 2)) * 64 + (g & 1) * 32 + (fr & 3) * 8;
    LAS char* const sr1 = lds + L_STASH + (wm * 9 + NB) * 1024 + (4 * g + (fr >> 2)) * 64 + ((g & 1) ^ 1) * 32 + (fr & 3) * 8;
    float cn[NF];
#pragma unroll
    for (int n = 0; n < NF; ++n) cn[n] = csum1(0, (NB + n) * 16 + fr);
#pragma unroll
    for (int n = 0; n < NF; ++n) { f32x2 v[4]; affN(n, cn[n], nm, rs, v);
      *(LAS u32x4*)(sw + n * 1024) = (u32x4){pk2v(gelu2(v[0])), pk2v(gelu2(v[1])), pk2v(gelu2(v[2])), pk2v(gelu2(v[3]))}; }
    asm volatile("" ::: "memory");
    float s1[2] = {0.f, 0.f}, s2[2] = {0.f, 0.f};
#pragma unroll
    for (int n = 0; n < NF; ++n)
#pragma unroll
      for (int ih = 0; ih < 2; ++ih) { const u32x2 t = __builtin_bit_cast(u32x2, __builtin_amdgcn_ds_read_tr16_b64_v4i16((LAS s16x4*)((ih ? sr1 : sr) + n * 1024)));
        s1[ih] = dot2(t.x, ONES, s1[ih]); s2[ih] = dot2(t.x, t.x, s2[ih]); s1[ih] = dot2(t.y, ONES, s1[ih]); s2[ih] = dot2(t.y, t.y, s2[ih]); }
    const float a0 = gsum(s1[0]), b0 = gsum(s2[0]), a1 = gsum(s1[1]), b1 = gsum(s2[1]);
    if (g < 2) *(LAS f32x2*)(lds + L_ZPART + (H * 128 + 32 * wm + 16 * g + fr) * 8) = g ? (f32x2){a1, b1} : (f32x2){a0, b0};
    waitl0();
  }
  STAMP(3);
  pass(IC<1>{});
  STAMP(4);
  u32x2 pq[3][2];
  f32x4 aq[3][2];
  if constexpr (H == 0) {
        { const int l = opq(lane0), li = (wm & 1) * 64 + l;
          if (wm < 2) dma4(rsWout, lds + L_DUMMY, li < 81 ? li * 128 : OOB, blockIdx.x * 81 * 128);
          else if (wm == 2) dma4(rsWo, lds + L_DUMMY, l < 21 ? l * 128 : OOB, blockIdx.x * 21 * 128);
          codewarm(); }
    f32x2 nm[4], rs[4]; rowstat(nm, rs);
    float cn[5];
#pragma unroll
    for (int n = 0; n < 5; ++n) cn[n] = csum1(1, n * 16 + fr);
    *(LAS u32x4*)(lds + L_KVF + (wm * 64 + lane) * 16) = (u32x4){0, 0, 0, 0};
    *(LAS u32x2*)(lds + L_KVF + 4096 + (wm * 64 + lane) * 8) = (u32x2){0, 0};
    bf16x8 pkf[3], vf[3];
#pragma unroll
    for (int n = 0; n < 5; ++n) { f32x2 v[4]; affN(n, cn[n], nm, rs, v);
      if (n >= 2) { u32x4 w = {pk2v(v[0]), pk2v(v[1]), pk2v(v[2]), pk2v(v[3])};
        if (n == 4 && fr == 0) w = (u32x4){ONES, ONES, ONES, ONES};
        vf[n - 2] = __builtin_bit_cast(bf16x8, w); }
      if (n < 2 || n == 4) pkf[n == 4 ? 2 : n] = __builtin_bit_cast(bf16x8, (u32x4){pk2v(phi2(v[0])), pk2v(phi2(v[1])), pk2v(phi2(v[2])), pk2v(phi2(v[3]))});
    }
#pragma unroll
    for (int td = 0; td < 3; ++td)
#pragma unroll
      for (int te = 0; te < 3; ++te) { if (td == 2 && te == 0) continue;
        *(LAS f32x4*)(lds + L_KVS + (wm * 8 + td * 3 + te - (td == 2)) * 1024 + lane * 16) = mfma(pkf[td], vf[te], (f32x4){0, 0, 0, 0}); }
  } else {
    u32x4 z[6];
#pragma unroll
    for (int n = 0; n < 6; ++n) z[n] = z2_read(3 + n);
#pragma unroll
    for (int n = 0; n < 6; ++n) z2n_tile(3 + n, z[n]);
#pragma unroll
    for (int n = 0; n < 3; ++n) { aq[n][0] = acc[n][0]; aq[n][1] = acc[n][1]; }
  }
  STAMP(6);
  pass(IC<2>{});
  STAMP(7);
  u32x2 z1[NF][2];
  f32x4 ag[NF][2];
  {
    constexpr int NR = NF + 2;
    LAS char* const aZ = lds + NB * 1024 + lane * 16;
    mk_aR();
    if constexpr (H == 0) vB[0] = lane * 16;
    f32x4 kvs = {0, 0, 0, 0}; u32x4 kl[2][2]; int kvo;
    if constexpr (H == 0) { const int t = wm * 64 + lane; kvo = t < 180 ? t * 16 : OOB; }
    bf16x8 fa[2][NR], fb[2][NR];
    auto rd1 = [&](bf16x8 (&f)[2][NR], int i, int slot) {
      const int h = i & 1; int ii = i >> 1; if (ii == 1) ii = 2; else if (ii == 2) ii = 1;
      if (ii < 2) f[h][ii] = *(LAS bf16x8*)((h ? aR1 : aR0) + slot * R_SZ + ii * 2048); else f[h][ii] = *(LAS bf16x8*)(aZ + slot * C_SZ + (h * 9 + ii - 2) * 1024);
    };
    float zn[2], zr[2], za, zb, ta, tb; f32x2 zc;
    LAS char* const aCs = lds + L_CS + 576 + (NB * 16 + 4 * g) * 4;
#pragma unroll
    for (int it = 0; it < 2; ++it) { zn[it] = *(LAS float*)(lds + L_XSTAT + (32 * wm + 16 * it + fr) * 4); zr[it] = *(LAS float*)(lds + L_XSTAT + 512 + (32 * wm + 16 * it + fr) * 4); }
    float GC1 = 2.3022082f; asm volatile("" : "+v"(GC1));
    auto zop = [&](int e, int k) { const int n = e >> 2, it = (e >> 1) & 1, h = e & 1;
      if (k == 0) zc = *(LAS f32x2*)(aCs + 576 + n * 64 + h * 8);
      if (k == 3) asm volatile("v_fma_f32 %0, %2, %4, %5\n\tv_fma_f32 %1, %3, %4, %6" : "=&v"(za), "=&v"(zb) : "v"(zc.x), "v"(zc.y), "v"(zn[it]), "v"(acc[n][it][2 * h]), "v"(acc[n][it][2 * h + 1]));
      if (k == 4) asm volatile("v_mul_f32 %0, %0, %2\n\tv_mul_f32 %1, %1, %2" : "+v"(za), "+v"(zb) : "v"(zr[it]));
      if (k == 5) asm volatile("v_mul_f32 %0, %2, %2\n\tv_mul_f32 %1, %3, %3" : "=&v"(ta), "=&v"(tb) : "v"(za), "v"(zb));
      if (k == 6) asm volatile("v_fmamk_f32 %0, %0, 0x3dd2d3e8, %2\n\tv_fmamk_f32 %1, %1, 0x3dd2d3e8, %2" : "+v"(ta), "+v"(tb) : "v"(GC1));
      if (k == 7) asm volatile("v_mul_f32 %0, %0, %2\n\tv_mul_f32 %1, %1, %3" : "+v"(ta), "+v"(tb) : "v"(za), "v"(zb));
      if (k == 8) asm volatile("v_exp_f32 %0, %0" : "+v"(ta));
      if (k == 9) asm volatile("v_exp_f32 %0, %0" : "+v"(tb));
      if (k == 10) asm volatile("v_add_f32 %0, 1.0, %0\n\tv_add_f32 %1, 1.0, %1" : "+v"(ta), "+v"(tb));
      if (k == 11) asm volatile("v_rcp_f32 %0, %0" : "+v"(ta));
      if (k == 12) asm volatile("v_rcp_f32 %0, %0" : "+v"(tb));
      if (k == 13) asm volatile("v_fma_f32 %0, -%0, %2, %0\n\tv_fma_f32 %1, -%1, %3, %1" : "+v"(za), "+v"(zb) : "v"(ta), "v"(tb));
      if (k == 14) { unsigned o; asm volatile("v_cvt_pk_bf16_f32 %0, %1, %2" : "=v"(o) : "v"(za), "v"(zb)); z1[n][it][h] = o; } };
    auto qop = [&](int e, int k) { const int n = e >> 2, it = (e >> 1) & 1, h = e & 1;
      if (k == 0) zc = *(LAS f32x2*)(aCs + n * 64 + h * 8);
      if (k == 3) asm volatile("v_fma_f32 %0, %2, %4, %5\n\tv_fma_f32 %1, %3, %4, %6" : "=&v"(za), "=&v"(zb) : "v"(zc.x), "v"(zc.y), "v"(zn[it]), "v"(aq[n][it][2 * h]), "v"(aq[n][it][2 * h + 1]));
      if (k == 4) asm volatile("v_mul_f32 %0, %0, %2\n\tv_mul_f32 %1, %1, %2" : "+v"(za), "+v"(zb) : "v"(zr[it]));
      if (k == 5) asm volatile("v_min_f32 %0, 0, %2\n\tv_min_f32 %1, 0, %3" : "=&v"(ta), "=&v"(tb) : "v"(za), "v"(zb));
      if (k == 6) asm volatile("v_mul_f32 %0, 0x3fb8aa3b, %0\n\tv_mul_f32 %1, 0x3fb8aa3b, %1" : "+v"(ta), "+v"(tb));
      if (k == 7) asm volatile("v_exp_f32 %0, %0" : "+v"(ta));
      if (k == 8) asm volatile("v_exp_f32 %0, %0" : "+v"(tb));
      if (k == 9) asm volatile("v_max_f32 %0, 0, %0\n\tv_max_f32 %1, 0, %1" : "+v"(za), "+v"(zb));
      if (k == 10) asm volatile("v_add_f32 %0, %0, %2\n\tv_add_f32 %1, %1, %3" : "+v"(za), "+v"(zb) : "v"(ta), "v"(tb));
      if (k == 11) asm volatile("v_add_f32 %0, 0x358637bd, %0\n\tv_add_f32 %1, 0x358637bd, %1" : "+v"(za), "+v"(zb));
      if (k == 12) { unsigned o; asm volatile("v_cvt_pk_bf16_f32 %0, %1, %2" : "=v"(o) : "v"(za), "v"(zb)); pq[n][it][h] = o; } };
    auto kvscatter = [&]() { const int t = wm * 64 + opq(lane0); const u32x2 tb = *(LAS u32x2*)(lds + L_TAB + min(t, 179) * 16 + 8);
      const unsigned k01 = pk2v(lo2(kvs)), k23 = pk2v(hi2(kvs)), msk = t < 180 ? 0xffffu : 0u; LAS char* const a = lds + (t < 180 ? L_KVF : L_DUMMY);
      *(LAS u16*)(a + (tb.x & msk)) = (u16)k01; *(LAS u16*)(a + ((tb.x >> 16) & msk)) = (u16)(k01 >> 16); *(LAS u16*)(a + (tb.y & msk)) = (u16)k23; *(LAS u16*)(a + ((tb.y >> 16) & msk)) = (u16)(k23 >> 16); };
    auto ostep = [&](int it) {
      const int l = opq(lane0), r = l & 15, gg = l >> 4; bf16x8 kf[3][2];
#pragma unroll
      for (int en = 0; en < 3; ++en) { kf[en][0] = *(LAS bf16x8*)(lds + L_KVF + (en * 2) * 1024 + l * 16); kf[en][1] = *(LAS bf16x8*)(lds + L_KVF + (en * 2 + 1) * 1024 + l * 16); }
      { const bf16x8 b0 = cat(pq[0][it], pq[1][it]), b1 = cat(pq[2][it], (u32x2){0, 0}); f32x4 nm[3];
#pragma unroll
        for (int en = 0; en < 3; ++en) nm[en] = mfma(kf[en][1], b1, mfma(kf[en][0], b0, (f32x4){0, 0, 0, 0}));
        const float rA = __builtin_amdgcn_rcpf(__shfl(nm[2][0], 16 + r) + 1e-6f), rB = __builtin_amdgcn_rcpf(__shfl(nm[2][1], 16 + r) + 1e-6f);
        const float rM = gg == 0 ? rA : rB;
#pragma unroll
        for (int en = 0; en < 3; ++en) { const float r01 = en == 0 ? rA : en == 1 ? rM : rB, r23 = en == 0 ? rA : rB;
          const u32x2 o = {pk2v(lo2(nm[en]) * r01), pk2v(hi2(nm[en]) * r23)};
          const int oc4 = 9 * gu + 4 * en + gg;
          int vo = (oc4 >> 3) * 8192 + ((oc4 >> 2) & 1) * 512 + ((oc4 & 3) * 16 + r) * 8;
          if (en == 2) vo = gg != 0 ? OOB : vo;
          __builtin_amdgcn_raw_buffer_store_b64(o, rsWs, vo, yb + (2 * wm + it) * 1024, SC1); } } };
    auto step = [&](bf16x8 (&cur)[2][NR], bf16x8 (&nxt)[2][NR], auto Pc) {
      constexpr int p = decltype(Pc)::value, st = 30 + p, e = p - (H ? 12 : 1);
      auto vop = [&](int k) { if (H == 1 && p < 12) qop(p, k); else if (e >= 0 && e < 4 * NF) zop(e, k); };
      if constexpr (H == 1) { if (p == 14 || p == 15) ostep(p - 14); if (p >= 20 && p < 25) cvoff(vA, 2, p - 20, p - 19); waitv<allow1(27 + p)>(); }
      else if (H == 0 && p >= 2 && p <= 9) { if (p <= 8) asm volatile("s_waitcnt vmcnt(6) ; HIDDEN_WAIT %0 %1" : "+v"(kl[p & 1][0]), "+v"(kl[p & 1][1]) :: "memory");
                                             else asm volatile("s_waitcnt vmcnt(4) ; HIDDEN_WAIT %0 %1" : "+v"(kl[p & 1][0]), "+v"(kl[p & 1][1]) :: "memory"); }
      else if (H == 0 && p == 1) waitv<6>();
      else if (H == 0 && (p == 18 || p == 20)) waitv<5>();
      else waitv<4>();
      if (p == 31) waitl0();
      bar();
      SB();
      if constexpr (H == 0) {
        if (p >= 2 && p <= 9) { kvs += __builtin_bit_cast(f32x4, kl[p & 1][0]) + __builtin_bit_cast(f32x4, kl[p & 1][1]); asm volatile("" : "+v"(kvs)); }
        if (p < 8) { HLOAD(kl[p & 1][0], kvo, rsWs, colb + C_KVP + 2 * p * KVP_STRIDE); HLOAD(kl[p & 1][1], kvo, rsWs, colb + C_KVP + (2 * p + 1) * KVP_STRIDE); }
        if (p == 10) kvscatter();
        if (p == 17) raise(rowb + C_FLAGO + pj * 4);
        if (p == 19) raise(W_EPOCH, blockIdx.x == 0);
        if (p == 22) codewarm();
        if (p == 27) flags_dma((opq(lane0) & 15) * 4, rowb + C_FLAGO);
        if (p == 28) check_flags(rowb + C_FLAGO + (lane0 & 15) * 4);
      }
      ring(st, 4, p + 3);
      SB();
      vop(0);
#pragma unroll
      for (int i = 0; i < 4 * NF; ++i) {
        const int n = i >> 2, m = (i >> 1) & 1, h = i & 1;
        if (p == 0 && h == 0) mf0(ag[n][m], cur[0][2 + n], cur[0][m]); else mfa(ag[n][m], cur[h][2 + n], cur[h][m]);
        SB();
        if (p != 31 && i < 2 * NR) rd1(nxt, i, p & 3);
        if (i > 0) vop(i);
        if ((i & 3) == 3 && i < 16) ring(st, i >> 2, p + 3);
        SB();
      }
    };
#pragma unroll
    for (int i = 0; i < 2 * NR; ++i) rd1(fa, i, 3);
    STAMP(9);
    sfor<0, 16>([&](auto Kc) __attribute__((always_inline)) { constexpr int k = decltype(Kc)::value; step(fa, fb, IC<2 * k>{}); step(fb, fa, IC<2 * k + 1>{}); if (k == 5) STAMP(8); });
    accfence(ag);
  }
  STAMP(10);
  relane();
  {
    u32x2 y[NF][2];
#pragma unroll
    for (int it = 0; it < 2; ++it) { const float b = blo(*(LAS u16*)(lds + L_BIAS + (32 * wm + 8 * (fr >> 2) + 4 * it + (fr & 3)) * 2));
#pragma unroll
      for (int n = 0; n < NF; ++n) y[n][it] = (u32x2){pk2v(unpk(z1[n][it].x) * (lo2(ag[n][it]) + b)), pk2v(unpk(z1[n][it].y) * (hi2(ag[n][it]) + b))}; }
    waitv<0>();
#pragma unroll
    for (int hh = 0; hh < 2; ++hh)
#pragma unroll
      for (int it = 0; it < 2; ++it) { const u32x4 v = __builtin_bit_cast(u32x4, cat(y[2 * hh][it], y[2 * hh + 1][it]));
        st16<SC1>(v, rsWs, lane * 16, yb + (4 + 2 * gu + H) * 16384 + hh * 8192 + (2 * wm + it) * 1024);
        *(LAS u32x4*)(lds + (H ? L_YOWN : R_OFF + 2 * R_SZ) + hh * 8192 + (2 * wm + it) * 1024 + lane * 16) = v; }
    if constexpr (H == 0) {
#pragma unroll
      for (int it = 0; it < 2; ++it) __builtin_amdgcn_raw_buffer_store_b64(y[4][it], rsWs, lane * 8, yb + 3 * 16384 + (gu >> 1) * 8192 + (2 * wm + it) * 1024 + (gu & 1) * 512, SC1);
    }
  }
  STAMP(11);
  {
    LAS char* const aY = lds + R_OFF + 2 * wm * 1024;
    bf16x8 rf[2][2][2]; s16x4 bq[3][4];
    mk_aC();
    auto rdB = [&](int u, int j) { const int q = u / NF, n = u % NF; if (u < 12 * NF && (q != 2 || j < 2)) bq[u % 3][j] = tr1(((j & 2) ? aC1 : aC0) + ((59 + q) & 3) * C_SZ + n * 32 + (j & 1) * (8 * 288)); };
    auto rdY = [&](int q, int j) { const int it = j >> 1, hh = j & 1; LAS char* a = aY + (q == 4 ? L_YOWN - R_OFF : ((59 + q) & 3) * R_SZ) + hh * 8192 + it * 1024;
      if (q == 2) { if (hh == 0) rf[q & 1][it][0] = cat(*(LAS u32x2*)(a + lane * 8), (u32x2){0, 0}); }
      else if (q < 2 || q == 11) rf[q & 1][it][hh] = cat(*(LAS u32x2*)(a + lane * 8), *(LAS u32x2*)(a + 512 + lane * 8));
      else rf[q & 1][it][hh] = *(LAS bf16x8*)(a + lane * 16); };
    rdY(0, 0); rdY(0, 1); rdY(0, 2); rdY(0, 3);
#pragma unroll
    for (int j = 0; j < 8; ++j) rdB(j >> 2, j & 3);
    SB();
    sfor<0, 12 * NF>([&](auto Uc) __attribute__((always_inline)) {
      constexpr int u = decltype(Uc)::value, q = u / NF, n = u % NF;
      if (n == NF - 2 && q < 11) {
        if constexpr (H == 1) { if (q == 10) waitv<0>(); else if (q >= 1) waitv<5>(); }
        else { if (q == 1 || q == 3 || q == 10) waitv<0>(); else if (q >= 4) waitv<4>(); }
        bar();
        if constexpr (H == 0) {
          if (q == 1) raise(rowb + C_FLAGD + gc * 128 + gu * 4);
          if (q == 2) flags_dma((opq(lane0) & 3) * 4, rowb + C_FLAGD + gc * 128);
          if (q == 3) { STAMP(12); check_flags(rowb + C_FLAGD + gc * 128 + (lane0 & 3) * 4); STAMP(13); ring_all(64); }
          if (q == 4) STAMP(14);
        }
        SB();
      }
      const bf16x8 b0 = cat4(bq[u % 3][0], bq[u % 3][1]), b1 = q == 2 ? b0 : cat4(bq[u % 3][2], bq[u % 3][3]);
#pragma unroll
      for (int j = 0; j < 4; ++j) { const int it = j >> 1, hh = j & 1;
        if (q == 0 && hh == 0) mf0(acc[n][it], b0, rf[q & 1][it][hh]); else if (q != 2 || hh == 0) mfa(acc[n][it], hh ? b1 : b0, rf[q & 1][it][hh]);
        SB();
        rdB(u + 2, j);
        if (n == NF - 2 && q < 11) rdY(q + 1, j);
        if (j == 3) { const int d = u - (NF - 2); if (d >= 0) { const int qq = d / NF + 3; if (qq < 12 && (H == 1 || qq >= 6)) ring_pos(59 + qq, d % NF, NF); } }
        SB(); }
    });
    accfence(acc);
  }
  STAMP(15);
  relane();
  {
    const rsrc_t rsOut = mkrs(P.out + (size_t)pi * 128 * D, 128 * D * 2);
    auto packp = [&](const f32x4 (&a)[2]) { u32x4 v = {pk2v(lo2(a[0])), pk2v(hi2(a[0])), pk2v(lo2(a[1])), pk2v(hi2(a[1]))};
      v.x = min(v.x, 0xfffffffeu); v.y = min(v.y, 0xfffffffeu); v.z = min(v.z, 0xfffffffeu); v.w = min(v.w, 0xfffffffeu); return v; };
    constexpr int NK = H ? 1 : 2;
    f32x4 kp[NK][2]; int cb[NK];
#pragma unroll
    for (int it = 0; it < 2; ++it) { kp[0][it] = gc == 0 ? acc[0][it] : gc == 1 ? acc[1][it] : gc == 2 ? acc[2][it] : acc[3][it]; if constexpr (H == 0) kp[1][it] = acc[4][it]; }
    cb[0] = 256 * gu + 32 * (H * 4 + gc); if constexpr (H == 0) cb[1] = 1024 + 32 * gu;
    const int off1 = wm == gc ? 0 : OOB;
    int ro[2]; u32x2 xr[NK][2];
#pragma unroll
    for (int it = 0; it < 2; ++it) { ro[it] = (32 * wm + 8 * (fr >> 2) + 4 * it + (fr & 3)) * 1152 + 8 * g;
#pragma unroll
      for (int k = 0; k < NK; ++k) xr[k][it] = __builtin_amdgcn_raw_buffer_load_b64(rsX, ro[it] + (k ? off1 : 0), cb[k], 0); }
#pragma unroll
    for (int n = 0; n < NF; ++n) { const int dg = n < 4 ? n : wm, slot = n < 4 ? H * 4 + wm : 8;
      st16<SC1>(packp(acc[n]), rsWs, dg == gc ? OOB : lane * 16, rowb + C_PART + ((4 * dg + gu) * 4 + gc) * PART_SRC + slot * 1024); }
    const int pb = rowb + C_PART + pj * 4 * PART_SRC;
    u32x4 v[NK][3];
    __builtin_amdgcn_s_sleep(FIN_SLEEP);
    _Pragma("unroll 1") for (unsigned sp = 0;; ++sp) {
      unsigned mx = 0;
#pragma unroll
      for (int k = 0; k < NK; ++k)
#pragma unroll
        for (int j = 0; j < 3; ++j) { const int so = pb + ((gc + 1 + j) & 3) * PART_SRC + (k ? 8 : H * 4 + wm) * 1024;
          v[k][j] = __builtin_amdgcn_raw_buffer_load_b128(rsWs, lane * 16 + (k ? off1 : 0), so, SC1);
          mx = max(max(mx, max(v[k][j].x, v[k][j].y)), max(v[k][j].z, v[k][j].w)); }
      if (LIKELY(__all(mx != ~0u))) break;
      if (sp > SPIN_MAX) { set_flag(P.ws, W_TMO, 1u); break; }
      __builtin_amdgcn_s_sleep(FIN_POLL);
    }
#pragma unroll
    for (int k = 0; k < NK; ++k)
#pragma unroll
      for (int it = 0; it < 2; ++it) { f32x2 a = lo2(kp[k][it]) + unpk(xr[k][it].x), b = hi2(kp[k][it]) + unpk(xr[k][it].y);
#pragma unroll
        for (int j = 0; j < 3; ++j) { a += unpk(v[k][j][2 * it]); b += unpk(v[k][j][2 * it + 1]); }
        __builtin_amdgcn_raw_buffer_store_b64((u32x2){pk2v(a), pk2v(b)}, rsOut, ro[it] + (k ? off1 : 0), cb[k], 0); }
  }
  if constexpr (H == 0) asm volatile(".Lmk_end:");
}

__global__ __launch_bounds__(512, 2) void fused(const u16* x, const u16* wqkv, const u16* wo, const u16* win, const u16* wsp, const u16* bsp, const u16* wout, u16* out, char* ws) {
  extern __shared__ __attribute__((aligned(16))) char smem[];
  LAS char* lds = (LAS char*)smem;
  const int lane = threadIdx.x & 63, wid = __builtin_amdgcn_readfirstlane(threadIdx.x >> 6);
  const int b = blockIdx.x, xc = b & 7, q = b >> 3;
  const int pi = 8 * (xc & 1) + (q & 7), pj = 4 * (xc >> 1) + (q >> 3);
  const unsigned epoch = *(const __attribute__((address_space(4))) unsigned*)(ws + W_EPOCH);
  const Ptrs P{x, wqkv, wo, win, wsp, bsp, wout, out, ws};
  unsigned long long pc; int clen;
  asm volatile("s_getpc_b64 %0\n.Lmk_pc:\n\ts_mov_b32 %1, .Lmk_end-.Lmk_pc" : "=s"(pc), "=s"(clen));
  const rsrc_t rsCode = mkrs((const void*)pc, clen);
  if (wid < 4) body<0>(P, lds, wid, lane, pi, pj, epoch, rsCode); else body<1>(P, lds, wid - 4, lane, pi, pj, epoch, rsCode);
}
}

extern "C" void kernel_launch(void* const* d_in, const int* in_sizes, int n_in, void* d_out, int out_size, void* d_ws, size_t ws_size, hipStream_t stream) {
  using mk::u16;
  (void)hipFuncSetAttribute((const void*)mk::fused, hipFuncAttributeMaxDynamicSharedMemorySize, mk::LDS_TOTAL);
  mk::fused<<<256, 512, mk::LDS_TOTAL, stream>>>((const u16*)d_in[0], (const u16*)d_in[1], (const u16*)d_in[2], (const u16*)d_in[4], (const u16*)d_in[5], (const u16*)d_in[6],
                                                 (const u16*)d_in[7], (u16*)d_out, (char*)d_ws);
}
```

```cpp
#include <hip/hip_runtime.h>
#include <hip/hip_bf16.h>
#include <type_traits>

namespace mk {
typedef unsigned short u16;
typedef short bf16x8 __attribute__((ext_vector_type(8)));
typedef short s16x4 __attribute__((ext_vector_type(4)));
typedef float f32x4 __attribute__((ext_vector_type(4)));
typedef float f32x2 __attribute__((ext_vector_type(2)));
typedef unsigned u32x4 __attribute__((ext_vector_type(4)));
typedef unsigned u32x2 __attribute__((ext_vector_type(2)));
typedef __bf16 bf16x2_t __attribute__((ext_vector_type(2)));
#define LAS __attribute__((address_space(3)))
#define GAS __attribute__((address_space(1)))
#define DI __device__ __forceinline__
template <int V> using IC = std::integral_constant<int, V>;
template <int B, int E, class F> __device__ __forceinline__ void sfor(F&& f) { if constexpr (B < E) { f(IC<B>{}); sfor<B + 1, E>(f); } }

constexpr int S = 2048, D = 576, FH = 2304;
constexpr int C_SZ = 18432, R_OFF = 73728, R_SZ = 16384, MISC = 139264;
constexpr int L_STASH = R_OFF, L_KVS = R_OFF + 36864;
constexpr int L_ZPART = MISC + 4096, L_KVF = L_ZPART + 2048, L_XSTAT = L_KVF + 6144, L_CS = L_XSTAT + 1024, L_TAB = L_CS + 1792;
constexpr int L_LAND0 = MISC;
constexpr int L_BIAS = L_TAB + 3072, L_FLG = L_BIAS + 256, L_DUMMY = L_FLG + 1024, L_STAMP = L_DUMMY + 1024;
constexpr int L_YOWN = MISC;
static_assert(L_YOWN + 16384 <= L_BIAS, "lds");
constexpr int LDS_TOTAL = 163840;
static_assert(L_STAMP + 512 <= LDS_TOTAL, "lds");
constexpr int W_EPOCH = 0, W_TMO = 256, W_STAMP = 32768, W_COL = 2 << 20, W_ROW = 18 << 20, W_END = 50 << 20;
constexpr int COL_SZ = 1 << 20, C_ZF = 0, C_KVP = 0x90000, C_CSG = 0xA0000, C_FLAGB = 0xB0000;
constexpr int ROW_SZ = 2 << 20, C_YF = 0, C_PART = 0xC0000, C_STATS = 0x150000, C_FLAGO = 0x158000, C_FLAGD = 0x158100;
constexpr int KVP_STRIDE = 2944, YF_GRP = 12 * 16384, PART_SRC = 9216, OOB = 0x40000000;
constexpr int SC1 = 16;
constexpr unsigned SPIN_MAX = 1u << 16;
#define FIN_SLEEP 16
#define FIN_POLL 4
#define ABL 0
constexpr int vpre1(int sg) { return sg == 10 ? 1 : 0; }
constexpr int vpost1(int sg) { return sg == 1 ? 2 : (sg >= 2 && sg <= 7) || sg == 10 || sg == 24 ? 1 : sg == 12 || sg == 13 ? 4 : sg == 17 ? 3 : sg == 18 ? 6 : sg == 19 ? 2 : sg == 27 + 14 || sg == 27 + 15 ? 3 : 0; }
constexpr int allow1(int sg) { return 5 + (sg == 25 ? 0 : vpost1(sg - 1)) + vpre1(sg) + vpost1(sg); }

template <int N> DI void waitv() { asm volatile("s_waitcnt vmcnt(%0)" ::"n"(N) : "memory"); }
DI void waitl0() { __builtin_amdgcn_s_waitcnt(0xC07F); asm volatile("" ::: "memory"); }
DI void bar() { asm volatile("" ::: "memory"); __builtin_amdgcn_s_barrier(); asm volatile("" ::: "memory"); }
#define SB() __builtin_amdgcn_sched_barrier(0)
DI f32x4 mfma(bf16x8 a, bf16x8 b, f32x4 c) { return __builtin_amdgcn_mfma_f32_16x16x32_bf16(a, b, c, 0, 0, 0); }
DI void mfa(f32x4& c, const bf16x8& a, const bf16x8& b) { asm volatile("v_mfma_f32_16x16x32_bf16 %0, %1, %2, %0" : "+v"(c) : "v"(a), "v"(b)); }
DI void mf0(f32x4& c, const bf16x8& a, const bf16x8& b) { asm volatile("v_mfma_f32_16x16x32_bf16 %0, %1, %2, 0" : "=&v"(c) : "v"(a), "v"(b)); }
DI int opq(int v) { asm volatile("" : "+v"(v)); return v; }
template <int N> DI void accfence(f32x4 (&a)[N][2]) {
  if constexpr (N == 5) asm volatile("s_nop 7\n\ts_nop 1" : "+v"(a[0][0]), "+v"(a[0][1]), "+v"(a[1][0]), "+v"(a[1][1]), "+v"(a[2][0]), "+v"(a[2][1]), "+v"(a[3][0]), "+v"(a[3][1]), "+v"(a[4][0]), "+v"(a[4][1]));
  else asm volatile("s_nop 7\n\ts_nop 1" : "+v"(a[0][0]), "+v"(a[0][1]), "+v"(a[1][0]), "+v"(a[1][1]), "+v"(a[2][0]), "+v"(a[2][1]), "+v"(a[3][0]), "+v"(a[3][1]));
}
DI unsigned pk2(float lo, float hi) { f32x2 v = {lo, hi}; return __builtin_bit_cast(unsigned, __builtin_convertvector(v, bf16x2_t)); }
DI float blo(unsigned u) { return __uint_as_float(u << 16); }
DI float dot2(unsigned a, unsigned b, float c) { return __builtin_amdgcn_fdot2_f32_bf16(__builtin_bit_cast(bf16x2_t, a), __builtin_bit_cast(bf16x2_t, b), c, false); }
DI f32x2 gelu2(f32x2 v) {
  const f32x2 a = v * __builtin_elementwise_fma(v * v, (f32x2){0.10294324f, 0.10294324f}, (f32x2){2.3022082f, 2.3022082f});
  const f32x2 d = (f32x2){__builtin_amdgcn_exp2f(a.x), __builtin_amdgcn_exp2f(a.y)} + 1.0f;
  const f32x2 r = {__builtin_amdgcn_rcpf(d.x), __builtin_amdgcn_rcpf(d.y)};
  return __builtin_elementwise_fma(-v, r, v);
}
DI unsigned pk2v(f32x2 v) { return __builtin_bit_cast(unsigned, __builtin_convertvector(v, bf16x2_t)); }
DI f32x2 unpk(unsigned u) { return (f32x2){__uint_as_float(u << 16), __uint_as_float(u & 0xffff0000u)}; }
DI f32x2 lo2(f32x4 a) { return (f32x2){a.x, a.y}; }
DI f32x2 hi2(f32x4 a) { return (f32x2){a.z, a.w}; }
DI f32x2 fma2(f32x2 a, f32x2 b, f32x2 c) { return __builtin_elementwise_fma(a, b, c); }
DI f32x2 phi2(f32x2 t) {
  const f32x2 a = t * 1.44269504f, p = t + 1.0f;
  const f32x2 r = {t.x > 0.f ? p.x : __builtin_amdgcn_exp2f(a.x), t.y > 0.f ? p.y : __builtin_amdgcn_exp2f(a.y)};
  return r + 1e-6f;
}
DI float hsum32(float v) { const unsigned u = __float_as_uint(v); const auto r = __builtin_amdgcn_permlane32_swap(u, u, false, false); return __uint_as_float(r[0]) + __uint_as_float(r[1]); }
DI float hsum16(float v) { const unsigned u = __float_as_uint(v); const auto r = __builtin_amdgcn_permlane16_swap(u, u, false, false); return __uint_as_float(r[0]) + __uint_as_float(r[1]); }
template <int N> DI float bcast16(float v) { return __builtin_bit_cast(float, __builtin_amdgcn_update_dpp(0, __builtin_bit_cast(int, v), 0x150 + N, 0xf, 0xf, true)); }
DI float ror8sum(float v) { return v + __builtin_bit_cast(float, __builtin_amdgcn_update_dpp(0, __builtin_bit_cast(int, v), 0x128, 0xf, 0xf, true)); }
DI float gsum(float v) { return hsum32(hsum16(v)); }
typedef __amdgpu_buffer_rsrc_t rsrc_t;
DI rsrc_t mkrs(const void* p, int bytes) { return __builtin_amdgcn_make_buffer_rsrc((void*)p, 0, bytes, 0x00020000); }
template <int AUX> DI void dma16(rsrc_t rs, LAS char* dst, int voff, int soff) {
  const unsigned m = (unsigned)(size_t)dst;
  if constexpr (AUX == 0) asm volatile("s_mov_b32 m0, %0\n\ts_nop 0\n\tbuffer_load_dwordx4 %1, %2, %3 offen lds" ::"s"(m), "v"(voff), "s"(rs), "s"(soff) : "memory");
  else asm volatile("s_mov_b32 m0, %0\n\ts_nop 0\n\tbuffer_load_dwordx4 %1, %2, %3 offen sc1 lds" ::"s"(m), "v"(voff), "s"(rs), "s"(soff) : "memory");
}
DI void dma4(rsrc_t rs, LAS char* dst, int voff, int soff) {
  const unsigned m = (unsigned)(size_t)dst;
  asm volatile("s_mov_b32 m0, %0\n\ts_nop 3\n\tbuffer_load_dword %1, %2, %3 offen sc1 lds" ::"s"(m), "v"(voff), "s"(rs), "s"(soff) : "memory");
}
template <int AUX> DI void st16(u32x4 v, rsrc_t rs, int voff, int soff) { asm volatile("" : "+v"(v)); __builtin_amdgcn_raw_buffer_store_b128(v, rs, voff, soff, AUX); asm volatile("s_nop 1" ::"v"(v) : "memory"); }
#define LIKELY(x) __builtin_expect(!!(x), 1)
#define UNLIKELY(x) __builtin_expect(!!(x), 0)
#define HLOAD(dst, voff, rs, soff) asm volatile("buffer_load_dwordx4 %0, %1, %2, %3 offen sc1 ; HIDDEN_LOAD" : "=v"(dst) : "v"(voff), "s"(rs), "s"(soff) : "memory")
DI s16x4 tr1(LAS char* a) { return __builtin_amdgcn_ds_read_tr16_b64_v4i16((LAS s16x4*)a); }
DI bf16x8 cat4(s16x4 a, s16x4 b) { return __builtin_shufflevector(a, b, 0, 1, 2, 3, 4, 5, 6, 7); }
DI bf16x8 cat(u32x2 a, u32x2 b) { u32x4 v = {a.x, a.y, b.x, b.y}; return __builtin_bit_cast(bf16x8, v); }

struct Ptrs { const u16 *x, *wqkv, *wo, *win, *wsp, *bsp, *wout; u16* out; char* ws; };

#define STAMP(k)

DI void poll_flags(char* ws, int off, bool active, unsigned tag) {
  GAS unsigned* p = (GAS unsigned*)(ws + off);
  _Pragma("unroll 1") for (unsigned sp = 0;; ++sp) {
    const unsigned v = __hip_atomic_load(p, __ATOMIC_RELAXED, __HIP_MEMORY_SCOPE_AGENT);
    if (__all(!active || v == tag)) break;
    if (sp > SPIN_MAX) { __hip_atomic_store((GAS unsigned*)(ws + W_TMO), 1u, __ATOMIC_RELAXED, __HIP_MEMORY_SCOPE_AGENT); break; }
    __builtin_amdgcn_s_sleep(2);
  }
  asm volatile("" ::: "memory");
}
DI void set_flag(char* ws, int off, unsigned tag) { __hip_atomic_store((GAS unsigned*)(ws + off), tag, __ATOMIC_RELAXED, __HIP_MEMORY_SCOPE_AGENT); }

template <int H>
DI void body(const Ptrs& P, LAS char* lds, const int wm, const int lane0, const int pi, const int pj, unsigned epoch, const rsrc_t rsCode) {
  constexpr int NF = H ? 4 : 5, NB = H ? 5 : 0;
  int lane = lane0, fr = lane & 15, g = lane >> 4;
  auto relane = [&]() { lane = opq(lane0); fr = lane & 15; g = lane >> 4; };
  const int gc = pj >> 2, gu = pj & 3, qo = 4 * (pj & 1);
  const int rot = (pi + 2 * pj) % 9;
  const rsrc_t rsX = mkrs(P.x + (size_t)pi * 128 * D, 128 * D * 2), rsWin = mkrs(P.win, D * 2 * FH * 2), rsQkv = mkrs(P.wqkv, D * 3 * D * 2);
  const rsrc_t rsSp = mkrs(P.wsp + (size_t)pi * 128 * S, 128 * S * 2), rsWs = mkrs(P.ws, W_END);
  const rsrc_t rsWout = mkrs(P.wout, FH * D * 2), rsWo = mkrs(P.wo, D * D * 2);
  const int colb = W_COL + pj * COL_SZ, rowb = W_ROW + pi * ROW_SZ, yb = rowb + C_YF + gc * YF_GRP;
  STAMP(0);

  auto rvoff = [&](int stride) { const int l = opq(lane0); return (8 * (l >> 5) + ((l >> 3) & 3)) * stride + ((l & 7) ^ (l >> 4)) * 16; };
  int vA[5], vB[5];
  auto cvoff = [&](int (&v)[5], int kind, int i0 = 0, int i1 = 5) {
    const int l = opq(lane0);
    const int t = kind == 0 ? pj : gu, b0 = 72 * pj - 8 * (pj & 1);
    const int a0 = 256 * t, a1 = (kind == 0 ? 4096 : 1024) + 32 * t - 128, a2 = 256 * t - 32;
#pragma unroll
    for (int idx = i0; idx < i1; ++idx) {
      const unsigned n = 64 * (wm + 4 * idx) + l, Pp = __umul24(n, 3641u) >> 16, cc = n - __umul24(Pp, 18u);
      unsigned k; int add;
      if (kind == 2) k = (Pp & ~24u) | ((Pp & 8u) << 1) | ((Pp & 16u) >> 1); else k = (Pp & ~12u) | ((Pp & 4u) << 1) | ((Pp & 8u) >> 1);
      if (kind != 1) { add = a0; add = cc >= 8 ? a1 : add; add = cc >= 10 ? a2 : add; }
      else { add = 1152; add = cc >= 4 ? 2304 - 64 : add; add = cc >= 8 ? 1152 - 64 : add; add = cc >= 9 ? 2304 - 80 : add; add = cc >= 10 ? -160 : add; add = cc >= 15 ? OOB : add; add += b0; }
      v[idx] = (int)(__umul24(k, kind == 0 ? 9216u : kind == 1 ? 3456u : 1152u) + 16u * cc) + add;
      if (idx == 4) v[idx] = wm >= 2 ? OOB : v[idx];
      asm volatile("" : "+v"(v[idx]));
    }
  };
  auto ksl = [&](int s) { int k = rot + s; return k >= 9 ? k - 9 : k; };
  auto kord = [&](int p) { return p < 2 ? 2 * pi + p : (p - 2 < 2 * pi ? p - 2 : p); };
  auto g3stage = [&](int q) { return q < 3 ? q : q == 11 ? 3 : 4 + ((2 * gu + (q - 3)) & 7); };
  auto ring = [&](int st, int idx, int dyn = 0) {
    const int slot = st & 3;
    if constexpr (H == 1) {
      LAS char* dst = lds + ((idx == 4 && wm >= 2) ? L_DUMMY : slot * C_SZ + (wm + 4 * idx) * 1024);
      if (st < 9) dma16<0>(rsWin, dst, vA[idx], 4608 + ksl(st) * (64 * 9216));
      else if (st < 18) dma16<0>(rsQkv, dst, vB[idx], ksl(st - 9) * (64 * 3456));
      else if (st < 27) dma16<0>(rsWin, dst, vA[idx], ksl(st - 18) * (64 * 9216));
      else if (st < 59) dma16<SC1>(rsWs, dst, vB[idx == 4], colb + C_ZF + kord(dyn) * C_SZ + (wm + 4 * idx) * 1024);
      else { const int q = st - 59;
        if (q < 3) dma16<0>(rsWo, dst, vA[idx], (144 * gc + 64 * q) * 1152);
        else if (q == 11) dma16<0>(rsWout, dst, vA[idx], (2048 + 64 * gc) * 1152);
        else dma16<0>(rsWout, dst, vA[idx], (512 * gc + 64 * (g3stage(q) - 4)) * 1152); }
    } else {
      if (idx == 4) return;
      LAS char* dst = lds + R_OFF + slot * R_SZ + (4 * wm + idx) * 1024;
      const int rb = 32 * wm + 16 * (idx & 1) + 4 * (idx >> 1);
      if (st < 9) dma16<0>(rsX, dst, vA[idx & 1], rb * 1152 + ksl(st) * 128);
      else if (st < 27) return;
      else if (st < 59) dma16<0>(rsSp, dst, vA[idx & 1], rb * 4096 + kord(dyn) * 128);
      else dma16<SC1>(rsWs, dst, vB[0], yb + g3stage(st - 59) * 16384 + (4 * wm + idx) * 1024);
    }
  };
  auto ring_all = [&](int st) { ring(st, 4); ring(st, 0); ring(st, 1); ring(st, 2); ring(st, 3); };
  auto ring_pos = [&](int st, int k, int nf) {
    const int dyn = st - 27;
    if (nf == 5) { ring(st, k == 0 ? 4 : k - 1, dyn); }
    else if (nf == 4) { if (k == 0) ring(st, 4, dyn); ring(st, k, dyn); }
    else { if (k == 0) { ring(st, 4, dyn); ring(st, 0, dyn); } else if (k == 1) { ring(st, 1, dyn); ring(st, 2, dyn); } else ring(st, 3, dyn); }
  };
  if constexpr (H == 1) cvoff(vA, 0); else { vA[0] = rvoff(1152); vA[1] = vA[0] ^ 64; }
  auto codewarm = [&]() { if (wm == 3) dma4(rsCode, lds + L_DUMMY, opq(lane0) * 128, ((blockIdx.x >> 3) & 15) * 8192); };
  if constexpr (H == 0) codewarm();
  ring_all(0); ring_all(1);
  STAMP(17);
  if constexpr (H == 1) cvoff(vB, 1);
  else {
    const int t = wm * 64 + lane;
    const int rd = (t * 205) >> 10, e0 = 4 * (t - 5 * rd), hd = rd >= 18, dc = qo + rd;
    const int gb = (dc >> 4) * 3072 - (dc >> 5) * 1024 + ((dc >> 2) & 3) * 256 + (dc & 3) * 4, sb = (dc >> 5) * 1024 + ((dc >> 2) & 3) * 256 + ((dc >> 4) & 1) * 8 + (dc & 3) * 2;
    unsigned go[4], so[4];
#pragma unroll
    for (int k = 0; k < 4; ++k) { const int e = e0 + k, ce = qo + 18 * hd + e; int off = (ce >> 4) * 1024 + (ce & 15) * 16; off = ce >= 32 ? off + 128 : off; off = e >= 18 ? 2048 : off;
      int ep = 18 * hd + e; ep = e == 18 ? 36 + hd : ep; ep = e == 19 ? 40 : ep;
      go[k] = gb + off; so[k] = sb + (ep >> 4) * 2048 + (ep & 15) * 16; }
    if (t < 180) *(LAS u32x4*)(lds + L_TAB + t * 16) = (u32x4){go[0] | (go[1] << 16), go[2] | (go[3] << 16), so[0] | (so[1] << 16), so[2] | (so[3] << 16)};
  }
  LAS char *aR0, *aR1, *aC0, *aC1;
  auto mk_aR = [&]() { const int l = opq(lane0), r = l & 15, o = (32 * wm + r) * 128 + (((l >> 4) ^ ((r >> 1) & 7)) * 16); aR0 = lds + R_OFF + o; aR1 = lds + R_OFF + (o ^ 64); };
  auto mk_aC = [&]() { const int l = opq(lane0); aC0 = lds + (16 * (l >> 5) + 4 * ((l >> 4) & 1) + ((l & 15) >> 2)) * 288 + (l & 3) * 8 + NB * 32; aC1 = aC0 + 32 * 288; asm volatile("" : "+v"(aC1)); };
  asm volatile("" : "+s"(epoch));
  const unsigned tag = epoch + 1u;
  unsigned ONES = 0x3f803f80u; asm volatile("" : "+s"(ONES));
  auto raise = [&](int off, bool on = true) { __builtin_amdgcn_raw_buffer_store_b32(tag, rsWs, (opq(lane0) == 0 && wm == 0 && on) ? 0 : OOB, off, SC1); };
  auto flags_dma = [&](int voff, int soff) { dma4(rsWs, lds + L_FLG + wm * 256, voff, soff); };
  auto check_flags = [&](int off) { const unsigned v = *(LAS unsigned*)(lds + L_FLG + wm * 256 + opq(lane0) * 4);
    if (UNLIKELY(!__all(v == tag))) { STAMP(18); poll_flags(P.ws, off, true, tag); } };

  bf16x8 xf[9][2][2];
  f32x4 acc[NF][2];
  f32x4 zm[2], zr2[2];
  auto z2_read = [&](int n) { const int l = opq(lane0), r = l & 15, q = l >> 4;
    return *(LAS u32x4*)(lds + L_STASH + wm * 9216 + n * 1024 + r * 64 + (((q >> 1) ^ ((r >> 2) & 1)) * 32) + (q & 1) * 16); };
  auto z2n_tile = [&](int n, u32x4 z) {
    const u32x4 o = {pk2v(fma2(unpk(z.x), lo2(zr2[0]), lo2(zm[0]))), pk2v(fma2(unpk(z.y), hi2(zr2[0]), hi2(zm[0]))), pk2v(fma2(unpk(z.z), lo2(zr2[1]), lo2(zm[1]))), pk2v(fma2(unpk(z.w), hi2(zr2[1]), hi2(zm[1])))};
    st16<SC1>(o, rsWs, opq(lane0) * 16, colb + C_ZF + (2 * pi + (wm >> 1)) * C_SZ + ((wm & 1) * 9 + n) * 1024); };

  auto pass = [&](auto PASSc) {
    constexpr int PASS = decltype(PASSc)::value;
    constexpr int NFP = (PASS == 1 && H == 1) ? 3 : NF, ST0 = 9 * PASS, NU = 9 * NFP;
    constexpr bool TR = (PASS == 2) || (PASS == 1 && H == 1);
    constexpr int L_LAND = PASS == 0 ? L_LAND0 : PASS == 1 ? L_KVS : R_OFF + 2 * R_SZ;
    const int csg = colb + C_CSG + PASS * 12288;
    s16x4 bq[3][4];
    u32x4 gl[8]; u32x2 zp[2], t2[9]; float tf[16]; f32x4 ct, xs1[2], xs2[2]; float csv = 0.f, cs5 = 0.f;
    auto rdB = [&](int u, int j) { const int s = u / NFP, n = u % NFP; if (u < NU) bq[u % 3][j] = tr1(((j & 2) ? aC1 : aC0) + ((ST0 + s) & 3) * C_SZ + n * 32 + (j & 1) * (8 * 288)); };
    auto rdX = [&](int s, int j) { xf[s][j >> 1][j & 1] = *(LAS bf16x8*)(((j & 1) ? aR1 : aR0) + (s & 3) * R_SZ + (j >> 1) * 2048); };
    auto slot = [&](auto SGc) __attribute__((always_inline)) {
      constexpr int sg = decltype(SGc)::value, s = sg - ST0;
      if (s == 1) { const int l = opq(lane0); const bool on = wm == 0 && pi < 9;
        __builtin_amdgcn_raw_buffer_store_b64((u32x2){__float_as_uint(csv), tag}, rsWs, on ? (NB * 16 + l) * 8 : OOB, csg + rot * 1152, SC1);
        if constexpr (H == 0) __builtin_amdgcn_raw_buffer_store_b64((u32x2){__float_as_uint(cs5), tag}, rsWs, (on && l < 16) ? (64 + l) * 8 : OOB, csg + rot * 1152, SC1); }
      if constexpr (H == 1) {
        if (sg >= 1 && sg <= 7) { const int k = (sg - 1) * 4 + wm, src = k / 9;
          st16<SC1>((u32x4){~0u, ~0u, ~0u, ~0u}, rsWs, (k < 27 && !(ABL & 1)) ? opq(lane0) * 16 : OOB, rowb + C_PART + (pj * 4 + ((gc + 1 + src) & 3)) * PART_SRC + (k - 9 * src) * 1024); }
        if (sg == 8) {
          asm volatile("s_nop 7" : "+v"(xs1[0]), "+v"(xs2[0]), "+v"(xs1[1]), "+v"(xs2[1]));
          const int l = opq(lane0), r = l & 15, j = r & 3;
          LAS char* const o = lds + ((r >> 2) == (l >> 4) ? L_XSTAT + (32 * wm + r) * 4 : L_DUMMY);
#pragma unroll
          for (int it = 0; it < 2; ++it) { float s1 = xs1[it].x, s2 = xs2[it].x;
            s1 = j == 1 ? xs1[it].y : s1; s2 = j == 1 ? xs2[it].y : s2; s1 = j == 2 ? xs1[it].z : s1; s2 = j == 2 ? xs2[it].z : s2; s1 = j == 3 ? xs1[it].w : s1; s2 = j == 3 ? xs2[it].w : s2;
            const float mean = s1 * (1.0f / D), var = fmaxf(s2 * (1.0f / D) - mean * mean, 0.f);
            *(LAS float*)(o + it * 64) = -mean; *(LAS float*)(o + it * 64 + 512) = __builtin_amdgcn_rsqf(var + 1e-5f); } }
        if (s == 7) { const int t = min(wm * 64 + opq(lane0), 143); unsigned bad = 0;
          auto f = [&](int j) { return __uint_as_float(t2[j].x); };
          float c = (((f(0) + f(1)) + (f(2) + f(3))) + ((f(4) + f(5)) + (f(6) + f(7)))) + f(8);
#pragma unroll
          for (int j = 0; j < 9; ++j) bad |= t2[j].y ^ tag;
          if (UNLIKELY(!__all(bad == 0))) {
            _Pragma("unroll 1") for (unsigned sp = 0; !__all(bad == 0); ++sp) {
              STAMP(20);
              if (sp > SPIN_MAX) { set_flag(P.ws, W_TMO, 1u); break; }
              bad = 0;
#pragma unroll
              for (int j = 0; j < 9; ++j) { t2[j] = __builtin_amdgcn_raw_buffer_load_b64(rsWs, t * 8, csg + j * 1152, SC1); bad |= t2[j].y ^ tag; }
              c = (((f(0) + f(1)) + (f(2) + f(3))) + ((f(4) + f(5)) + (f(6) + f(7)))) + f(8);
            } }
          *(LAS float*)(lds + L_CS + PASS * 576 + t * 4) = c; }
        auto svo = [&]() { const int l = opq(lane0); return ((l >> 3) & 1) * 16384 + (32 * wm + 8 * (l >> 4) + (l & 7)) * 16; };
        if (sg == 12 || sg == 13) { const int vo = svo();
#pragma unroll
          for (int j = 4 * (sg - 12); j < 4 * (sg - 11); ++j) HLOAD(gl[j], vo, rsWs, rowb + C_STATS + j * 2048); }
        if (sg == 15) {
          asm volatile("s_waitcnt vmcnt(10) ; HIDDEN_WAIT %0 %1 %2 %3 %4 %5 %6 %7" : "+v"(gl[0]), "+v"(gl[1]), "+v"(gl[2]), "+v"(gl[3]), "+v"(gl[4]), "+v"(gl[5]), "+v"(gl[6]), "+v"(gl[7]) :: "memory");
          float s1, s2; unsigned bad;
          auto red = [&]() { auto a = [&](int j) { return __uint_as_float(gl[j].x); }; auto b = [&](int j) { return __uint_as_float(gl[j].z); }; bad = 0;
            s1 = ((a(0) + a(1)) + (a(2) + a(3))) + ((a(4) + a(5)) + (a(6) + a(7))); s2 = ((b(0) + b(1)) + (b(2) + b(3))) + ((b(4) + b(5)) + (b(6) + b(7)));
#pragma unroll
            for (int j = 0; j < 8; ++j) { bad |= gl[j].y ^ tag; bad |= gl[j].w ^ tag; } };
          red();
          if (UNLIKELY(!__all(bad == 0))) {
            _Pragma("unroll 1") for (unsigned sp = 0; !__all(bad == 0); ++sp) {
              STAMP(19);
              if (sp > SPIN_MAX) { set_flag(P.ws, W_TMO, 1u); break; }
              const int vo = svo();
#pragma unroll
              for (int j = 0; j < 8; ++j) gl[j] = __builtin_amdgcn_raw_buffer_load_b128(rsWs, vo, rowb + C_STATS + j * 2048, SC1);
              red();
            } }
          s1 = ror8sum(s1); s2 = ror8sum(s2);
          const float mu = s1 * (1.0f / FH), var = fmaxf(s2 * (1.0f / FH) - mu * mu, 0.f);
          tf[1] = __builtin_amdgcn_rsqf(var + 1e-5f); tf[0] = -mu * tf[1]; asm volatile("" : "+v"(tf[0]), "+v"(tf[1])); }
        if (sg == 16) {
          zm[0] = (f32x4){bcast16<0>(tf[0]), bcast16<1>(tf[0]), bcast16<2>(tf[0]), bcast16<3>(tf[0])}; zm[1] = (f32x4){bcast16<4>(tf[0]), bcast16<5>(tf[0]), bcast16<6>(tf[0]), bcast16<7>(tf[0])};
          zr2[0] = (f32x4){bcast16<0>(tf[1]), bcast16<1>(tf[1]), bcast16<2>(tf[1]), bcast16<3>(tf[1])}; zr2[1] = (f32x4){bcast16<4>(tf[1]), bcast16<5>(tf[1]), bcast16<6>(tf[1]), bcast16<7>(tf[1])};
          asm volatile("" : "+v"(zm[0]), "+v"(zm[1]), "+v"(zr2[0]), "+v"(zr2[1])); }
        if (sg == 17) { z2n_tile(0, gl[0]); z2n_tile(1, gl[1]); z2n_tile(2, gl[2]); }
        if (sg == 19) { const int t = wm * 64 + opq(lane0); u32x4 o;
#pragma unroll
          for (int k = 0; k < 4; ++k) o[k] = __float_as_uint((tf[4 * k] + tf[4 * k + 1]) + (tf[4 * k + 2] + tf[4 * k + 3]));
          st16<SC1>(o, rsWs, t < 180 ? t * 16 : OOB, colb + C_KVP + pi * KVP_STRIDE); }
        if (sg == 23) { vB[0] = opq(lane0) * 16; vB[1] = wm >= 2 ? OOB : vB[0]; }
        if (sg == 24) flags_dma((opq(lane0) & 15) * 4, colb + C_FLAGB);
        if (sg == 26) check_flags(colb + C_FLAGB + (lane0 & 15) * 4);
      }
    };
    auto front = [&](auto SGc) __attribute__((always_inline)) {
      constexpr int sg = decltype(SGc)::value, s = sg - ST0;
      if constexpr (H == 0) {
        if (s == 4) { const int vo = opq(lane0) * 16;
#pragma unroll
          for (int k = 0; k < 3; ++k) dma16<SC1>(rsWs, lds + L_LAND + (wm + 4 * k) * 1024, vo, csg + (wm + 4 * k) * 1024); }
        if (sg == 21) raise(colb + C_FLAGB + pi * 4);
        if (sg == 22) { vA[0] = rvoff(4096); vA[1] = vA[0] ^ 64; }
      } else {
        if (sg == 9) { const int r = wm * 64 + opq(lane0); const f32x2 a = __builtin_bit_cast(f32x2, zp[0]) + __builtin_bit_cast(f32x2, zp[1]);
          st16<SC1>((u32x4){__float_as_uint(a.x), tag, __float_as_uint(a.y), tag}, rsWs, r < 128 ? r * 16 : OOB, rowb + C_STATS + pj * 2048); }
        if (s == 6) { const int t = min(wm * 64 + opq(lane0), 143);
#pragma unroll
          for (int j = 0; j < 9; ++j) t2[j] = *(LAS u32x2*)(lds + L_LAND + j * 1152 + t * 8); }
        if (sg == 16) { gl[0] = z2_read(0); gl[1] = z2_read(1); gl[2] = z2_read(2); }
      }
    };
#pragma unroll
    for (int n = NFP; n < NF; ++n) { acc[n][0] = (f32x4){0, 0, 0, 0}; acc[n][1] = (f32x4){0, 0, 0, 0}; }
    mk_aC();
    if (PASS == 0) { mk_aR(); rdX(0, 0); rdX(0, 1); rdX(0, 2); rdX(0, 3); }
#pragma unroll
    for (int j = 0; j < 8; ++j) rdB(j >> 2, j & 3);
    if (PASS == 0) ring_all(2);
    bf16x8 ones8 = __builtin_bit_cast(bf16x8, (u32x4){ONES, ONES, ONES, ONES}); asm volatile("" : "+v"(ones8));
    const int gq = opq(lane0) >> 4;
    SB();
    sfor<0, NU>([&](auto Uc) __attribute__((always_inline)) {
      constexpr int u = decltype(Uc)::value, s = u / NFP, n = u % NFP, sg = ST0 + s, d = u - (NFP - 2);
      if (n == NFP - 2) {
        slot(IC<sg>{});
        if constexpr (H == 1) waitv<allow1(sg)>();
        else { if (sg == 1 || sg == 2) waitv<6>(); else if (sg == 5) waitv<7>(); else if (sg < 7 || sg == 26) waitv<4>(); else if (sg == 7 || sg == 15 || sg == 24) waitv<0>(); }
        if ((sg == 8 && H == 1) || (sg == 18 && H == 0)) waitl0();
        bar();
        if (H == 1 && sg == 9) { const int r = (wm * 64 + opq(lane0)) & 127; zp[0] = *(LAS u32x2*)(lds + L_ZPART + r * 8); zp[1] = *(LAS u32x2*)(lds + L_ZPART + 1024 + r * 8); }
        if (H == 1 && sg == 18) zp[0] = *(LAS u32x2*)(lds + L_TAB + min(wm * 64 + opq(lane0), 179) * 16);
        SB();
      }
      const bf16x8 b0 = cat4(bq[u % 3][0], bq[u % 3][1]), b1 = cat4(bq[u % 3][2], bq[u % 3][3]);
#pragma unroll
      for (int j = 0; j < 4; ++j) { const int it = j >> 1, hh = j & 1; const bf16x8 &bb = hh ? b1 : b0, &oa = TR ? bb : xf[s][it][hh], &ob = TR ? xf[s][it][hh] : bb;
        if (s == 0 && hh == 0) mf0(acc[n][it], oa, ob); else mfa(acc[n][it], oa, ob);
        SB();
        rdB(u + 2, j);
        if (PASS == 0 && n == NFP - 2 && s < 8) rdX(s + 1, j);
        if (j == 3) {
          if (u >= 1 && u <= NFP) { asm volatile("s_nop 0" : "+v"(ct)); if (u - 1 < 4) csv = gq == u - 1 ? ct.x : csv; else cs5 = ct.x; asm volatile("" : "+v"(csv), "+v"(cs5)); }
          if (n == NFP - 2) front(IC<sg>{});
          if (H == 1 && sg == 19 && n == 0) {
#pragma unroll
            for (int k = 0; k < 4; ++k) { LAS char* const a = lds + L_KVS + ((k & 1) ? zp[0][k >> 1] >> 16 : zp[0][k >> 1] & 0xffffu);
#pragma unroll
              for (int w = 0; w < 4; ++w) tf[4 * k + w] = *(LAS float*)(a + w * 8192); } }
          if constexpr (d >= 0) ring_pos(ST0 + d / NFP + 3, d % NFP, NFP); }
        SB(); }
      if (s == 0) { mf0(ct, ones8, b0); mfa(ct, ones8, b1); }
      if (PASS == 0 && H == 1 && n < 2 && (s == 0 || !(ABL & 2))) {
#pragma unroll
        for (int it = 0; it < 2; ++it) { f32x4& xs = n ? xs2[it] : xs1[it]; const bf16x8 &o0 = n ? xf[s][it][0] : ones8, &o1 = n ? xf[s][it][1] : ones8;
          if (s == 0) mf0(xs, xf[s][it][0], o0); else mfa(xs, xf[s][it][0], o0); mfa(xs, xf[s][it][1], o1); } }
      SB();
    });
#pragma unroll
    for (int k = 2; k < NFP; ++k) ring_pos(ST0 + 11, k, NFP);
    accfence(acc);
    relane();
  };
  auto csum1 = [&](int ps, int col) { return *(LAS float*)(lds + L_CS + ps * 576 + col * 4); };
  auto rowstat = [&](f32x2 (&nm)[4], f32x2 (&rs)[4]) {
#pragma unroll
    for (int it = 0; it < 2; ++it) { const f32x4 a = *(LAS f32x4*)(lds + L_XSTAT + (32 * wm + 16 * it + 4 * g) * 4), b = *(LAS f32x4*)(lds + L_XSTAT + 512 + (32 * wm + 16 * it + 4 * g) * 4);
      nm[2 * it] = lo2(a); nm[2 * it + 1] = hi2(a); rs[2 * it] = lo2(b); rs[2 * it + 1] = hi2(b); } };
  auto affN = [&](int n, float c, const f32x2 (&nm)[4], const f32x2 (&rs)[4], f32x2 (&v)[4]) { const f32x2 c2 = {c, c};
    v[0] = fma2(nm[0], c2, lo2(acc[n][0])) * rs[0]; v[1] = fma2(nm[1], c2, hi2(acc[n][0])) * rs[1]; v[2] = fma2(nm[2], c2, lo2(acc[n][1])) * rs[2]; v[3] = fma2(nm[3], c2, hi2(acc[n][1])) * rs[3]; };

  if constexpr (H == 1) { if (!(ABL & 4)) __builtin_amdgcn_s_setprio(1); waitv<5>(); } else waitv<4>();
  bar();
  STAMP(1);
  pass(IC<0>{});
  STAMP(2);
  if constexpr (H == 0) {
    dma4(mkrs(P.wsp, S * S * 2), lds + L_DUMMY, lane * 128, (blockIdx.x * 4 + wm) * 8192);
    if (wm == 0) dma4(mkrs(P.bsp, S * 2), lds + L_BIAS, lane * 4, pi * 256);
  }
  {
    f32x2 nm[4], rs[4]; rowstat(nm, rs);
    LAS char* const sw = lds + L_STASH + (wm * 9 + NB) * 1024 + fr * 64 + (((g >> 1) ^ ((fr >> 2) & 1)) * 32) + (g & 1) * 16;
    LAS char* const sr = lds + L_STASH + (wm * 9 + NB) * 1024 + (4 * g + (fr >> 2)) * 64 + (g & 1) * 32 + (fr & 3) * 8;
    LAS char* const sr1 = lds + L_STASH + (wm * 9 + NB) * 1024 + (4 * g + (fr >> 2)) * 64 + ((g & 1) ^ 1) * 32 + (fr & 3) * 8;
    float cn[NF];
#pragma unroll
    for (int n = 0; n < NF; ++n) cn[n] = csum1(0, (NB + n) * 16 + fr);
#pragma unroll
    for (int n = 0; n < NF; ++n) { f32x2 v[4]; affN(n, cn[n], nm, rs, v);
      *(LAS u32x4*)(sw + n * 1024) = (u32x4){pk2v(gelu2(v[0])), pk2v(gelu2(v[1])), pk2v(gelu2(v[2])), pk2v(gelu2(v[3]))}; }
    asm volatile("" ::: "memory");
    float s1[2] = {0.f, 0.f}, s2[2] = {0.f, 0.f};
#pragma unroll
    for (int n = 0; n < NF; ++n)
#pragma unroll
      for (int ih = 0; ih < 2; ++ih) { const u32x2 t = __builtin_bit_cast(u32x2, __builtin_amdgcn_ds_read_tr16_b64_v4i16((LAS s16x4*)((ih ? sr1 : sr) + n * 1024)));
        s1[ih] = dot2(t.x, ONES, s1[ih]); s2[ih] = dot2(t.x, t.x, s2[ih]); s1[ih] = dot2(t.y, ONES, s1[ih]); s2[ih] = dot2(t.y, t.y, s2[ih]); }
    const float a0 = gsum(s1[0]), b0 = gsum(s2[0]), a1 = gsum(s1[1]), b1 = gsum(s2[1]);
    if (g < 2) *(LAS f32x2*)(lds + L_ZPART + (H * 128 + 32 * wm + 16 * g + fr) * 8) = g ? (f32x2){a1, b1} : (f32x2){a0, b0};
    waitl0();
  }
  STAMP(3);
  pass(IC<1>{});
  STAMP(4);
  u32x2 pq[3][2];
  f32x4 aq[3][2];
  if constexpr (H == 0) {
        { const int l = opq(lane0), li = (wm & 1) * 64 + l;
          if (wm < 2) dma4(rsWout, lds + L_DUMMY, li < 81 ? li * 128 : OOB, blockIdx.x * 81 * 128);
          else if (wm == 2) dma4(rsWo, lds + L_DUMMY, l < 21 ? l * 128 : OOB, blockIdx.x * 21 * 128);
          codewarm(); }
    f32x2 nm[4], rs[4]; rowstat(nm, rs);
    float cn[5];
#pragma unroll
    for (int n = 0; n < 5; ++n) cn[n] = csum1(1, n * 16 + fr);
    *(LAS u32x4*)(lds + L_KVF + (wm * 64 + lane) * 16) = (u32x4){0, 0, 0, 0};
    *(LAS u32x2*)(lds + L_KVF + 4096 + (wm * 64 + lane) * 8) = (u32x2){0, 0};
    bf16x8 pkf[3], vf[3];
#pragma unroll
    for (int n = 0; n < 5; ++n) { f32x2 v[4]; affN(n, cn[n], nm, rs, v);
      if (n >= 2) { u32x4 w = {pk2v(v[0]), pk2v(v[1]), pk2v(v[2]), pk2v(v[3])};
        if (n == 4 && fr == 0) w = (u32x4){ONES, ONES, ONES, ONES};
        vf[n - 2] = __builtin_bit_cast(bf16x8, w); }
      if (n < 2 || n == 4) pkf[n == 4 ? 2 : n] = __builtin_bit_cast(bf16x8, (u32x4){pk2v(phi2(v[0])), pk2v(phi2(v[1])), pk2v(phi2(v[2])), pk2v(phi2(v[3]))});
    }
#pragma unroll
    for (int td = 0; td < 3; ++td)
#pragma unroll
      for (int te = 0; te < 3; ++te) { if (td == 2 && te == 0) continue;
        *(LAS f32x4*)(lds + L_KVS + (wm * 8 + td * 3 + te - (td == 2)) * 1024 + lane * 16) = mfma(pkf[td], vf[te], (f32x4){0, 0, 0, 0}); }
  } else {
    u32x4 z[6];
#pragma unroll
    for (int n = 0; n < 6; ++n) z[n] = z2_read(3 + n);
#pragma unroll
    for (int n = 0; n < 6; ++n) z2n_tile(3 + n, z[n]);
#pragma unroll
    for (int n = 0; n < 3; ++n) { aq[n][0] = acc[n][0]; aq[n][1] = acc[n][1]; }
  }
  STAMP(6);
  pass(IC<2>{});
  STAMP(7);
  u32x2 z1[NF][2];
  f32x4 ag[NF][2];
  {
    constexpr int NR = NF + 2;
    LAS char* const aZ = lds + NB * 1024 + lane * 16;
    mk_aR();
    if constexpr (H == 0) vB[0] = lane * 16;
    f32x4 kvs = {0, 0, 0, 0}; u32x4 kl[2][2]; int kvo;
    if constexpr (H == 0) { const int t = wm * 64 + lane; kvo = t < 180 ? t * 16 : OOB; }
    bf16x8 fa[2][NR], fb[2][NR];
    auto rd1 = [&](bf16x8 (&f)[2][NR], int i, int slot) {
      const int h = i & 1; int ii = i >> 1; if (ii == 1) ii = 2; else if (ii == 2) ii = 1;
      if (ii < 2) f[h][ii] = *(LAS bf16x8*)((h ? aR1 : aR0) + slot * R_SZ + ii * 2048); else f[h][ii] = *(LAS bf16x8*)(aZ + slot * C_SZ + (h * 9 + ii - 2) * 1024);
    };
    float zn[2], zr[2], za, zb, ta, tb; f32x2 zc;
    LAS char* const aCs = lds + L_CS + 576 + (NB * 16 + 4 * g) * 4;
#pragma unroll
    for (int it = 0; it < 2; ++it) { zn[it] = *(LAS float*)(lds + L_XSTAT + (32 * wm + 16 * it + fr) * 4); zr[it] = *(LAS float*)(lds + L_XSTAT + 512 + (32 * wm + 16 * it + fr) * 4); }
    float GC1 = 2.3022082f; asm volatile("" : "+v"(GC1));
    auto zop = [&](int e, int k) { const int n = e >> 2, it = (e >> 1) & 1, h = e & 1;
      if (k == 0) zc = *(LAS f32x2*)(aCs + 576 + n * 64 + h * 8);
      if (k == 3) asm volatile("v_fma_f32 %0, %2, %4, %5\n\tv_fma_f32 %1, %3, %4, %6" : "=&v"(za), "=&v"(zb) : "v"(zc.x), "v"(zc.y), "v"(zn[it]), "v"(acc[n][it][2 * h]), "v"(acc[n][it][2 * h + 1]));
      if (k == 4) asm volatile("v_mul_f32 %0, %0, %2\n\tv_mul_f32 %1, %1, %2" : "+v"(za), "+v"(zb) : "v"(zr[it]));
      if (k == 5) asm volatile("v_mul_f32 %0, %2, %2\n\tv_mul_f32 %1, %3, %3" : "=&v"(ta), "=&v"(tb) : "v"(za), "v"(zb));
      if (k == 6) asm volatile("v_fmamk_f32 %0, %0, 0x3dd2d3e8, %2\n\tv_fmamk_f32 %1, %1, 0x3dd2d3e8, %2" : "+v"(ta), "+v"(tb) : "v"(GC1));
      if (k == 7) asm volatile("v_mul_f32 %0, %0, %2\n\tv_mul_f32 %1, %1, %3" : "+v"(ta), "+v"(tb) : "v"(za), "v"(zb));
      if (k == 8) asm volatile("v_exp_f32 %0, %0" : "+v"(ta));
      if (k == 9) asm volatile("v_exp_f32 %0, %0" : "+v"(tb));
      if (k == 10) asm volatile("v_add_f32 %0, 1.0, %0\n\tv_add_f32 %1, 1.0, %1" : "+v"(ta), "+v"(tb));
      if (k == 11) asm volatile("v_rcp_f32 %0, %0" : "+v"(ta));
      if (k == 12) asm volatile("v_rcp_f32 %0, %0" : "+v"(tb));
      if (k == 13) asm volatile("v_fma_f32 %0, -%0, %2, %0\n\tv_fma_f32 %1, -%1, %3, %1" : "+v"(za), "+v"(zb) : "v"(ta), "v"(tb));
      if (k == 14) { unsigned o; asm volatile("v_cvt_pk_bf16_f32 %0, %1, %2" : "=v"(o) : "v"(za), "v"(zb)); z1[n][it][h] = o; } };
    auto qop = [&](int e, int k) { const int n = e >> 2, it = (e >> 1) & 1, h = e & 1;
      if (k == 0) zc = *(LAS f32x2*)(aCs + n * 64 + h * 8);
      if (k == 3) asm volatile("v_fma_f32 %0, %2, %4, %5\n\tv_fma_f32 %1, %3, %4, %6" : "=&v"(za), "=&v"(zb) : "v"(zc.x), "v"(zc.y), "v"(zn[it]), "v"(aq[n][it][2 * h]), "v"(aq[n][it][2 * h + 1]));
      if (k == 4) asm volatile("v_mul_f32 %0, %0, %2\n\tv_mul_f32 %1, %1, %2" : "+v"(za), "+v"(zb) : "v"(zr[it]));
      if (k == 5) asm volatile("v_min_f32 %0, 0, %2\n\tv_min_f32 %1, 0, %3" : "=&v"(ta), "=&v"(tb) : "v"(za), "v"(zb));
      if (k == 6) asm volatile("v_mul_f32 %0, 0x3fb8aa3b, %0\n\tv_mul_f32 %1, 0x3fb8aa3b, %1" : "+v"(ta), "+v"(tb));
      if (k == 7) asm volatile("v_exp_f32 %0, %0" : "+v"(ta));
      if (k == 8) asm volatile("v_exp_f32 %0, %0" : "+v"(tb));
      if (k == 9) asm volatile("v_max_f32 %0, 0, %0\n\tv_max_f32 %1, 0, %1" : "+v"(za), "+v"(zb));
      if (k == 10) asm volatile("v_add_f32 %0, %0, %2\n\tv_add_f32 %1, %1, %3" : "+v"(za), "+v"(zb) : "v"(ta), "v"(tb));
      if (k == 11) asm volatile("v_add_f32 %0, 0x358637bd, %0\n\tv_add_f32 %1, 0x358637bd, %1" : "+v"(za), "+v"(zb));
      if (k == 12) { unsigned o; asm volatile("v_cvt_pk_bf16_f32 %0, %1, %2" : "=v"(o) : "v"(za), "v"(zb)); pq[n][it][h] = o; } };
    auto kvscatter = [&]() { const int t = wm * 64 + opq(lane0); const u32x2 tb = *(LAS u32x2*)(lds + L_TAB + min(t, 179) * 16 + 8);
      const unsigned k01 = pk2v(lo2(kvs)), k23 = pk2v(hi2(kvs)), msk = t < 180 ? 0xffffu : 0u; LAS char* const a = lds + (t < 180 ? L_KVF : L_DUMMY);
      *(LAS u16*)(a + (tb.x & msk)) = (u16)k01; *(LAS u16*)(a + ((tb.x >> 16) & msk)) = (u16)(k01 >> 16); *(LAS u16*)(a + (tb.y & msk)) = (u16)k23; *(LAS u16*)(a + ((tb.y >> 16) & msk)) = (u16)(k23 >> 16); };
    auto ostep = [&](int it) {
      const int l = opq(lane0), r = l & 15, gg = l >> 4; bf16x8 kf[3][2];
#pragma unroll
      for (int en = 0; en < 3; ++en) { kf[en][0] = *(LAS bf16x8*)(lds + L_KVF + (en * 2) * 1024 + l * 16); kf[en][1] = *(LAS bf16x8*)(lds + L_KVF + (en * 2 + 1) * 1024 + l * 16); }
      { const bf16x8 b0 = cat(pq[0][it], pq[1][it]), b1 = cat(pq[2][it], (u32x2){0, 0}); f32x4 nm[3];
#pragma unroll
        for (int en = 0; en < 3; ++en) nm[en] = mfma(kf[en][1], b1, mfma(kf[en][0], b0, (f32x4){0, 0, 0, 0}));
        const float rA = __builtin_amdgcn_rcpf(__shfl(nm[2][0], 16 + r) + 1e-6f), rB = __builtin_amdgcn_rcpf(__shfl(nm[2][1], 16 + r) + 1e-6f);
        const float rM = gg == 0 ? rA : rB;
#pragma unroll
        for (int en = 0; en < 3; ++en) { const float r01 = en == 0 ? rA : en == 1 ? rM : rB, r23 = en == 0 ? rA : rB;
          const u32x2 o = {pk2v(lo2(nm[en]) * r01), pk2v(hi2(nm[en]) * r23)};
          const int oc4 = 9 * gu + 4 * en + gg;
          int vo = (oc4 >> 3) * 8192 + ((oc4 >> 2) & 1) * 512 + ((oc4 & 3) * 16 + r) * 8;
          if (en == 2) vo = gg != 0 ? OOB : vo;
          __builtin_amdgcn_raw_buffer_store_b64(o, rsWs, vo, yb + (2 * wm + it) * 1024, SC1); } } };
    auto step = [&](bf16x8 (&cur)[2][NR], bf16x8 (&nxt)[2][NR], auto Pc) {
      constexpr int p = decltype(Pc)::value, st = 30 + p, e = p - (H ? 12 : 1);
      auto vop = [&](int k) { if (H == 1 && p < 12) qop(p, k); else if (e >= 0 && e < 4 * NF) zop(e, k); };
      if constexpr (H == 1) { if (p == 14 || p == 15) ostep(p - 14); if (p >= 20 && p < 25) cvoff(vA, 2, p - 20, p - 19); waitv<allow1(27 + p)>(); }
      else if (H == 0 && p >= 2 && p <= 9) { if (p <= 8) asm volatile("s_waitcnt vmcnt(6) ; HIDDEN_WAIT %0 %1" : "+v"(kl[p & 1][0]), "+v"(kl[p & 1][1]) :: "memory");
                                             else asm volatile("s_waitcnt vmcnt(4) ; HIDDEN_WAIT %0 %1" : "+v"(kl[p & 1][0]), "+v"(kl[p & 1][1]) :: "memory"); }
      else if (H == 0 && p == 1) waitv<6>();
      else if (H == 0 && (p == 18 || p == 20)) waitv<5>();
      else waitv<4>();
      if (p == 31) waitl0();
      bar();
      SB();
      if constexpr (H == 0) {
        if (p >= 2 && p <= 9) { kvs += __builtin_bit_cast(f32x4, kl[p & 1][0]) + __builtin_bit_cast(f32x4, kl[p & 1][1]); asm volatile("" : "+v"(kvs)); }
        if (p < 8) { HLOAD(kl[p & 1][0], kvo, rsWs, colb + C_KVP + 2 * p * KVP_STRIDE); HLOAD(kl[p & 1][1], kvo, rsWs, colb + C_KVP + (2 * p + 1) * KVP_STRIDE); }
        if (p == 10) kvscatter();
        if (p == 17) raise(rowb + C_FLAGO + pj * 4);
        if (p == 19) raise(W_EPOCH, blockIdx.x == 0);
        if (p == 22) codewarm();
        if (p == 27) flags_dma((opq(lane0) & 15) * 4, rowb + C_FLAGO);
        if (p == 28) check_flags(rowb + C_FLAGO + (lane0 & 15) * 4);
      }
      ring(st, 4, p + 3);
      SB();
      vop(0);
#pragma unroll
      for (int i = 0; i < 4 * NF; ++i) {
        const int n = i >> 2, m = (i >> 1) & 1, h = i & 1;
        if (p == 0 && h == 0) mf0(ag[n][m], cur[0][2 + n], cur[0][m]); else mfa(ag[n][m], cur[h][2 + n], cur[h][m]);
        SB();
        if (p != 31 && i < 2 * NR) rd1(nxt, i, p & 3);
        if (i > 0) vop(i);
        if ((i & 3) == 3 && i < 16) ring(st, i >> 2, p + 3);
        SB();
      }
    };
#pragma unroll
    for (int i = 0; i < 2 * NR; ++i) rd1(fa, i, 3);
    STAMP(9);
    sfor<0, 16>([&](auto Kc) __attribute__((always_inline)) { constexpr int k = decltype(Kc)::value; step(fa, fb, IC<2 * k>{}); step(fb, fa, IC<2 * k + 1>{}); if (k == 5) STAMP(8); });
    accfence(ag);
  }
  STAMP(10);
  relane();
  {
    u32x2 y[NF][2];
#pragma unroll
    for (int it = 0; it < 2; ++it) { const float b = blo(*(LAS u16*)(lds + L_BIAS + (32 * wm + 8 * (fr >> 2) + 4 * it + (fr & 3)) * 2));
#pragma unroll
      for (int n = 0; n < NF; ++n) y[n][it] = (u32x2){pk2v(unpk(z1[n][it].x) * (lo2(ag[n][it]) + b)), pk2v(unpk(z1[n][it].y) * (hi2(ag[n][it]) + b))}; }
    waitv<0>();
#pragma unroll
    for (int hh = 0; hh < 2; ++hh)
#pragma unroll
      for (int it = 0; it < 2; ++it) { const u32x4 v = __builtin_bit_cast(u32x4, cat(y[2 * hh][it], y[2 * hh + 1][it]));
        st16<SC1>(v, rsWs, lane * 16, yb + (4 + 2 * gu + H) * 16384 + hh * 8192 + (2 * wm + it) * 1024);
        *(LAS u32x4*)(lds + (H ? L_YOWN : R_OFF + 2 * R_SZ) + hh * 8192 + (2 * wm + it) * 1024 + lane * 16) = v; }
    if constexpr (H == 0) {
#pragma unroll
      for (int it = 0; it < 2; ++it) __builtin_amdgcn_raw_buffer_store_b64(y[4][it], rsWs, lane * 8, yb + 3 * 16384 + (gu >> 1) * 8192 + (2 * wm + it) * 1024 + (gu & 1) * 512, SC1);
    }
  }
  STAMP(11);
  {
    LAS char* const aY = lds + R_OFF + 2 * wm * 1024;
    bf16x8 rf[2][2][2]; s16x4 bq[3][4];
    mk_aC();
    auto rdB = [&](int u, int j) { const int q = u / NF, n = u % NF; if (u < 12 * NF && (q != 2 || j < 2)) bq[u % 3][j] = tr1(((j & 2) ? aC1 : aC0) + ((59 + q) & 3) * C_SZ + n * 32 + (j & 1) * (8 * 288)); };
    auto rdY = [&](int q, int j) { const int it = j >> 1, hh = j & 1; LAS char* a = aY + (q == 4 ? L_YOWN - R_OFF : ((59 + q) & 3) * R_SZ) + hh * 8192 + it * 1024;
      if (q == 2) { if (hh == 0) rf[q & 1][it][0] = cat(*(LAS u32x2*)(a + lane * 8), (u32x2){0, 0}); }
      else if (q < 2 || q == 11) rf[q & 1][it][hh] = cat(*(LAS u32x2*)(a + lane * 8), *(LAS u32x2*)(a + 512 + lane * 8));
      else rf[q & 1][it][hh] = *(LAS bf16x8*)(a + lane * 16); };
    rdY(0, 0); rdY(0, 1); rdY(0, 2); rdY(0, 3);
#pragma unroll
    for (int j = 0; j < 8; ++j) rdB(j >> 2, j & 3);
    SB();
    sfor<0, 12 * NF>([&](auto Uc) __attribute__((always_inline)) {
      constexpr int u = decltype(Uc)::value, q = u / NF, n = u % NF;
      if (n == NF - 2 && q < 11) {
        if constexpr (H == 1) { if (q == 10) waitv<0>(); else if (q >= 1) waitv<5>(); }
        else { if (q == 1 || q == 3 || q == 10) waitv<0>(); else if (q >= 4) waitv<4>(); }
        bar();
        if constexpr (H == 0) {
          if (q == 1) raise(rowb + C_FLAGD + gc * 128 + gu * 4);
          if (q == 2) flags_dma((opq(lane0) & 3) * 4, rowb + C_FLAGD + gc * 128);
          if (q == 3) { STAMP(12); check_flags(rowb + C_FLAGD + gc * 128 + (lane0 & 3) * 4); STAMP(13); ring_all(64); }
          if (q == 4) STAMP(14);
        }
        SB();
      }
      const bf16x8 b0 = cat4(bq[u % 3][0], bq[u % 3][1]), b1 = q == 2 ? b0 : cat4(bq[u % 3][2], bq[u % 3][3]);
#pragma unroll
      for (int j = 0; j < 4; ++j) { const int it = j >> 1, hh = j & 1;
        if (q == 0 && hh == 0) mf0(acc[n][it], b0, rf[q & 1][it][hh]); else if (q != 2 || hh == 0) mfa(acc[n][it], hh ? b1 : b0, rf[q & 1][it][hh]);
        SB();
        rdB(u + 2, j);
        if (n == NF - 2 && q < 11) rdY(q + 1, j);
        if (j == 3) { const int d = u - (NF - 2); if (d >= 0) { const int qq = d / NF + 3; if (qq < 12 && (H == 1 || qq >= 6)) ring_pos(59 + qq, d % NF, NF); } }
        SB(); }
    });
    accfence(acc);
  }
  STAMP(15);
  relane();
  {
    const rsrc_t rsOut = mkrs(P.out + (size_t)pi * 128 * D, 128 * D * 2);
    auto packp = [&](const f32x4 (&a)[2]) { u32x4 v = {pk2v(lo2(a[0])), pk2v(hi2(a[0])), pk2v(lo2(a[1])), pk2v(hi2(a[1]))};
      v.x = min(v.x, 0xfffffffeu); v.y = min(v.y, 0xfffffffeu); v.z = min(v.z, 0xfffffffeu); v.w = min(v.w, 0xfffffffeu); return v; };
    constexpr int NK = H ? 1 : 2;
    f32x4 kp[NK][2]; int cb[NK];
#pragma unroll
    for (int it = 0; it < 2; ++it) { kp[0][it] = gc == 0 ? acc[0][it] : gc == 1 ? acc[1][it] : gc == 2 ? acc[2][it] : acc[3][it]; if constexpr (H == 0) kp[1][it] = acc[4][it]; }
    cb[0] = 256 * gu + 32 * (H * 4 + gc); if constexpr (H == 0) cb[1] = 1024 + 32 * gu;
    const int off1 = wm == gc ? 0 : OOB;
    int ro[2]; u32x2 xr[NK][2];
#pragma unroll
    for (int it = 0; it < 2; ++it) { ro[it] = (32 * wm + 8 * (fr >> 2) + 4 * it + (fr & 3)) * 1152 + 8 * g;
#pragma unroll
      for (int k = 0; k < NK; ++k) xr[k][it] = __builtin_amdgcn_raw_buffer_load_b64(rsX, ro[it] + (k ? off1 : 0), cb[k], 0); }
#pragma unroll
    for (int n = 0; n < NF; ++n) { const int dg = n < 4 ? n : wm, slot = n < 4 ? H * 4 + wm : 8;
      st16<SC1>(packp(acc[n]), rsWs, dg == gc ? OOB : lane * 16, rowb + C_PART + ((4 * dg + gu) * 4 + gc) * PART_SRC + slot * 1024); }
    const int pb = rowb + C_PART + pj * 4 * PART_SRC;
    u32x4 v[NK][3];
    __builtin_amdgcn_s_sleep(FIN_SLEEP);
    _Pragma("unroll 1") for (unsigned sp = 0;; ++sp) {
      unsigned mx = 0;
#pragma unroll
      for (int k = 0; k < NK; ++k)
#pragma unroll
        for (int j = 0; j < 3; ++j) { const int so = pb + ((gc + 1 + j) & 3) * PART_SRC + (k ? 8 : H * 4 + wm) * 1024;
          v[k][j] = __builtin_amdgcn_raw_buffer_load_b128(rsWs, lane * 16 + (k ? off1 : 0), so, SC1);
          mx = max(max(mx, max(v[k][j].x, v[k][j].y)), max(v[k][j].z, v[k][j].w)); }
      if (LIKELY(__all(mx != ~0u))) break;
      if (sp > SPIN_MAX) { set_flag(P.ws, W_TMO, 1u); break; }
      __builtin_amdgcn_s_sleep(FIN_POLL);
    }
#pragma unroll
    for (int k = 0; k < NK; ++k)
#pragma unroll
      for (int it = 0; it < 2; ++it) { f32x2 a = lo2(kp[k][it]) + unpk(xr[k][it].x), b = hi2(kp[k][it]) + unpk(xr[k][it].y);
#pragma unroll
        for (int j = 0; j < 3; ++j) { a += unpk(v[k][j][2 * it]); b += unpk(v[k][j][2 * it + 1]); }
        __builtin_amdgcn_raw_buffer_store_b64((u32x2){pk2v(a), pk2v(b)}, rsOut, ro[it] + (k ? off1 : 0), cb[k], 0); }
  }
  if constexpr (H == 0) asm volatile(".Lmk_end:");
}

__global__ __launch_bounds__(512, 2) void fused(const u16* x, const u16* wqkv, const u16* wo, const u16* win, const u16* wsp, const u16* bsp, const u16* wout, u16* out, char* ws) {
  extern __shared__ __attribute__((aligned(16))) char smem[];
  LAS char* lds = (LAS char*)smem;
  const int lane = threadIdx.x & 63, wid = __builtin_amdgcn_readfirstlane(threadIdx.x >> 6);
  const int b = blockIdx.x, xc = b & 7, q = b >> 3;
  const int pi = 8 * (xc & 1) + (q & 7), pj = 4 * (xc >> 1) + (q >> 3);
  const unsigned epoch = *(const __attribute__((address_space(4))) unsigned*)(ws + W_EPOCH);
  const Ptrs P{x, wqkv, wo, win, wsp, bsp, wout, out, ws};
  unsigned long long pc; int clen;
  asm volatile("s_getpc_b64 %0\n.Lmk_pc:\n\ts_mov_b32 %1, .Lmk_end-.Lmk_pc" : "=s"(pc), "=s"(clen));
  const rsrc_t rsCode = mkrs((const void*)pc, clen);
  if (wid < 4) body<0>(P, lds, wid, lane, pi, pj, epoch, rsCode); else body<1>(P, lds, wid - 4, lane, pi, pj, epoch, rsCode);
}

template <int MODE> __global__ __launch_bounds__(512) void prefix(const u32x4* a0, const u32x4* a1, const u32x4* a2, const u32x4* a3, const u32x4* a4, const u32x4* a5, const u32x4* ws, const u32x4* out, unsigned* sink) {
  const unsigned long long t0 = __builtin_amdgcn_s_memrealtime();
  unsigned s = 0; const int t = blockIdx.x * 512 + threadIdx.x, T = 256 * 512;
  if (MODE & 1) { const u32x4* p[6] = {a0, a1, a2, a3, a4, a5}; const int n[6] = {S * D / 8, D * 3 * D / 8, D * D / 8, D * 2 * FH / 8, S * S / 8, FH * D / 8};
#pragma unroll
    for (int k = 0; k < 6; ++k) for (int i = t; i < n[k]; i += T) s += p[k][i].x; }
  if (MODE & 2) { for (int i = threadIdx.x; i < 800; i += 512) s += ws[(size_t)i * 4096 + (blockIdx.x & 63) * 8].x; if (threadIdx.x < 36) s += out[threadIdx.x * 4096].x; }
  if (MODE & 8) { unsigned long long pc; int back, len;
    asm volatile("s_getpc_b64 %0\n.Lpf_pc:\n\ts_mov_b32 %1, .Lpf_pc-.Lmk_pc\n\ts_mov_b32 %2, .Lmk_end-.Lmk_pc" : "=s"(pc), "=s"(back), "=s"(len));
    const char* c = (const char*)(pc - back); for (int i = threadIdx.x * 128; i < len; i += 512 * 128) s += *(const unsigned*)(c + (i & ~3)); }
  if (MODE & 16) { u32x4* w = (u32x4*)ws; for (int i = t; i < (48 << 20) / 16; i += T) w[(2 << 20) / 16 + i] = (u32x4){1, 2, 3, 4}; }
  if (MODE & 4) { f32x4 c = {0, 0, 0, 0}; bf16x8 a = {1, 2, 3, 4, 5, 6, 7, 8};
    while (__builtin_amdgcn_s_memrealtime() - t0 < 2000) { for (int i = 0; i < 16; ++i) c = mfma(a, a, c); } s += __float_as_uint(c.x); }
  while (__builtin_amdgcn_s_memrealtime() - t0 < 2000) __builtin_amdgcn_s_sleep(1);
  if (s == 0x12345678) *sink = s;
}
}

extern "C" void kernel_launch(void* const* d_in, const int* in_sizes, int n_in, void* d_out, int out_size, void* d_ws, size_t ws_size, hipStream_t stream) {
  using mk::u16;
  (void)hipFuncSetAttribute((const void*)mk::fused, hipFuncAttributeMaxDynamicSharedMemorySize, mk::LDS_TOTAL);
  mk::prefix<8><<<256, 512, 0, stream>>>((const mk::u32x4*)d_in[0], (const mk::u32x4*)d_in[1], (const mk::u32x4*)d_in[2], (const mk::u32x4*)d_in[4], (const mk::u32x4*)d_in[5], (const mk::u32x4*)d_in[7], (const mk::u32x4*)d_ws, (const mk::u32x4*)d_out, (unsigned*)((char*)d_ws + (100 << 20)));
  mk::fused<<<256, 512, mk::LDS_TOTAL, stream>>>((const u16*)d_in[0], (const u16*)d_in[1], (const u16*)d_in[2], (const u16*)d_in[4], (const u16*)d_in[5], (const u16*)d_in[6],
                                                 (const u16*)d_in[7], (u16*)d_out, (char*)d_ws);
}
```

```cpp
#include <hip/hip_runtime.h>
#include <hip/hip_bf16.h>
#include <type_traits>

namespace mk {
typedef unsigned short u16;
typedef short bf16x8 __attribute__((ext_vector_type(8)));
typedef short s16x4 __attribute__((ext_vector_type(4)));
typedef float f32x4 __attribute__((ext_vector_type(4)));
typedef float f32x2 __attribute__((ext_vector_type(2)));
typedef unsigned u32x4 __attribute__((ext_vector_type(4)));
typedef unsigned u32x2 __attribute__((ext_vector_type(2)));
typedef __bf16 bf16x2_t __attribute__((ext_vector_type(2)));
#define LAS __attribute__((address_space(3)))
#define GAS __attribute__((address_space(1)))
#define DI __device__ __forceinline__
template <int V> using IC = std::integral_constant<int, V>;
template <int B, int E, class F> __device__ __forceinline__ void sfor(F&& f) { if constexpr (B < E) { f(IC<B>{}); sfor<B + 1, E>(f); } }

constexpr int S = 2048, D = 576, FH = 2304;
constexpr int C_SZ = 18432, R_OFF = 73728, R_SZ = 16384, MISC = 139264;
constexpr int L_STASH = R_OFF, L_KVS = R_OFF + 36864;
constexpr int L_ZPART = MISC + 4096, L_KVF = L_ZPART + 2048, L_XSTAT = L_KVF + 6144, L_CS = L_XSTAT + 1024, L_TAB = L_CS + 1792;
constexpr int L_LAND0 = MISC;
constexpr int L_BIAS = L_TAB + 3072, L_FLG = L_BIAS + 256, L_DUMMY = L_FLG + 1024, L_STAMP = L_DUMMY + 1024;
constexpr int L_YOWN = MISC;
static_assert(L_YOWN + 16384 <= L_BIAS, "lds");
constexpr int LDS_TOTAL = 163840;
static_assert(L_STAMP + 512 <= LDS_TOTAL, "lds");
constexpr int W_EPOCH = 0, W_TMO = 256, W_STAMP = 32768, W_COL = 2 << 20, W_ROW = 18 << 20, W_END = 50 << 20;
constexpr int COL_SZ = 1 << 20, C_ZF = 0, C_KVP = 0x90000, C_CSG = 0xA0000, C_FLAGB = 0xB0000;
constexpr int ROW_SZ = 2 << 20, C_YF = 0, C_PART = 0xC0000, C_STATS = 0x150000, C_FLAGO = 0x158000, C_FLAGD = 0x158100;
constexpr int KVP_STRIDE = 2944, YF_GRP = 12 * 16384, PART_SRC = 9216, OOB = 0x40000000;
constexpr int SC1 = 16;
constexpr unsigned SPIN_MAX = 1u << 16;
#define FIN_SLEEP 16
#define FIN_POLL 4
#define ABL 0
constexpr int vpre1(int sg) { return sg == 10 ? 1 : 0; }
constexpr int vpost1(int sg) { return sg == 1 ? 2 : (sg >= 2 && sg <= 7) || sg == 10 || sg == 24 ? 1 : sg == 12 || sg == 13 ? 4 : sg == 17 ? 3 : sg == 18 ? 6 : sg == 19 ? 2 : sg == 27 + 14 || sg == 27 + 15 ? 3 : 0; }
constexpr int allow1(int sg) { return 5 + (sg == 25 ? 0 : vpost1(sg - 1)) + vpre1(sg) + vpost1(sg); }

template <int N> DI void waitv() { asm volatile("s_waitcnt vmcnt(%0)" ::"n"(N) : "memory"); }
DI void waitl0() { __builtin_amdgcn_s_waitcnt(0xC07F); asm volatile("" ::: "memory"); }
DI void bar() { asm volatile("" ::: "memory"); __builtin_amdgcn_s_barrier(); asm volatile("" ::: "memory"); }
#define SB() __builtin_amdgcn_sched_barrier(0)
DI f32x4 mfma(bf16x8 a, bf16x8 b, f32x4 c) { return __builtin_amdgcn_mfma_f32_16x16x32_bf16(a, b, c, 0, 0, 0); }
DI void mfa(f32x4& c, const bf16x8& a, const bf16x8& b) { asm volatile("v_mfma_f32_16x16x32_bf16 %0, %1, %2, %0" : "+v"(c) : "v"(a), "v"(b)); }
DI void mf0(f32x4& c, const bf16x8& a, const bf16x8& b) { asm volatile("v_mfma_f32_16x16x32_bf16 %0, %1, %2, 0" : "=&v"(c) : "v"(a), "v"(b)); }
DI int opq(int v) { asm volatile("" : "+v"(v)); return v; }
template <int N> DI void accfence(f32x4 (&a)[N][2]) {
  if constexpr (N == 5) asm volatile("s_nop 7\n\ts_nop 1" : "+v"(a[0][0]), "+v"(a[0][1]), "+v"(a[1][0]), "+v"(a[1][1]), "+v"(a[2][0]), "+v"(a[2][1]), "+v"(a[3][0]), "+v"(a[3][1]), "+v"(a[4][0]), "+v"(a[4][1]));
  else asm volatile("s_nop 7\n\ts_nop 1" : "+v"(a[0][0]), "+v"(a[0][1]), "+v"(a[1][0]), "+v"(a[1][1]), "+v"(a[2][0]), "+v"(a[2][1]), "+v"(a[3][0]), "+v"(a[3][1]));
}
DI unsigned pk2(float lo, float hi) { f32x2 v = {lo, hi}; return __builtin_bit_cast(unsigned, __builtin_convertvector(v, bf16x2_t)); }
DI float blo(unsigned u) { return __uint_as_float(u << 16); }
DI float dot2(unsigned a, unsigned b, float c) { return __builtin_amdgcn_fdot2_f32_bf16(__builtin_bit_cast(bf16x2_t, a), __builtin_bit_cast(bf16x2_t, b), c, false); }
DI f32x2 gelu2(f32x2 v) {
  const f32x2 a = v * __builtin_elementwise_fma(v * v, (f32x2){0.10294324f, 0.10294324f}, (f32x2){2.3022082f, 2.3022082f});
  const f32x2 d = (f32x2){__builtin_amdgcn_exp2f(a.x), __builtin_amdgcn_exp2f(a.y)} + 1.0f;
  const f32x2 r = {__builtin_amdgcn_rcpf(d.x), __builtin_amdgcn_rcpf(d.y)};
  return __builtin_elementwise_fma(-v, r, v);
}
DI unsigned pk2v(f32x2 v) { return __builtin_bit_cast(unsigned, __builtin_convertvector(v, bf16x2_t)); }
DI f32x2 unpk(unsigned u) { return (f32x2){__uint_as_float(u << 16), __uint_as_float(u & 0xffff0000u)}; }
DI f32x2 lo2(f32x4 a) { return (f32x2){a.x, a.y}; }
DI f32x2 hi2(f32x4 a) { return (f32x2){a.z, a.w}; }
DI f32x2 fma2(f32x2 a, f32x2 b, f32x2 c) { return __builtin_elementwise_fma(a, b, c); }
DI f32x2 phi2(f32x2 t) {
  const f32x2 a = t * 1.44269504f, p = t + 1.0f;
  const f32x2 r = {t.x > 0.f ? p.x : __builtin_amdgcn_exp2f(a.x), t.y > 0.f ? p.y : __builtin_amdgcn_exp2f(a.y)};
  return r + 1e-6f;
}
DI float hsum32(float v) { const unsigned u = __float_as_uint(v); const auto r = __builtin_amdgcn_permlane32_swap(u, u, false, false); return __uint_as_float(r[0]) + __uint_as_float(r[1]); }
DI float hsum16(float v) { const unsigned u = __float_as_uint(v); const auto r = __builtin_amdgcn_permlane16_swap(u, u, false, false); return __uint_as_float(r[0]) + __uint_as_float(r[1]); }
template <int N> DI float bcast16(float v) { return __builtin_bit_cast(float, __builtin_amdgcn_update_dpp(0, __builtin_bit_cast(int, v), 0x150 + N, 0xf, 0xf, true)); }
DI float ror8sum(float v) { return v + __builtin_bit_cast(float, __builtin_amdgcn_update_dpp(0, __builtin_bit_cast(int, v), 0x128, 0xf, 0xf, true)); }
DI float gsum(float v) { return hsum32(hsum16(v)); }
typedef __amdgpu_buffer_rsrc_t rsrc_t;
DI rsrc_t mkrs(const void* p, int bytes) { return __builtin_amdgcn_make_buffer_rsrc((void*)p, 0, bytes, 0x00020000); }
template <int AUX> DI void dma16(rsrc_t rs, LAS char* dst, int voff, int soff) {
  const unsigned m = (unsigned)(size_t)dst;
  if constexpr (AUX == 0) asm volatile("s_mov_b32 m0, %0\n\ts_nop 0\n\tbuffer_load_dwordx4 %1, %2, %3 offen lds" ::"s"(m), "v"(voff), "s"(rs), "s"(soff) : "memory");
  else asm volatile("s_mov_b32 m0, %0\n\ts_nop 0\n\tbuffer_load_dwordx4 %1, %2, %3 offen sc1 lds" ::"s"(m), "v"(voff), "s"(rs), "s"(soff) : "memory");
}
DI void dma4(rsrc_t rs, LAS char* dst, int voff, int soff) {
  const unsigned m = (unsigned)(size_t)dst;
  asm volatile("s_mov_b32 m0, %0\n\ts_nop 3\n\tbuffer_load_dword %1, %2, %3 offen sc1 lds" ::"s"(m), "v"(voff), "s"(rs), "s"(soff) : "memory");
}
template <int AUX> DI void st16(u32x4 v, rsrc_t rs, int voff, int soff) { asm volatile("" : "+v"(v)); __builtin_amdgcn_raw_buffer_store_b128(v, rs, voff, soff, AUX); asm volatile("s_nop 1" ::"v"(v) : "memory"); }
#define LIKELY(x) __builtin_expect(!!(x), 1)
#define UNLIKELY(x) __builtin_expect(!!(x), 0)
#define HLOAD(dst, voff, rs, soff) asm volatile("buffer_load_dwordx4 %0, %1, %2, %3 offen sc1 ; HIDDEN_LOAD" : "=v"(dst) : "v"(voff), "s"(rs), "s"(soff) : "memory")
DI s16x4 tr1(LAS char* a) { return __builtin_amdgcn_ds_read_tr16_b64_v4i16((LAS s16x4*)a); }
DI bf16x8 cat4(s16x4 a, s16x4 b) { return __builtin_shufflevector(a, b, 0, 1, 2, 3, 4, 5, 6, 7); }
DI bf16x8 cat(u32x2 a, u32x2 b) { u32x4 v = {a.x, a.y, b.x, b.y}; return __builtin_bit_cast(bf16x8, v); }

struct Ptrs { const u16 *x, *wqkv, *wo, *win, *wsp, *bsp, *wout; u16* out; char* ws; };

#define STAMP(k)

DI void poll_flags(char* ws, int off, bool active, unsigned tag) {
  GAS unsigned* p = (GAS unsigned*)(ws + off);
  _Pragma("unroll 1") for (unsigned sp = 0;; ++sp) {
    const unsigned v = __hip_atomic_load(p, __ATOMIC_RELAXED, __HIP_MEMORY_SCOPE_AGENT);
    if (__all(!active || v == tag)) break;
    if (sp > SPIN_MAX) { __hip_atomic_store((GAS unsigned*)(ws + W_TMO), 1u, __ATOMIC_RELAXED, __HIP_MEMORY_SCOPE_AGENT); break; }
    __builtin_amdgcn_s_sleep(2);
  }
  asm volatile("" ::: "memory");
}
DI void set_flag(char* ws, int off, unsigned tag) { __hip_atomic_store((GAS unsigned*)(ws + off), tag, __ATOMIC_RELAXED, __HIP_MEMORY_SCOPE_AGENT); }

template <int H>
DI void body(const Ptrs& P, LAS char* lds, const int wm, const int lane0, const int pi, const int pj, unsigned epoch, const rsrc_t rsCode) {
  constexpr int NF = H ? 4 : 5, NB = H ? 5 : 0;
  int lane = lane0, fr = lane & 15, g = lane >> 4;
  auto relane = [&]() { lane = opq(lane0); fr = lane & 15; g = lane >> 4; };
  const int gc = pj >> 2, gu = pj & 3, qo = 4 * (pj & 1);
  const int rot = (pi + 2 * pj) % 9;
  const rsrc_t rsX = mkrs(P.x + (size_t)pi * 128 * D, 128 * D * 2), rsWin = mkrs(P.win, D * 2 * FH * 2), rsQkv = mkrs(P.wqkv, D * 3 * D * 2);
  const rsrc_t rsSp = mkrs(P.wsp + (size_t)pi * 128 * S, 128 * S * 2), rsWs = mkrs(P.ws, W_END);
  const rsrc_t rsWout = mkrs(P.wout, FH * D * 2), rsWo = mkrs(P.wo, D * D * 2);
  const int colb = W_COL + pj * COL_SZ, rowb = W_ROW + pi * ROW_SZ, yb = rowb + C_YF + gc * YF_GRP;
  STAMP(0);

  auto rvoff = [&](int stride) { const int l = opq(lane0); return (8 * (l >> 5) + ((l >> 3) & 3)) * stride + ((l & 7) ^ (l >> 4)) * 16; };
  int vA[5], vB[5];
  auto cvoff = [&](int (&v)[5], int kind, int i0 = 0, int i1 = 5) {
    const int l = opq(lane0);
    const int t = kind == 0 ? pj : gu, b0 = 72 * pj - 8 * (pj & 1);
    const int a0 = 256 * t, a1 = (kind == 0 ? 4096 : 1024) + 32 * t - 128, a2 = 256 * t - 32;
#pragma unroll
    for (int idx = i0; idx < i1; ++idx) {
      const unsigned n = 64 * (wm + 4 * idx) + l, Pp = __umul24(n, 3641u) >> 16, cc = n - __umul24(Pp, 18u);
      unsigned k; int add;
      if (kind == 2) k = (Pp & ~24u) | ((Pp & 8u) << 1) | ((Pp & 16u) >> 1); else k = (Pp & ~12u) | ((Pp & 4u) << 1) | ((Pp & 8u) >> 1);
      if (kind != 1) { add = a0; add = cc >= 8 ? a1 : add; add = cc >= 10 ? a2 : add; }
      else { add = 1152; add = cc >= 4 ? 2304 - 64 : add; add = cc >= 8 ? 1152 - 64 : add; add = cc >= 9 ? 2304 - 80 : add; add = cc >= 10 ? -160 : add; add = cc >= 15 ? OOB : add; add += b0; }
      v[idx] = (int)(__umul24(k, kind == 0 ? 9216u : kind == 1 ? 3456u : 1152u) + 16u * cc) + add;
      if (idx == 4) v[idx] = wm >= 2 ? OOB : v[idx];
      asm volatile("" : "+v"(v[idx]));
    }
  };
  auto ksl = [&](int s) { int k = rot + s; return k >= 9 ? k - 9 : k; };
  auto kord = [&](int p) { return p < 2 ? 2 * pi + p : (p - 2 < 2 * pi ? p - 2 : p); };
  auto g3stage = [&](int q) { return q < 3 ? q : q == 11 ? 3 : 4 + ((2 * gu + (q - 3)) & 7); };
  auto ring = [&](int st, int idx, int dyn = 0) {
    const int slot = st & 3;
    if constexpr (H == 1) {
      LAS char* dst = lds + ((idx == 4 && wm >= 2) ? L_DUMMY : slot * C_SZ + (wm + 4 * idx) * 1024);
      if (st < 9) dma16<0>(rsWin, dst, vA[idx], 4608 + ksl(st) * (64 * 9216));
      else if (st < 18) dma16<0>(rsQkv, dst, vB[idx], ksl(st - 9) * (64 * 3456));
      else if (st < 27) dma16<0>(rsWin, dst, vA[idx], ksl(st - 18) * (64 * 9216));
      else if (st < 59) dma16<SC1>(rsWs, dst, vB[idx == 4], colb + C_ZF + kord(dyn) * C_SZ + (wm + 4 * idx) * 1024);
      else { const int q = st - 59;
        if (q < 3) dma16<0>(rsWo, dst, vA[idx], (144 * gc + 64 * q) * 1152);
        else if (q == 11) dma16<0>(rsWout, dst, vA[idx], (2048 + 64 * gc) * 1152);
        else dma16<0>(rsWout, dst, vA[idx], (512 * gc + 64 * (g3stage(q) - 4)) * 1152); }
    } else {
      if (idx == 4) return;
      LAS char* dst = lds + R_OFF + slot * R_SZ + (4 * wm + idx) * 1024;
      const int rb = 32 * wm + 16 * (idx & 1) + 4 * (idx >> 1);
      if (st < 9) dma16<0>(rsX, dst, vA[idx & 1], rb * 1152 + ksl(st) * 128);
      else if (st < 27) return;
      else if (st < 59) dma16<0>(rsSp, dst, vA[idx & 1], rb * 4096 + kord(dyn) * 128);
      else dma16<SC1>(rsWs, dst, vB[0], yb + g3stage(st - 59) * 16384 + (4 * wm + idx) * 1024);
    }
  };
  auto ring_all = [&](int st) { ring(st, 4); ring(st, 0); ring(st, 1); ring(st, 2); ring(st, 3); };
  auto ring_pos = [&](int st, int k, int nf) {
    const int dyn = st - 27;
    if (nf == 5) { ring(st, k == 0 ? 4 : k - 1, dyn); }
    else if (nf == 4) { if (k == 0) ring(st, 4, dyn); ring(st, k, dyn); }
    else { if (k == 0) { ring(st, 4, dyn); ring(st, 0, dyn); } else if (k == 1) { ring(st, 1, dyn); ring(st, 2, dyn); } else ring(st, 3, dyn); }
  };
  if constexpr (H == 1) cvoff(vA, 0); else { vA[0] = rvoff(1152); vA[1] = vA[0] ^ 64; }
  auto codewarm = [&]() { if (wm == 3) dma4(rsCode, lds + L_DUMMY, opq(lane0) * 128, ((blockIdx.x >> 3) & 15) * 8192); };
  if constexpr (H == 0) codewarm();
  ring_all(0); ring_all(1);
  STAMP(17);
  if constexpr (H == 1) cvoff(vB, 1);
  else {
    const int t = wm * 64 + lane;
    const int rd = (t * 205) >> 10, e0 = 4 * (t - 5 * rd), hd = rd >= 18, dc = qo + rd;
    const int gb = (dc >> 4) * 3072 - (dc >> 5) * 1024 + ((dc >> 2) & 3) * 256 + (dc & 3) * 4, sb = (dc >> 5) * 1024 + ((dc >> 2) & 3) * 256 + ((dc >> 4) & 1) * 8 + (dc & 3) * 2;
    unsigned go[4], so[4];
#pragma unroll
    for (int k = 0; k < 4; ++k) { const int e = e0 + k, ce = qo + 18 * hd + e; int off = (ce >> 4) * 1024 + (ce & 15) * 16; off = ce >= 32 ? off + 128 : off; off = e >= 18 ? 2048 : off;
      int ep = 18 * hd + e; ep = e == 18 ? 36 + hd : ep; ep = e == 19 ? 40 : ep;
      go[k] = gb + off; so[k] = sb + (ep >> 4) * 2048 + (ep & 15) * 16; }
    if (t < 180) *(LAS u32x4*)(lds + L_TAB + t * 16) = (u32x4){go[0] | (go[1] << 16), go[2] | (go[3] << 16), so[0] | (so[1] << 16), so[2] | (so[3] << 16)};
  }
  LAS char *aR0, *aR1, *aC0, *aC1;
  auto mk_aR = [&]() { const int l = opq(lane0), r = l & 15, o = (32 * wm + r) * 128 + (((l >> 4) ^ ((r >> 1) & 7)) * 16); aR0 = lds + R_OFF + o; aR1 = lds + R_OFF + (o ^ 64); };
  auto mk_aC = [&]() { const int l = opq(lane0); aC0 = lds + (16 * (l >> 5) + 4 * ((l >> 4) & 1) + ((l & 15) >> 2)) * 288 + (l & 3) * 8 + NB * 32; aC1 = aC0 + 32 * 288; asm volatile("" : "+v"(aC1)); };
  asm volatile("" : "+s"(epoch));
  const unsigned tag = epoch * 0x9E3779B1u + 0x7F4A7C15u;
  unsigned ONES = 0x3f803f80u; asm volatile("" : "+s"(ONES));
  auto raise = [&](int off, bool on = true) { __builtin_amdgcn_raw_buffer_store_b32(tag, rsWs, (opq(lane0) == 0 && wm == 0 && on) ? 0 : OOB, off, SC1); };
  auto flags_dma = [&](int voff, int soff) { dma4(rsWs, lds + L_FLG + wm * 256, voff, soff); };
  auto check_flags = [&](int off) { const unsigned v = *(LAS unsigned*)(lds + L_FLG + wm * 256 + opq(lane0) * 4);
    if (UNLIKELY(!__all(v == tag))) { STAMP(18); poll_flags(P.ws, off, true, tag); } };

  bf16x8 xf[9][2][2];
  f32x4 acc[NF][2];
  f32x4 zm[2], zr2[2];
  auto z2_read = [&](int n) { const int l = opq(lane0), r = l & 15, q = l >> 4;
    return *(LAS u32x4*)(lds + L_STASH + wm * 9216 + n * 1024 + r * 64 + (((q >> 1) ^ ((r >> 2) & 1)) * 32) + (q & 1) * 16); };
  auto z2n_tile = [&](int n, u32x4 z) {
    const u32x4 o = {pk2v(fma2(unpk(z.x), lo2(zr2[0]), lo2(zm[0]))), pk2v(fma2(unpk(z.y), hi2(zr2[0]), hi2(zm[0]))), pk2v(fma2(unpk(z.z), lo2(zr2[1]), lo2(zm[1]))), pk2v(fma2(unpk(z.w), hi2(zr2[1]), hi2(zm[1])))};
    st16<SC1>(o, rsWs, opq(lane0) * 16, colb + C_ZF + (2 * pi + (wm >> 1)) * C_SZ + ((wm & 1) * 9 + n) * 1024); };

  auto pass = [&](auto PASSc) {
    constexpr int PASS = decltype(PASSc)::value;
    constexpr int NFP = (PASS == 1 && H == 1) ? 3 : NF, ST0 = 9 * PASS, NU = 9 * NFP;
    constexpr bool TR = (PASS == 2) || (PASS == 1 && H == 1);
    constexpr int L_LAND = PASS == 0 ? L_LAND0 : PASS == 1 ? L_KVS : R_OFF + 2 * R_SZ;
    const int csg = colb + C_CSG + PASS * 12288;
    s16x4 bq[3][4];
    u32x4 gl[8]; u32x2 zp[2], t2[9]; float tf[16]; f32x4 ct, xs1[2], xs2[2]; float csv = 0.f, cs5 = 0.f;
    auto rdB = [&](int u, int j) { const int s = u / NFP, n = u % NFP; if (u < NU) bq[u % 3][j] = tr1(((j & 2) ? aC1 : aC0) + ((ST0 + s) & 3) * C_SZ + n * 32 + (j & 1) * (8 * 288)); };
    auto rdX = [&](int s, int j) { xf[s][j >> 1][j & 1] = *(LAS bf16x8*)(((j & 1) ? aR1 : aR0) + (s & 3) * R_SZ + (j >> 1) * 2048); };
    auto slot = [&](auto SGc) __attribute__((always_inline)) {
      constexpr int sg = decltype(SGc)::value, s = sg - ST0;
      if (s == 1) { const int l = opq(lane0); const bool on = wm == 0 && pi < 9;
        __builtin_amdgcn_raw_buffer_store_b64((u32x2){__float_as_uint(csv), tag}, rsWs, on ? (NB * 16 + l) * 8 : OOB, csg + rot * 1152, SC1);
        if constexpr (H == 0) __builtin_amdgcn_raw_buffer_store_b64((u32x2){__float_as_uint(cs5), tag}, rsWs, (on && l < 16) ? (64 + l) * 8 : OOB, csg + rot * 1152, SC1); }
      if constexpr (H == 1) {
        if (sg >= 1 && sg <= 7) { const int k = (sg - 1) * 4 + wm, src = k / 9;
          st16<SC1>((u32x4){~0u, ~0u, ~0u, ~0u}, rsWs, (k < 27 && !(ABL & 1)) ? opq(lane0) * 16 : OOB, rowb + C_PART + (pj * 4 + ((gc + 1 + src) & 3)) * PART_SRC + (k - 9 * src) * 1024); }
        if (sg == 8) {
          asm volatile("s_nop 7" : "+v"(xs1[0]), "+v"(xs2[0]), "+v"(xs1[1]), "+v"(xs2[1]));
          const int l = opq(lane0), r = l & 15, j = r & 3;
          LAS char* const o = lds + ((r >> 2) == (l >> 4) ? L_XSTAT + (32 * wm + r) * 4 : L_DUMMY);
#pragma unroll
          for (int it = 0; it < 2; ++it) { float s1 = xs1[it].x, s2 = xs2[it].x;
            s1 = j == 1 ? xs1[it].y : s1; s2 = j == 1 ? xs2[it].y : s2; s1 = j == 2 ? xs1[it].z : s1; s2 = j == 2 ? xs2[it].z : s2; s1 = j == 3 ? xs1[it].w : s1; s2 = j == 3 ? xs2[it].w : s2;
            const float mean = s1 * (1.0f / D), var = fmaxf(s2 * (1.0f / D) - mean * mean, 0.f);
            *(LAS float*)(o + it * 64) = -mean; *(LAS float*)(o + it * 64 + 512) = __builtin_amdgcn_rsqf(var + 1e-5f); } }
        if (s == 7) { const int t = min(wm * 64 + opq(lane0), 143); unsigned bad = 0;
          auto f = [&](int j) { return __uint_as_float(t2[j].x); };
          float c = (((f(0) + f(1)) + (f(2) + f(3))) + ((f(4) + f(5)) + (f(6) + f(7)))) + f(8);
#pragma unroll
          for (int j = 0; j < 9; ++j) bad |= t2[j].y ^ tag;
          if (UNLIKELY(!__all(bad == 0))) {
            _Pragma("unroll 1") for (unsigned sp = 0; !__all(bad == 0); ++sp) {
              STAMP(20);
              if (sp > SPIN_MAX) { set_flag(P.ws, W_TMO, 1u); break; }
              bad = 0;
#pragma unroll
              for (int j = 0; j < 9; ++j) { t2[j] = __builtin_amdgcn_raw_buffer_load_b64(rsWs, t * 8, csg + j * 1152, SC1); bad |= t2[j].y ^ tag; }
              c = (((f(0) + f(1)) + (f(2) + f(3))) + ((f(4) + f(5)) + (f(6) + f(7)))) + f(8);
            } }
          *(LAS float*)(lds + L_CS + PASS * 576 + t * 4) = c; }
        auto svo = [&]() { const int l = opq(lane0); return ((l >> 3) & 1) * 16384 + (32 * wm + 8 * (l >> 4) + (l & 7)) * 16; };
        if (sg == 12 || sg == 13) { const int vo = svo();
#pragma unroll
          for (int j = 4 * (sg - 12); j < 4 * (sg - 11); ++j) HLOAD(gl[j], vo, rsWs, rowb + C_STATS + j * 2048); }
        if (sg == 15) {
          asm volatile("s_waitcnt vmcnt(10) ; HIDDEN_WAIT %0 %1 %2 %3 %4 %5 %6 %7" : "+v"(gl[0]), "+v"(gl[1]), "+v"(gl[2]), "+v"(gl[3]), "+v"(gl[4]), "+v"(gl[5]), "+v"(gl[6]), "+v"(gl[7]) :: "memory");
          float s1, s2; unsigned bad;
          auto red = [&]() { auto a = [&](int j) { return __uint_as_float(gl[j].x); }; auto b = [&](int j) { return __uint_as_float(gl[j].z); }; bad = 0;
            s1 = ((a(0) + a(1)) + (a(2) + a(3))) + ((a(4) + a(5)) + (a(6) + a(7))); s2 = ((b(0) + b(1)) + (b(2) + b(3))) + ((b(4) + b(5)) + (b(6) + b(7)));
#pragma unroll
            for (int j = 0; j < 8; ++j) { bad |= gl[j].y ^ tag; bad |= gl[j].w ^ tag; } };
          red();
          if (UNLIKELY(!__all(bad == 0))) {
            _Pragma("unroll 1") for (unsigned sp = 0; !__all(bad == 0); ++sp) {
              STAMP(19);
              if (sp > SPIN_MAX) { set_flag(P.ws, W_TMO, 1u); break; }
              const int vo = svo();
#pragma unroll
              for (int j = 0; j < 8; ++j) gl[j] = __builtin_amdgcn_raw_buffer_load_b128(rsWs, vo, rowb + C_STATS + j * 2048, SC1);
              red();
            } }
          s1 = ror8sum(s1); s2 = ror8sum(s2);
          const float mu = s1 * (1.0f / FH), var = fmaxf(s2 * (1.0f / FH) - mu * mu, 0.f);
          tf[1] = __builtin_amdgcn_rsqf(var + 1e-5f); tf[0] = -mu * tf[1]; asm volatile("" : "+v"(tf[0]), "+v"(tf[1])); }
        if (sg == 16) {
          zm[0] = (f32x4){bcast16<0>(tf[0]), bcast16<1>(tf[0]), bcast16<2>(tf[0]), bcast16<3>(tf[0])}; zm[1] = (f32x4){bcast16<4>(tf[0]), bcast16<5>(tf[0]), bcast16<6>(tf[0]), bcast16<7>(tf[0])};
          zr2[0] = (f32x4){bcast16<0>(tf[1]), bcast16<1>(tf[1]), bcast16<2>(tf[1]), bcast16<3>(tf[1])}; zr2[1] = (f32x4){bcast16<4>(tf[1]), bcast16<5>(tf[1]), bcast16<6>(tf[1]), bcast16<7>(tf[1])};
          asm volatile("" : "+v"(zm[0]), "+v"(zm[1]), "+v"(zr2[0]), "+v"(zr2[1])); }
        if (sg == 17) { z2n_tile(0, gl[0]); z2n_tile(1, gl[1]); z2n_tile(2, gl[2]); }
        if (sg == 19) { const int t = wm * 64 + opq(lane0); u32x4 o;
#pragma unroll
          for (int k = 0; k < 4; ++k) o[k] = __float_as_uint((tf[4 * k] + tf[4 * k + 1]) + (tf[4 * k + 2] + tf[4 * k + 3]));
          st16<SC1>(o, rsWs, t < 180 ? t * 16 : OOB, colb + C_KVP + pi * KVP_STRIDE); }
        if (sg == 23) { vB[0] = opq(lane0) * 16; vB[1] = wm >= 2 ? OOB : vB[0]; }
        if (sg == 24) flags_dma((opq(lane0) & 15) * 4, colb + C_FLAGB);
        if (sg == 26) check_flags(colb + C_FLAGB + (lane0 & 15) * 4);
      }
    };
    auto front = [&](auto SGc) __attribute__((always_inline)) {
      constexpr int sg = decltype(SGc)::value, s = sg - ST0;
      if constexpr (H == 0) {
        if (s == 4) { const int vo = opq(lane0) * 16;
#pragma unroll
          for (int k = 0; k < 3; ++k) dma16<SC1>(rsWs, lds + L_LAND + (wm + 4 * k) * 1024, vo, csg + (wm + 4 * k) * 1024); }
        if (sg == 21) raise(colb + C_FLAGB + pi * 4);
        if (sg == 22) { vA[0] = rvoff(4096); vA[1] = vA[0] ^ 64; }
      } else {
        if (sg == 9) { const int r = wm * 64 + opq(lane0); const f32x2 a = __builtin_bit_cast(f32x2, zp[0]) + __builtin_bit_cast(f32x2, zp[1]);
          st16<SC1>((u32x4){__float_as_uint(a.x), tag, __float_as_uint(a.y), tag}, rsWs, r < 128 ? r * 16 : OOB, rowb + C_STATS + pj * 2048); }
        if (s == 6) { const int t = min(wm * 64 + opq(lane0), 143);
#pragma unroll
          for (int j = 0; j < 9; ++j) t2[j] = *(LAS u32x2*)(lds + L_LAND + j * 1152 + t * 8); }
        if (sg == 16) { gl[0] = z2_read(0); gl[1] = z2_read(1); gl[2] = z2_read(2); }
      }
    };
#pragma unroll
    for (int n = NFP; n < NF; ++n) { acc[n][0] = (f32x4){0, 0, 0, 0}; acc[n][1] = (f32x4){0, 0, 0, 0}; }
    mk_aC();
    if (PASS == 0) { mk_aR(); rdX(0, 0); rdX(0, 1); rdX(0, 2); rdX(0, 3); }
#pragma unroll
    for (int j = 0; j < 8; ++j) rdB(j >> 2, j & 3);
    if (PASS == 0) ring_all(2);
    bf16x8 ones8 = __builtin_bit_cast(bf16x8, (u32x4){ONES, ONES, ONES, ONES}); asm volatile("" : "+v"(ones8));
    const int gq = opq(lane0) >> 4;
    SB();
    sfor<0, NU>([&](auto Uc) __attribute__((always_inline)) {
      constexpr int u = decltype(Uc)::value, s = u / NFP, n = u % NFP, sg = ST0 + s, d = u - (NFP - 2);
      if (n == NFP - 2) {
        slot(IC<sg>{});
        if constexpr (H == 1) waitv<allow1(sg)>();
        else { if (sg == 1 || sg == 2) waitv<6>(); else if (sg == 5) waitv<7>(); else if (sg < 7 || sg == 26) waitv<4>(); else if (sg == 7 || sg == 15 || sg == 24) waitv<0>(); }
        if ((sg == 8 && H == 1) || (sg == 18 && H == 0)) waitl0();
        bar();
        if (H == 1 && sg == 9) { const int r = (wm * 64 + opq(lane0)) & 127; zp[0] = *(LAS u32x2*)(lds + L_ZPART + r * 8); zp[1] = *(LAS u32x2*)(lds + L_ZPART + 1024 + r * 8); }
        if (H == 1 && sg == 18) zp[0] = *(LAS u32x2*)(lds + L_TAB + min(wm * 64 + opq(lane0), 179) * 16);
        SB();
      }
      const bf16x8 b0 = cat4(bq[u % 3][0], bq[u % 3][1]), b1 = cat4(bq[u % 3][2], bq[u % 3][3]);
#pragma unroll
      for (int j = 0; j < 4; ++j) { const int it = j >> 1, hh = j & 1; const bf16x8 &bb = hh ? b1 : b0, &oa = TR ? bb : xf[s][it][hh], &ob = TR ? xf[s][it][hh] : bb;
        if (s == 0 && hh == 0) mf0(acc[n][it], oa, ob); else mfa(acc[n][it], oa, ob);
        SB();
        rdB(u + 2, j);
        if (PASS == 0 && n == NFP - 2 && s < 8) rdX(s + 1, j);
        if (j == 3) {
          if (u >= 1 && u <= NFP) { asm volatile("s_nop 0" : "+v"(ct)); if (u - 1 < 4) csv = gq == u - 1 ? ct.x : csv; else cs5 = ct.x; asm volatile("" : "+v"(csv), "+v"(cs5)); }
          if (n == NFP - 2) front(IC<sg>{});
          if (H == 1 && sg == 19 && n == 0) {
#pragma unroll
            for (int k = 0; k < 4; ++k) { LAS char* const a = lds + L_KVS + ((k & 1) ? zp[0][k >> 1] >> 16 : zp[0][k >> 1] & 0xffffu);
#pragma unroll
              for (int w = 0; w < 4; ++w) tf[4 * k + w] = *(LAS float*)(a + w * 8192); } }
          if constexpr (d >= 0) ring_pos(ST0 + d / NFP + 3, d % NFP, NFP); }
        SB(); }
      if (s == 0) { mf0(ct, ones8, b0); mfa(ct, ones8, b1); }
      if (PASS == 0 && H == 1 && n < 2 && (s == 0 || !(ABL & 2))) {
#pragma unroll
        for (int it = 0; it < 2; ++it) { f32x4& xs = n ? xs2[it] : xs1[it]; const bf16x8 &o0 = n ? xf[s][it][0] : ones8, &o1 = n ? xf[s][it][1] : ones8;
          if (s == 0) mf0(xs, xf[s][it][0], o0); else mfa(xs, xf[s][it][0], o0); mfa(xs, xf[s][it][1], o1); } }
      SB();
    });
#pragma unroll
    for (int k = 2; k < NFP; ++k) ring_pos(ST0 + 11, k, NFP);
    accfence(acc);
    relane();
  };
  auto csum1 = [&](int ps, int col) { return *(LAS float*)(lds + L_CS + ps * 576 + col * 4); };
  auto rowstat = [&](f32x2 (&nm)[4], f32x2 (&rs)[4]) {
#pragma unroll
    for (int it = 0; it < 2; ++it) { const f32x4 a = *(LAS f32x4*)(lds + L_XSTAT + (32 * wm + 16 * it + 4 * g) * 4), b = *(LAS f32x4*)(lds + L_XSTAT + 512 + (32 * wm + 16 * it + 4 * g) * 4);
      nm[2 * it] = lo2(a); nm[2 * it + 1] = hi2(a); rs[2 * it] = lo2(b); rs[2 * it + 1] = hi2(b); } };
  auto affN = [&](int n, float c, const f32x2 (&nm)[4], const f32x2 (&rs)[4], f32x2 (&v)[4]) { const f32x2 c2 = {c, c};
    v[0] = fma2(nm[0], c2, lo2(acc[n][0])) * rs[0]; v[1] = fma2(nm[1], c2, hi2(acc[n][0])) * rs[1]; v[2] = fma2(nm[2], c2, lo2(acc[n][1])) * rs[2]; v[3] = fma2(nm[3], c2, hi2(acc[n][1])) * rs[3]; };

  if constexpr (H == 1) { if (!(ABL & 4)) __builtin_amdgcn_s_setprio(1); waitv<5>(); } else waitv<4>();
  bar();
  STAMP(1);
  pass(IC<0>{});
  STAMP(2);
  if constexpr (H == 0) {
    dma4(mkrs(P.wsp, S * S * 2), lds + L_DUMMY, lane * 128, (blockIdx.x * 4 + wm) * 8192);
    if (wm == 0) dma4(mkrs(P.bsp, S * 2), lds + L_BIAS, lane * 4, pi * 256);
  }
  {
    f32x2 nm[4], rs[4]; rowstat(nm, rs);
    LAS char* const sw = lds + L_STASH + (wm * 9 + NB) * 1024 + fr * 64 + (((g >> 1) ^ ((fr >> 2) & 1)) * 32) + (g & 1) * 16;
    LAS char* const sr = lds + L_STASH + (wm * 9 + NB) * 1024 + (4 * g + (fr >> 2)) * 64 + (g & 1) * 32 + (fr & 3) * 8;
    LAS char* const sr1 = lds + L_STASH + (wm * 9 + NB) * 1024 + (4 * g + (fr >> 2)) * 64 + ((g & 1) ^ 1) * 32 + (fr & 3) * 8;
    float cn[NF];
#pragma unroll
    for (int n = 0; n < NF; ++n) cn[n] = csum1(0, (NB + n) * 16 + fr);
#pragma unroll
    for (int n = 0; n < NF; ++n) { f32x2 v[4]; affN(n, cn[n], nm, rs, v);
      *(LAS u32x4*)(sw + n * 1024) = (u32x4){pk2v(gelu2(v[0])), pk2v(gelu2(v[1])), pk2v(gelu2(v[2])), pk2v(gelu2(v[3]))}; }
    asm volatile("" ::: "memory");
    float s1[2] = {0.f, 0.f}, s2[2] = {0.f, 0.f};
#pragma unroll
    for (int n = 0; n < NF; ++n)
#pragma unroll
      for (int ih = 0; ih < 2; ++ih) { const u32x2 t = __builtin_bit_cast(u32x2, __builtin_amdgcn_ds_read_tr16_b64_v4i16((LAS s16x4*)((ih ? sr1 : sr) + n * 1024)));
        s1[ih] = dot2(t.x, ONES, s1[ih]); s2[ih] = dot2(t.x, t.x, s2[ih]); s1[ih] = dot2(t.y, ONES, s1[ih]); s2[ih] = dot2(t.y, t.y, s2[ih]); }
    const float a0 = gsum(s1[0]), b0 = gsum(s2[0]), a1 = gsum(s1[1]), b1 = gsum(s2[1]);
    if (g < 2) *(LAS f32x2*)(lds + L_ZPART + (H * 128 + 32 * wm + 16 * g + fr) * 8) = g ? (f32x2){a1, b1} : (f32x2){a0, b0};
    waitl0();
  }
  STAMP(3);
  pass(IC<1>{});
  STAMP(4);
  u32x2 pq[3][2];
  f32x4 aq[3][2];
  if constexpr (H == 0) {
        { const int l = opq(lane0), li = (wm & 1) * 64 + l;
          if (wm < 2) dma4(rsWout, lds + L_DUMMY, li < 81 ? li * 128 : OOB, blockIdx.x * 81 * 128);
          else if (wm == 2) dma4(rsWo, lds + L_DUMMY, l < 21 ? l * 128 : OOB, blockIdx.x * 21 * 128);
          codewarm(); }
    f32x2 nm[4], rs[4]; rowstat(nm, rs);
    float cn[5];
#pragma unroll
    for (int n = 0; n < 5; ++n) cn[n] = csum1(1, n * 16 + fr);
    *(LAS u32x4*)(lds + L_KVF + (wm * 64 + lane) * 16) = (u32x4){0, 0, 0, 0};
    *(LAS u32x2*)(lds + L_KVF + 4096 + (wm * 64 + lane) * 8) = (u32x2){0, 0};
    bf16x8 pkf[3], vf[3];
#pragma unroll
    for (int n = 0; n < 5; ++n) { f32x2 v[4]; affN(n, cn[n], nm, rs, v);
      if (n >= 2) { u32x4 w = {pk2v(v[0]), pk2v(v[1]), pk2v(v[2]), pk2v(v[3])};
        if (n == 4 && fr == 0) w = (u32x4){ONES, ONES, ONES, ONES};
        vf[n - 2] = __builtin_bit_cast(bf16x8, w); }
      if (n < 2 || n == 4) pkf[n == 4 ? 2 : n] = __builtin_bit_cast(bf16x8, (u32x4){pk2v(phi2(v[0])), pk2v(phi2(v[1])), pk2v(phi2(v[2])), pk2v(phi2(v[3]))});
    }
#pragma unroll
    for (int td = 0; td < 3; ++td)
#pragma unroll
      for (int te = 0; te < 3; ++te) { if (td == 2 && te == 0) continue;
        *(LAS f32x4*)(lds + L_KVS + (wm * 8 + td * 3 + te - (td == 2)) * 1024 + lane * 16) = mfma(pkf[td], vf[te], (f32x4){0, 0, 0, 0}); }
  } else {
    u32x4 z[6];
#pragma unroll
    for (int n = 0; n < 6; ++n) z[n] = z2_read(3 + n);
#pragma unroll
    for (int n = 0; n < 6; ++n) z2n_tile(3 + n, z[n]);
#pragma unroll
    for (int n = 0; n < 3; ++n) { aq[n][0] = acc[n][0]; aq[n][1] = acc[n][1]; }
  }
  STAMP(6);
  pass(IC<2>{});
  STAMP(7);
  u32x2 z1[NF][2];
  f32x4 ag[NF][2];
  {
    constexpr int NR = NF + 2;
    LAS char* const aZ = lds + NB * 1024 + lane * 16;
    mk_aR();
    if constexpr (H == 0) vB[0] = lane * 16;
    f32x4 kvs = {0, 0, 0, 0}; u32x4 kl[2][2]; int kvo;
    if constexpr (H == 0) { const int t = wm * 64 + lane; kvo = t < 180 ? t * 16 : OOB; }
    bf16x8 fa[2][NR], fb[2][NR];
    auto rd1 = [&](bf16x8 (&f)[2][NR], int i, int slot) {
      const int h = i & 1; int ii = i >> 1; if (ii == 1) ii = 2; else if (ii == 2) ii = 1;
      if (ii < 2) f[h][ii] = *(LAS bf16x8*)((h ? aR1 : aR0) + slot * R_SZ + ii * 2048); else f[h][ii] = *(LAS bf16x8*)(aZ + slot * C_SZ + (h * 9 + ii - 2) * 1024);
    };
    float zn[2], zr[2], za, zb, ta, tb; f32x2 zc;
    LAS char* const aCs = lds + L_CS + 576 + (NB * 16 + 4 * g) * 4;
#pragma unroll
    for (int it = 0; it < 2; ++it) { zn[it] = *(LAS float*)(lds + L_XSTAT + (32 * wm + 16 * it + fr) * 4); zr[it] = *(LAS float*)(lds + L_XSTAT + 512 + (32 * wm + 16 * it + fr) * 4); }
    float GC1 = 2.3022082f; asm volatile("" : "+v"(GC1));
    auto zop = [&](int e, int k) { const int n = e >> 2, it = (e >> 1) & 1, h = e & 1;
      if (k == 0) zc = *(LAS f32x2*)(aCs + 576 + n * 64 + h * 8);
      if (k == 3) asm volatile("v_fma_f32 %0, %2, %4, %5\n\tv_fma_f32 %1, %3, %4, %6" : "=&v"(za), "=&v"(zb) : "v"(zc.x), "v"(zc.y), "v"(zn[it]), "v"(acc[n][it][2 * h]), "v"(acc[n][it][2 * h + 1]));
      if (k == 4) asm volatile("v_mul_f32 %0, %0, %2\n\tv_mul_f32 %1, %1, %2" : "+v"(za), "+v"(zb) : "v"(zr[it]));
      if (k == 5) asm volatile("v_mul_f32 %0, %2, %2\n\tv_mul_f32 %1, %3, %3" : "=&v"(ta), "=&v"(tb) : "v"(za), "v"(zb));
      if (k == 6) asm volatile("v_fmamk_f32 %0, %0, 0x3dd2d3e8, %2\n\tv_fmamk_f32 %1, %1, 0x3dd2d3e8, %2" : "+v"(ta), "+v"(tb) : "v"(GC1));
      if (k == 7) asm volatile("v_mul_f32 %0, %0, %2\n\tv_mul_f32 %1, %1, %3" : "+v"(ta), "+v"(tb) : "v"(za), "v"(zb));
      if (k == 8) asm volatile("v_exp_f32 %0, %0" : "+v"(ta));
      if (k == 9) asm volatile("v_exp_f32 %0, %0" : "+v"(tb));
      if (k == 10) asm volatile("v_add_f32 %0, 1.0, %0\n\tv_add_f32 %1, 1.0, %1" : "+v"(ta), "+v"(tb));
      if (k == 11) asm volatile("v_rcp_f32 %0, %0" : "+v"(ta));
      if (k == 12) asm volatile("v_rcp_f32 %0, %0" : "+v"(tb));
      if (k == 13) asm volatile("v_fma_f32 %0, -%0, %2, %0\n\tv_fma_f32 %1, -%1, %3, %1" : "+v"(za), "+v"(zb) : "v"(ta), "v"(tb));
      if (k == 14) { unsigned o; asm volatile("v_cvt_pk_bf16_f32 %0, %1, %2" : "=v"(o) : "v"(za), "v"(zb)); z1[n][it][h] = o; } };
    auto qop = [&](int e, int k) { const int n = e >> 2, it = (e >> 1) & 1, h = e & 1;
      if (k == 0) zc = *(LAS f32x2*)(aCs + n * 64 + h * 8);
      if (k == 3) asm volatile("v_fma_f32 %0, %2, %4, %5\n\tv_fma_f32 %1, %3, %4, %6" : "=&v"(za), "=&v"(zb) : "v"(zc.x), "v"(zc.y), "v"(zn[it]), "v"(aq[n][it][2 * h]), "v"(aq[n][it][2 * h + 1]));
      if (k == 4) asm volatile("v_mul_f32 %0, %0, %2\n\tv_mul_f32 %1, %1, %2" : "+v"(za), "+v"(zb) : "v"(zr[it]));
      if (k == 5) asm volatile("v_min_f32 %0, 0, %2\n\tv_min_f32 %1, 0, %3" : "=&v"(ta), "=&v"(tb) : "v"(za), "v"(zb));
      if (k == 6) asm volatile("v_mul_f32 %0, 0x3fb8aa3b, %0\n\tv_mul_f32 %1, 0x3fb8aa3b, %1" : "+v"(ta), "+v"(tb));
      if (k == 7) asm volatile("v_exp_f32 %0, %0" : "+v"(ta));
      if (k == 8) asm volatile("v_exp_f32 %0, %0" : "+v"(tb));
      if (k == 9) asm volatile("v_max_f32 %0, 0, %0\n\tv_max_f32 %1, 0, %1" : "+v"(za), "+v"(zb));
      if (k == 10) asm volatile("v_add_f32 %0, %0, %2\n\tv_add_f32 %1, %1, %3" : "+v"(za), "+v"(zb) : "v"(ta), "v"(tb));
      if (k == 11) asm volatile("v_add_f32 %0, 0x358637bd, %0\n\tv_add_f32 %1, 0x358637bd, %1" : "+v"(za), "+v"(zb));
      if (k == 12) { unsigned o; asm volatile("v_cvt_pk_bf16_f32 %0, %1, %2" : "=v"(o) : "v"(za), "v"(zb)); pq[n][it][h] = o; } };
    auto kvscatter = [&]() { const int t = wm * 64 + opq(lane0); const u32x2 tb = *(LAS u32x2*)(lds + L_TAB + min(t, 179) * 16 + 8);
      const unsigned k01 = pk2v(lo2(kvs)), k23 = pk2v(hi2(kvs)), msk = t < 180 ? 0xffffu : 0u; LAS char* const a = lds + (t < 180 ? L_KVF : L_DUMMY);
      *(LAS u16*)(a + (tb.x & msk)) = (u16)k01; *(LAS u16*)(a + ((tb.x >> 16) & msk)) = (u16)(k01 >> 16); *(LAS u16*)(a + (tb.y & msk)) = (u16)k23; *(LAS u16*)(a + ((tb.y >> 16) & msk)) = (u16)(k23 >> 16); };
    auto ostep = [&](int it) {
      const int l = opq(lane0), r = l & 15, gg = l >> 4; bf16x8 kf[3][2];
#pragma unroll
      for (int en = 0; en < 3; ++en) { kf[en][0] = *(LAS bf16x8*)(lds + L_KVF + (en * 2) * 1024 + l * 16); kf[en][1] = *(LAS bf16x8*)(lds + L_KVF + (en * 2 + 1) * 1024 + l * 16); }
      { const bf16x8 b0 = cat(pq[0][it], pq[1][it]), b1 = cat(pq[2][it], (u32x2){0, 0}); f32x4 nm[3];
#pragma unroll
        for (int en = 0; en < 3; ++en) nm[en] = mfma(kf[en][1], b1, mfma(kf[en][0], b0, (f32x4){0, 0, 0, 0}));
        const float rA = __builtin_amdgcn_rcpf(__shfl(nm[2][0], 16 + r) + 1e-6f), rB = __builtin_amdgcn_rcpf(__shfl(nm[2][1], 16 + r) + 1e-6f);
        const float rM = gg == 0 ? rA : rB;
#pragma unroll
        for (int en = 0; en < 3; ++en) { const float r01 = en == 0 ? rA : en == 1 ? rM : rB, r23 = en == 0 ? rA : rB;
          const u32x2 o = {pk2v(lo2(nm[en]) * r01), pk2v(hi2(nm[en]) * r23)};
          const int oc4 = 9 * gu + 4 * en + gg;
          int vo = (oc4 >> 3) * 8192 + ((oc4 >> 2) & 1) * 512 + ((oc4 & 3) * 16 + r) * 8;
          if (en == 2) vo = gg != 0 ? OOB : vo;
          __builtin_amdgcn_raw_buffer_store_b64(o, rsWs, vo, yb + (2 * wm + it) * 1024, SC1); } } };
    auto step = [&](bf16x8 (&cur)[2][NR], bf16x8 (&nxt)[2][NR], auto Pc) {
      constexpr int p = decltype(Pc)::value, st = 30 + p, e = p - (H ? 12 : 1);
      auto vop = [&](int k) { if (H == 1 && p < 12) qop(p, k); else if (e >= 0 && e < 4 * NF) zop(e, k); };
      if constexpr (H == 1) { if (p == 14 || p == 15) ostep(p - 14); if (p >= 20 && p < 25) cvoff(vA, 2, p - 20, p - 19); waitv<allow1(27 + p)>(); }
      else if (H == 0 && p >= 2 && p <= 9) { if (p <= 8) asm volatile("s_waitcnt vmcnt(6) ; HIDDEN_WAIT %0 %1" : "+v"(kl[p & 1][0]), "+v"(kl[p & 1][1]) :: "memory");
                                             else asm volatile("s_waitcnt vmcnt(4) ; HIDDEN_WAIT %0 %1" : "+v"(kl[p & 1][0]), "+v"(kl[p & 1][1]) :: "memory"); }
      else if (H == 0 && p == 1) waitv<6>();
      else if (H == 0 && (p == 18 || p == 20)) waitv<5>();
      else waitv<4>();
      if (p == 31) waitl0();
      bar();
      SB();
      if constexpr (H == 0) {
        if (p >= 2 && p <= 9) { kvs += __builtin_bit_cast(f32x4, kl[p & 1][0]) + __builtin_bit_cast(f32x4, kl[p & 1][1]); asm volatile("" : "+v"(kvs)); }
        if (p < 8) { HLOAD(kl[p & 1][0], kvo, rsWs, colb + C_KVP + 2 * p * KVP_STRIDE); HLOAD(kl[p & 1][1], kvo, rsWs, colb + C_KVP + (2 * p + 1) * KVP_STRIDE); }
        if (p == 10) kvscatter();
        if (p == 17) raise(rowb + C_FLAGO + pj * 4);
        if (p == 19) raise(W_EPOCH, blockIdx.x == 0);
        if (p == 22) codewarm();
        if (p == 27) flags_dma((opq(lane0) & 15) * 4, rowb + C_FLAGO);
        if (p == 28) check_flags(rowb + C_FLAGO + (lane0 & 15) * 4);
      }
      ring(st, 4, p + 3);
      SB();
      vop(0);
#pragma unroll
      for (int i = 0; i < 4 * NF; ++i) {
        const int n = i >> 2, m = (i >> 1) & 1, h = i & 1;
        if (p == 0 && h == 0) mf0(ag[n][m], cur[0][2 + n], cur[0][m]); else mfa(ag[n][m], cur[h][2 + n], cur[h][m]);
        SB();
        if (p != 31 && i < 2 * NR) rd1(nxt, i, p & 3);
        if (i > 0) vop(i);
        if ((i & 3) == 3 && i < 16) ring(st, i >> 2, p + 3);
        SB();
      }
    };
#pragma unroll
    for (int i = 0; i < 2 * NR; ++i) rd1(fa, i, 3);
    STAMP(9);
    sfor<0, 16>([&](auto Kc) __attribute__((always_inline)) { constexpr int k = decltype(Kc)::value; step(fa, fb, IC<2 * k>{}); step(fb, fa, IC<2 * k + 1>{}); if (k == 5) STAMP(8); });
    accfence(ag);
  }
  STAMP(10);
  relane();
  {
    u32x2 y[NF][2];
#pragma unroll
    for (int it = 0; it < 2; ++it) { const float b = blo(*(LAS u16*)(lds + L_BIAS + (32 * wm + 8 * (fr >> 2) + 4 * it + (fr & 3)) * 2));
#pragma unroll
      for (int n = 0; n < NF; ++n) y[n][it] = (u32x2){pk2v(unpk(z1[n][it].x) * (lo2(ag[n][it]) + b)), pk2v(unpk(z1[n][it].y) * (hi2(ag[n][it]) + b))}; }
    waitv<0>();
#pragma unroll
    for (int hh = 0; hh < 2; ++hh)
#pragma unroll
      for (int it = 0; it < 2; ++it) { const u32x4 v = __builtin_bit_cast(u32x4, cat(y[2 * hh][it], y[2 * hh + 1][it]));
        st16<SC1>(v, rsWs, lane * 16, yb + (4 + 2 * gu + H) * 16384 + hh * 8192 + (2 * wm + it) * 1024);
        *(LAS u32x4*)(lds + (H ? L_YOWN : R_OFF + 2 * R_SZ) + hh * 8192 + (2 * wm + it) * 1024 + lane * 16) = v; }
    if constexpr (H == 0) {
#pragma unroll
      for (int it = 0; it < 2; ++it) __builtin_amdgcn_raw_buffer_store_b64(y[4][it], rsWs, lane * 8, yb + 3 * 16384 + (gu >> 1) * 8192 + (2 * wm + it) * 1024 + (gu & 1) * 512, SC1);
    }
  }
  STAMP(11);
  {
    LAS char* const aY = lds + R_OFF + 2 * wm * 1024;
    bf16x8 rf[2][2][2]; s16x4 bq[3][4];
    mk_aC();
    auto rdB = [&](int u, int j) { const int q = u / NF, n = u % NF; if (u < 12 * NF && (q != 2 || j < 2)) bq[u % 3][j] = tr1(((j & 2) ? aC1 : aC0) + ((59 + q) & 3) * C_SZ + n * 32 + (j & 1) * (8 * 288)); };
    auto rdY = [&](int q, int j) { const int it = j >> 1, hh = j & 1; LAS char* a = aY + (q == 4 ? L_YOWN - R_OFF : ((59 + q) & 3) * R_SZ) + hh * 8192 + it * 1024;
      if (q == 2) { if (hh == 0) rf[q & 1][it][0] = cat(*(LAS u32x2*)(a + lane * 8), (u32x2){0, 0}); }
      else if (q < 2 || q == 11) rf[q & 1][it][hh] = cat(*(LAS u32x2*)(a + lane * 8), *(LAS u32x2*)(a + 512 + lane * 8));
      else rf[q & 1][it][hh] = *(LAS bf16x8*)(a + lane * 16); };
    rdY(0, 0); rdY(0, 1); rdY(0, 2); rdY(0, 3);
#pragma unroll
    for (int j = 0; j < 8; ++j) rdB(j >> 2, j & 3);
    SB();
    sfor<0, 12 * NF>([&](auto Uc) __attribute__((always_inline)) {
      constexpr int u = decltype(Uc)::value, q = u / NF, n = u % NF;
      if (n == NF - 2 && q < 11) {
        if constexpr (H == 1) { if (q == 10) waitv<0>(); else if (q >= 1) waitv<5>(); }
        else { if (q == 1 || q == 3 || q == 10) waitv<0>(); else if (q >= 4) waitv<4>(); }
        bar();
        if constexpr (H == 0) {
          if (q == 1) raise(rowb + C_FLAGD + gc * 128 + gu * 4);
          if (q == 2) flags_dma((opq(lane0) & 3) * 4, rowb + C_FLAGD + gc * 128);
          if (q == 3) { STAMP(12); check_flags(rowb + C_FLAGD + gc * 128 + (lane0 & 3) * 4); STAMP(13); ring_all(64); }
          if (q == 4) STAMP(14);
        }
        SB();
      }
      const bf16x8 b0 = cat4(bq[u % 3][0], bq[u % 3][1]), b1 = q == 2 ? b0 : cat4(bq[u % 3][2], bq[u % 3][3]);
#pragma unroll
      for (int j = 0; j < 4; ++j) { const int it = j >> 1, hh = j & 1;
        if (q == 0 && hh == 0) mf0(acc[n][it], b0, rf[q & 1][it][hh]); else if (q != 2 || hh == 0) mfa(acc[n][it], hh ? b1 : b0, rf[q & 1][it][hh]);
        SB();
        rdB(u + 2, j);
        if (n == NF - 2 && q < 11) rdY(q + 1, j);
        if (j == 3) { const int d = u - (NF - 2); if (d >= 0) { const int qq = d / NF + 3; if (qq < 12 && (H == 1 || qq >= 6)) ring_pos(59 + qq, d % NF, NF); } }
        SB(); }
    });
    accfence(acc);
  }
  STAMP(15);
  relane();
  {
    const rsrc_t rsOut = mkrs(P.out + (size_t)pi * 128 * D, 128 * D * 2);
    auto packp = [&](const f32x4 (&a)[2]) { u32x4 v = {pk2v(lo2(a[0])), pk2v(hi2(a[0])), pk2v(lo2(a[1])), pk2v(hi2(a[1]))};
      v.x = min(v.x, 0xfffffffeu); v.y = min(v.y, 0xfffffffeu); v.z = min(v.z, 0xfffffffeu); v.w = min(v.w, 0xfffffffeu); return v; };
    constexpr int NK = H ? 1 : 2;
    f32x4 kp[NK][2]; int cb[NK];
#pragma unroll
    for (int it = 0; it < 2; ++it) { kp[0][it] = gc == 0 ? acc[0][it] : gc == 1 ? acc[1][it] : gc == 2 ? acc[2][it] : acc[3][it]; if constexpr (H == 0) kp[1][it] = acc[4][it]; }
    cb[0] = 256 * gu + 32 * (H * 4 + gc); if constexpr (H == 0) cb[1] = 1024 + 32 * gu;
    const int off1 = wm == gc ? 0 : OOB;
    int ro[2]; u32x2 xr[NK][2];
#pragma unroll
    for (int it = 0; it < 2; ++it) { ro[it] = (32 * wm + 8 * (fr >> 2) + 4 * it + (fr & 3)) * 1152 + 8 * g;
#pragma unroll
      for (int k = 0; k < NK; ++k) xr[k][it] = __builtin_amdgcn_raw_buffer_load_b64(rsX, ro[it] + (k ? off1 : 0), cb[k], 0); }
#pragma unroll
    for (int n = 0; n < NF; ++n) { const int dg = n < 4 ? n : wm, slot = n < 4 ? H * 4 + wm : 8;
      st16<SC1>(packp(acc[n]), rsWs, dg == gc ? OOB : lane * 16, rowb + C_PART + ((4 * dg + gu) * 4 + gc) * PART_SRC + slot * 1024); }
    const int pb = rowb + C_PART + pj * 4 * PART_SRC;
    u32x4 v[NK][3];
    __builtin_amdgcn_s_sleep(FIN_SLEEP);
    _Pragma("unroll 1") for (unsigned sp = 0;; ++sp) {
      unsigned mx = 0;
#pragma unroll
      for (int k = 0; k < NK; ++k)
#pragma unroll
        for (int j = 0; j < 3; ++j) { const int so = pb + ((gc + 1 + j) & 3) * PART_SRC + (k ? 8 : H * 4 + wm) * 1024;
          v[k][j] = __builtin_amdgcn_raw_buffer_load_b128(rsWs, lane * 16 + (k ? off1 : 0), so, SC1);
          mx = max(max(mx, max(v[k][j].x, v[k][j].y)), max(v[k][j].z, v[k][j].w)); }
      if (LIKELY(__all(mx != ~0u))) break;
      if (sp > SPIN_MAX) { set_flag(P.ws, W_TMO, 1u); break; }
      __builtin_amdgcn_s_sleep(FIN_POLL);
    }
#pragma unroll
    for (int k = 0; k < NK; ++k)
#pragma unroll
      for (int it = 0; it < 2; ++it) { f32x2 a = lo2(kp[k][it]) + unpk(xr[k][it].x), b = hi2(kp[k][it]) + unpk(xr[k][it].y);
#pragma unroll
        for (int j = 0; j < 3; ++j) { a += unpk(v[k][j][2 * it]); b += unpk(v[k][j][2 * it + 1]); }
        __builtin_amdgcn_raw_buffer_store_b64((u32x2){pk2v(a), pk2v(b)}, rsOut, ro[it] + (k ? off1 : 0), cb[k], SC1); }
  }
  if constexpr (H == 0) asm volatile(".Lmk_end:");
}

__global__ __launch_bounds__(512, 2) void fused(const u16* x, const u16* wqkv, const u16* wo, const u16* win, const u16* wsp, const u16* bsp, const u16* wout, u16* out, char* ws) {
  extern __shared__ __attribute__((aligned(16))) char smem[];
  LAS char* lds = (LAS char*)smem;
  const int lane = threadIdx.x & 63, wid = __builtin_amdgcn_readfirstlane(threadIdx.x >> 6);
  const int b = blockIdx.x, xc = b & 7, q = b >> 3;
  const int pi = 8 * (xc & 1) + (q & 7), pj = 4 * (xc >> 1) + (q >> 3);
  const unsigned epoch = *(const __attribute__((address_space(4))) unsigned*)(ws + W_EPOCH);
  const Ptrs P{x, wqkv, wo, win, wsp, bsp, wout, out, ws};
  unsigned long long pc; int clen;
  asm volatile("s_getpc_b64 %0\n.Lmk_pc:\n\ts_mov_b32 %1, .Lmk_end-.Lmk_pc" : "=s"(pc), "=s"(clen));
  const rsrc_t rsCode = mkrs((const void*)pc, clen);
  if (wid < 4) body<0>(P, lds, wid, lane, pi, pj, epoch, rsCode); else body<1>(P, lds, wid - 4, lane, pi, pj, epoch, rsCode);
}
}

extern "C" void kernel_launch(void* const* d_in, const int* in_sizes, int n_in, void* d_out, int out_size, void* d_ws, size_t ws_size, hipStream_t stream) {
  using mk::u16;
  (void)hipFuncSetAttribute((const void*)mk::fused, hipFuncAttributeMaxDynamicSharedMemorySize, mk::LDS_TOTAL);
  mk::fused<<<256, 512, mk::LDS_TOTAL, stream>>>((const u16*)d_in[0], (const u16*)d_in[1], (const u16*)d_in[2], (const u16*)d_in[4], (const u16*)d_in[5], (const u16*)d_in[6],
                                                 (const u16*)d_in[7], (u16*)d_out, (char*)d_ws);
}
```

```cpp
#include <hip/hip_runtime.h>
#include <hip/hip_bf16.h>
#include <type_traits>

namespace mk {
typedef unsigned short u16;
typedef short bf16x8 __attribute__((ext_vector_type(8)));
typedef short s16x4 __attribute__((ext_vector_type(4)));
typedef float f32x4 __attribute__((ext_vector_type(4)));
typedef float f32x2 __attribute__((ext_vector_type(2)));
typedef unsigned u32x4 __attribute__((ext_vector_type(4)));
typedef unsigned u32x2 __attribute__((ext_vector_type(2)));
typedef __bf16 bf16x2_t __attribute__((ext_vector_type(2)));
#define LAS __attribute__((address_space(3)))
#define GAS __attribute__((address_space(1)))
#define DI __device__ __forceinline__
template <int V> using IC = std::integral_constant<int, V>;
template <int B, int E, class F> __device__ __forceinline__ void sfor(F&& f) { if constexpr (B < E) { f(IC<B>{}); sfor<B + 1, E>(f); } }

constexpr int S = 2048, D = 576, FH = 2304;
constexpr int C_SZ = 18432, CM_SZ = 16384, CT_OFF = 65536, CT_SZ = 2048, R_OFF = 73728, R_SZ = 16384, MISC = 139264;
constexpr int L_STASH = R_OFF, L_KVS = R_OFF + 36864;
constexpr int L_ZPART = MISC + 4096, L_KVF = L_ZPART + 2048, L_XSTAT = L_KVF + 6144, L_CS = L_XSTAT + 1024, L_TAB = L_CS + 1792;
constexpr int L_LAND0 = MISC;
constexpr int L_BIAS = L_TAB + 3072, L_FLG = L_BIAS + 256, L_DUMMY = L_FLG + 1024, L_STAMP = L_DUMMY + 1024;
constexpr int L_YOWN = MISC;
static_assert(L_YOWN + 16384 <= L_BIAS, "lds");
constexpr int LDS_TOTAL = 163840;
static_assert(L_STAMP + 512 <= LDS_TOTAL, "lds");
constexpr int W_EPOCH = 0, W_TMO = 256, W_STAMP = 32768, W_COL = 2 << 20, W_ROW = 18 << 20, W_END = 50 << 20;
constexpr int COL_SZ = 1 << 20, C_ZF = 0, C_KVP = 0x90000, C_CSG = 0xA0000, C_FLAGB = 0xB0000;
constexpr int ROW_SZ = 2 << 20, C_YF = 0, C_PART = 0xC0000, C_STATS = 0x150000, C_FLAGO = 0x158000, C_FLAGD = 0x158100;
constexpr int KVP_STRIDE = 2944, YF_GRP = 12 * 16384, PART_SRC = 9216, OOB = 0x40000000;
constexpr int SC1 = 16;
constexpr unsigned SPIN_MAX = 1u << 16;
#define FIN_SLEEP 16
#define FIN_POLL 4
#define WARM_SP 1
#define WARM_W3 1
#define ABL 0
constexpr int vpre1(int sg) { return sg == 10 ? 1 : 0; }
constexpr int vpost1(int sg) { return sg == 1 || sg == 10 || sg == 24 ? 1 : sg == 12 || sg == 13 ? 4 : sg == 17 ? 3 : sg == 18 ? 6 : sg == 19 ? 2 : sg == 27 + 14 || sg == 27 + 15 ? 3 : 0; }
constexpr int allow1(int sg) { return 5 + (sg == 25 ? 0 : vpost1(sg - 1)) + vpre1(sg) + vpost1(sg); }

template <int N> DI void waitv() { asm volatile("s_waitcnt vmcnt(%0)" ::"n"(N) : "memory"); }
DI void waitl0() { __builtin_amdgcn_s_waitcnt(0xC07F); asm volatile("" ::: "memory"); }
DI void bar() { asm volatile("" ::: "memory"); __builtin_amdgcn_s_barrier(); asm volatile("" ::: "memory"); }
#define SB() __builtin_amdgcn_sched_barrier(0)
DI f32x4 mfma(bf16x8 a, bf16x8 b, f32x4 c) { return __builtin_amdgcn_mfma_f32_16x16x32_bf16(a, b, c, 0, 0, 0); }
DI void mfa(f32x4& c, const bf16x8& a, const bf16x8& b) { asm volatile("v_mfma_f32_16x16x32_bf16 %0, %1, %2, %0" : "+v"(c) : "v"(a), "v"(b)); }
DI void mf0(f32x4& c, const bf16x8& a, const bf16x8& b) { asm volatile("v_mfma_f32_16x16x32_bf16 %0, %1, %2, 0" : "=&v"(c) : "v"(a), "v"(b)); }
DI int opq(int v) { asm volatile("" : "+v"(v)); return v; }
template <int N> DI void accfence(f32x4 (&a)[N][2]) {
  if constexpr (N == 5) asm volatile("s_nop 7\n\ts_nop 1" : "+v"(a[0][0]), "+v"(a[0][1]), "+v"(a[1][0]), "+v"(a[1][1]), "+v"(a[2][0]), "+v"(a[2][1]), "+v"(a[3][0]), "+v"(a[3][1]), "+v"(a[4][0]), "+v"(a[4][1]));
  else asm volatile("s_nop 7\n\ts_nop 1" : "+v"(a[0][0]), "+v"(a[0][1]), "+v"(a[1][0]), "+v"(a[1][1]), "+v"(a[2][0]), "+v"(a[2][1]), "+v"(a[3][0]), "+v"(a[3][1]));
}
DI unsigned pk2(float lo, float hi) { f32x2 v = {lo, hi}; return __builtin_bit_cast(unsigned, __builtin_convertvector(v, bf16x2_t)); }
DI float blo(unsigned u) { return __uint_as_float(u << 16); }
DI float dot2(unsigned a, unsigned b, float c) { return __builtin_amdgcn_fdot2_f32_bf16(__builtin_bit_cast(bf16x2_t, a), __builtin_bit_cast(bf16x2_t, b), c, false); }
DI f32x2 gelu2(f32x2 v) {
  const f32x2 a = v * __builtin_elementwise_fma(v * v, (f32x2){0.10294324f, 0.10294324f}, (f32x2){2.3022082f, 2.3022082f});
  const f32x2 d = (f32x2){__builtin_amdgcn_exp2f(a.x), __builtin_amdgcn_exp2f(a.y)} + 1.0f;
  const f32x2 r = {__builtin_amdgcn_rcpf(d.x), __builtin_amdgcn_rcpf(d.y)};
  return __builtin_elementwise_fma(-v, r, v);
}
DI unsigned pk2v(f32x2 v) { return __builtin_bit_cast(unsigned, __builtin_convertvector(v, bf16x2_t)); }
DI f32x2 unpk(unsigned u) { return (f32x2){__uint_as_float(u << 16), __uint_as_float(u & 0xffff0000u)}; }
DI f32x2 lo2(f32x4 a) { return (f32x2){a.x, a.y}; }
DI f32x2 hi2(f32x4 a) { return (f32x2){a.z, a.w}; }
DI f32x2 fma2(f32x2 a, f32x2 b, f32x2 c) { return __builtin_elementwise_fma(a, b, c); }
DI f32x2 phi2(f32x2 t) {
  const f32x2 a = t * 1.44269504f, p = t + 1.0f;
  const f32x2 r = {t.x > 0.f ? p.x : __builtin_amdgcn_exp2f(a.x), t.y > 0.f ? p.y : __builtin_amdgcn_exp2f(a.y)};
  return r + 1e-6f;
}
DI float hsum32(float v) { const unsigned u = __float_as_uint(v); const auto r = __builtin_amdgcn_permlane32_swap(u, u, false, false); return __uint_as_float(r[0]) + __uint_as_float(r[1]); }
DI float hsum16(float v) { const unsigned u = __float_as_uint(v); const auto r = __builtin_amdgcn_permlane16_swap(u, u, false, false); return __uint_as_float(r[0]) + __uint_as_float(r[1]); }
template <int N> DI float bcast16(float v) { return __builtin_bit_cast(float, __builtin_amdgcn_update_dpp(0, __builtin_bit_cast(int, v), 0x150 + N, 0xf, 0xf, true)); }
DI float ror8sum(float v) { return v + __builtin_bit_cast(float, __builtin_amdgcn_update_dpp(0, __builtin_bit_cast(int, v), 0x128, 0xf, 0xf, true)); }
DI float gsum(float v) { return hsum32(hsum16(v)); }
typedef __amdgpu_buffer_rsrc_t rsrc_t;
DI rsrc_t mkrs(const void* p, int bytes) { return __builtin_amdgcn_make_buffer_rsrc((void*)p, 0, bytes, 0x00020000); }
template <int AUX> DI void dma16(rsrc_t rs, LAS char* dst, int voff, int soff) {
  const unsigned m = (unsigned)(size_t)dst;
  if constexpr (AUX == 0) asm volatile("s_mov_b32 m0, %0\n\ts_nop 0\n\tbuffer_load_dwordx4 %1, %2, %3 offen lds" ::"s"(m), "v"(voff), "s"(rs), "s"(soff) : "memory");
  else asm volatile("s_mov_b32 m0, %0\n\ts_nop 0\n\tbuffer_load_dwordx4 %1, %2, %3 offen sc1 lds" ::"s"(m), "v"(voff), "s"(rs), "s"(soff) : "memory");
}
DI void dma4(rsrc_t rs, LAS char* dst, int voff, int soff) {
  const unsigned m = (unsigned)(size_t)dst;
  asm volatile("s_mov_b32 m0, %0\n\ts_nop 3\n\tbuffer_load_dword %1, %2, %3 offen sc1 lds" ::"s"(m), "v"(voff), "s"(rs), "s"(soff) : "memory");
}
template <int AUX> DI void st16(u32x4 v, rsrc_t rs, int voff, int soff) { asm volatile("" : "+v"(v)); __builtin_amdgcn_raw_buffer_store_b128(v, rs, voff, soff, AUX); asm volatile("s_nop 1" ::"v"(v) : "memory"); }
#define LIKELY(x) __builtin_expect(!!(x), 1)
#define UNLIKELY(x) __builtin_expect(!!(x), 0)
#define HLOAD(dst, voff, rs, soff) asm volatile("buffer_load_dwordx4 %0, %1, %2, %3 offen sc1 ; HIDDEN_LOAD" : "=v"(dst) : "v"(voff), "s"(rs), "s"(soff) : "memory")
DI s16x4 tr1(LAS char* a) { return __builtin_amdgcn_ds_read_tr16_b64_v4i16((LAS s16x4*)a); }
DI bf16x8 cat4(s16x4 a, s16x4 b) { return __builtin_shufflevector(a, b, 0, 1, 2, 3, 4, 5, 6, 7); }
DI bf16x8 cat(u32x2 a, u32x2 b) { u32x4 v = {a.x, a.y, b.x, b.y}; return __builtin_bit_cast(bf16x8, v); }

struct Ptrs { const u16 *x, *wqkv, *wo, *win, *wsp, *bsp, *wout; u16* out; char* ws; };

#define STAMP(k)

DI void poll_flags(char* ws, int off, bool active, unsigned tag) {
  GAS unsigned* p = (GAS unsigned*)(ws + off);
  _Pragma("unroll 1") for (unsigned sp = 0;; ++sp) {
    const unsigned v = __hip_atomic_load(p, __ATOMIC_RELAXED, __HIP_MEMORY_SCOPE_AGENT);
    if (__all(!active || v == tag)) break;
    if (sp > SPIN_MAX) { __hip_atomic_store((GAS unsigned*)(ws + W_TMO), 1u, __ATOMIC_RELAXED, __HIP_MEMORY_SCOPE_AGENT); break; }
    __builtin_amdgcn_s_sleep(2);
  }
  asm volatile("" ::: "memory");
}
DI void set_flag(char* ws, int off, unsigned tag) { __hip_atomic_store((GAS unsigned*)(ws + off), tag, __ATOMIC_RELAXED, __HIP_MEMORY_SCOPE_AGENT); }

template <int H>
DI void body(const Ptrs& P, LAS char* lds, const int wm, const int lane0, const int pi, const int pj, unsigned epoch, const rsrc_t rsCode) {
  constexpr int NF = H ? 4 : 5, NB = H ? 5 : 0;
  int lane = lane0, fr = lane & 15, g = lane >> 4;
  auto relane = [&]() { lane = opq(lane0); fr = lane & 15; g = lane >> 4; };
  const int gc = pj >> 2, gu = pj & 3, qo = 4 * (pj & 1);
  const int rot = (pi + 2 * pj) % 9;
  const rsrc_t rsX = mkrs(P.x + (size_t)pi * 128 * D, 128 * D * 2), rsWin = mkrs(P.win, D * 2 * FH * 2), rsQkv = mkrs(P.wqkv, D * 3 * D * 2);
  const rsrc_t rsSp = mkrs(P.wsp + (size_t)pi * 128 * S, 128 * S * 2), rsWs = mkrs(P.ws, W_END);
  const rsrc_t rsWout = mkrs(P.wout, FH * D * 2), rsWo = mkrs(P.wo, D * D * 2);
  const int colb = W_COL + pj * COL_SZ, rowb = W_ROW + pi * ROW_SZ, yb = rowb + C_YF + gc * YF_GRP;
  STAMP(0);

  auto rvoff = [&](int stride) { const int l = opq(lane0); return (8 * (l >> 5) + ((l >> 3) & 3)) * stride + ((l & 7) ^ (l >> 4)) * 16; };
  int vA[2], vB[2];
  auto cvoff = [&](int (&v)[2], int kind) {
    const int l = opq(lane0), stride = kind == 0 ? 9216 : kind == 1 ? 3456 : 1152, t = kind == 0 ? pj : gu, b0 = 72 * pj - 8 * (pj & 1);
    const int sc = (l & 15) ^ ((4 * (wm & 1) + (l >> 4)) << 1), rw = (kind == 2 ? 16 * (wm >> 1) + 4 * (wm & 1) : 8 * (wm & 1) + 4 * (wm >> 1)) + (l >> 4);
    int add = 256 * t;
    if (kind == 1) { add = 1152; add = sc >= 4 ? 2304 - 64 : add; add = sc >= 8 ? 1152 - 64 : add; add = sc >= 9 ? 2304 - 80 : add; add = sc >= 10 ? -160 : add; add = sc >= 15 ? OOB : add; add += b0; }
    v[0] = rw * stride + 16 * sc + add;
    const int Pp = 32 * wm + (l >> 1), k = kind == 2 ? (Pp & ~24) | ((Pp & 8) << 1) | ((Pp & 16) >> 1) : (Pp & ~12) | ((Pp & 4) << 1) | ((Pp & 8) >> 1);
    const int tb = (l & 1) * 16 + (kind == 0 ? 4096 : 1024) + 32 * t;
    v[1] = (wm >= 2 || kind == 1) ? OOB : k * stride + tb;
    asm volatile("" : "+v"(v[0]), "+v"(v[1]));
  };
  auto ksl = [&](int s) { int k = rot + s; return k >= 9 ? k - 9 : k; };
  auto kord = [&](int p) { return p < 2 ? 2 * pi + p : (p - 2 < 2 * pi ? p - 2 : p); };
  auto g3stage = [&](int q) { return q < 3 ? q : q == 11 ? 3 : 4 + ((2 * gu + (q - 3)) & 7); };
  auto ring = [&](int st, int idx, int dyn = 0) {
    const int slot = st & 3;
    if constexpr (H == 1) {
      LAS char* dst = lds + (idx < 4 ? slot * CM_SZ + (wm + 4 * idx) * 1024 : wm >= 2 ? L_DUMMY : CT_OFF + slot * CT_SZ + wm * 1024);
      const int tl = idx == 4, r1 = tl ? 0 : 16 * idx, r3 = tl ? 0 : 32 * (idx >> 1) + 8 * (idx & 1);
      if (st < 9) dma16<0>(rsWin, dst, vA[tl], 4608 + (ksl(st) * 64 + r1) * 9216);
      else if (st < 18) dma16<0>(rsQkv, dst, vB[tl], (ksl(st - 9) * 64 + r1) * 3456);
      else if (st < 27) dma16<0>(rsWin, dst, vA[tl], (ksl(st - 18) * 64 + r1) * 9216);
      else if (st < 59) dma16<SC1>(rsWs, dst, vB[tl], colb + C_ZF + kord(dyn) * C_SZ + (wm + 4 * idx) * 1024);
      else { const int q = st - 59;
        if (q < 3) dma16<0>(rsWo, dst, vA[tl], (144 * gc + 64 * q + r3) * 1152);
        else if (q == 11) dma16<0>(rsWout, dst, vA[tl], (2048 + 64 * gc + r3) * 1152);
        else dma16<0>(rsWout, dst, vA[tl], (512 * gc + 64 * (g3stage(q) - 4) + r3) * 1152); }
    } else {
      if (idx == 4) return;
      LAS char* dst = lds + R_OFF + slot * R_SZ + (4 * wm + idx) * 1024;
      const int rb = 32 * wm + 16 * (idx & 1) + 4 * (idx >> 1);
      if (st < 9) dma16<0>(rsX, dst, vA[idx & 1], rb * 1152 + ksl(st) * 128);
      else if (st < 27) return;
      else if (st < 59) dma16<0>(rsSp, dst, vA[idx & 1], rb * 4096 + kord(dyn) * 128);
      else dma16<SC1>(rsWs, dst, vB[0], yb + g3stage(st - 59) * 16384 + (4 * wm + idx) * 1024);
    }
  };
  auto ring_all = [&](int st) { ring(st, 4); ring(st, 0); ring(st, 1); ring(st, 2); ring(st, 3); };
  auto ring_pos = [&](int st, int k, int nf) {
    const int dyn = st - 27;
    if (nf == 5) { ring(st, k == 0 ? 4 : k - 1, dyn); }
    else if (nf == 4) { if (k == 0) ring(st, 4, dyn); ring(st, k, dyn); }
    else { if (k == 0) { ring(st, 4, dyn); ring(st, 0, dyn); } else if (k == 1) { ring(st, 1, dyn); ring(st, 2, dyn); } else ring(st, 3, dyn); }
  };
  if constexpr (H == 1) cvoff(vA, 0); else { vA[0] = rvoff(1152); vA[1] = vA[0] ^ 64; }
  auto codewarm = [&]() { if (wm == 3) dma4(rsCode, lds + L_DUMMY, opq(lane0) * 128, ((blockIdx.x >> 3) & 15) * 8192); };
  if constexpr (H == 0) codewarm();
  ring_all(0);
  if constexpr (H == 1) ring_all(1);
  STAMP(17);
  if constexpr (H == 1) cvoff(vB, 1);
  else {
    const int t = wm * 64 + lane;
    const int rd = (t * 205) >> 10, e0 = 4 * (t - 5 * rd), hd = rd >= 18, dc = qo + rd;
    const int gb = (dc >> 4) * 3072 - (dc >> 5) * 1024 + ((dc >> 2) & 3) * 256 + (dc & 3) * 4, sb = (dc >> 5) * 1024 + ((dc >> 2) & 3) * 256 + ((dc >> 4) & 1) * 8 + (dc & 3) * 2;
    unsigned go[4], so[4];
#pragma unroll
    for (int k = 0; k < 4; ++k) { const int e = e0 + k, ce = qo + 18 * hd + e; int off = (ce >> 4) * 1024 + (ce & 15) * 16; off = ce >= 32 ? off + 128 : off; off = e >= 18 ? 2048 : off;
      int ep = 18 * hd + e; ep = e == 18 ? 36 + hd : ep; ep = e == 19 ? 40 : ep;
      go[k] = gb + off; so[k] = sb + (ep >> 4) * 2048 + (ep & 15) * 16; }
    if (t < 180) *(LAS u32x4*)(lds + L_TAB + t * 16) = (u32x4){go[0] | (go[1] << 16), go[2] | (go[3] << 16), so[0] | (so[1] << 16), so[2] | (so[3] << 16)};
  }
  if constexpr (H == 0) ring_all(1);
  LAS char *aR0, *aR1, *aC[NF];
  auto mk_aR = [&]() { const int l = opq(lane0), r = l & 15, o = (32 * wm + r) * 128 + (((l >> 4) ^ ((r >> 1) & 7)) * 16); aR0 = lds + R_OFF + o; aR1 = lds + R_OFF + (o ^ 64); };
  auto mk_aC = [&](int rotn, bool allmain = false) { const int l = opq(lane0), r0 = 16 * (l >> 5) + ((l >> 2) & 7);
#pragma unroll
    for (int n = 0; n < NF; ++n) { aC[n] = lds + ((n < 4 || allmain) ? r0 * 256 + (((allmain ? (NB + n) & 7 : 4 * H + ((n + rotn) & 3)) ^ (r0 & 7)) * 32) : CT_OFF + r0 * 32) + (l & 3) * 8; asm volatile("" : "+v"(aC[n])); } };
  auto trC = [&](int st, int n, int j) { return tr1(aC[n] + ((n < 4 || (st >= 9 && st < 18)) ? (st & 3) * CM_SZ + (j >> 1) * 8192 + (j & 1) * 2048 : (st & 3) * CT_SZ + (j >> 1) * 1024 + (j & 1) * 256)); };
  asm volatile("" : "+s"(epoch));
  const unsigned tag = epoch * 0x9E3779B1u + 0x7F4A7C15u;
  unsigned ONES = 0x3f803f80u; asm volatile("" : "+s"(ONES));
  auto raise = [&](int off, bool on = true) { __builtin_amdgcn_raw_buffer_store_b32(tag, rsWs, (opq(lane0) == 0 && wm == 0 && on) ? 0 : OOB, off, SC1); };
  auto flags_dma = [&](int voff, int soff) { dma4(rsWs, lds + L_FLG + wm * 256, voff, soff); };
  auto check_flags = [&](int off) { const unsigned v = *(LAS unsigned*)(lds + L_FLG + wm * 256 + opq(lane0) * 4);
    if (UNLIKELY(!__all(v == tag))) { STAMP(18); poll_flags(P.ws, off, true, tag); } };

  bf16x8 xf[9][2][2];
  f32x4 acc[NF][2];
  f32x4 zm[2], zr2[2];
  auto z2_read = [&](int n) { const int l = opq(lane0), r = l & 15, q = l >> 4;
    return *(LAS u32x4*)(lds + L_STASH + wm * 9216 + n * 1024 + r * 64 + (((q >> 1) ^ ((r >> 2) & 1)) * 32) + (q & 1) * 16); };
  auto z2n_tile = [&](int n, u32x4 z) {
    const u32x4 o = {pk2v(fma2(unpk(z.x), lo2(zr2[0]), lo2(zm[0]))), pk2v(fma2(unpk(z.y), hi2(zr2[0]), hi2(zm[0]))), pk2v(fma2(unpk(z.z), lo2(zr2[1]), lo2(zm[1]))), pk2v(fma2(unpk(z.w), hi2(zr2[1]), hi2(zm[1])))};
    st16<SC1>(o, rsWs, opq(lane0) * 16, colb + C_ZF + (2 * pi + (wm >> 1)) * C_SZ + (n == 4 ? 16 + (wm & 1) : (wm & 1) * 8 + (n < 4 ? n : n - 1)) * 1024); };

  auto pass = [&](auto PASSc) {
    constexpr int PASS = decltype(PASSc)::value;
    constexpr int NFP = (PASS == 1 && H == 1) ? 3 : NF, ST0 = 9 * PASS, NU = 9 * NFP;
    constexpr bool TR = (PASS == 2) || (PASS == 1 && H == 1);
    constexpr int L_LAND = PASS == 0 ? L_LAND0 : PASS == 1 ? L_KVS : R_OFF + 2 * R_SZ;
    const int csg = colb + C_CSG + PASS * 12288;
    s16x4 bq[3][4];
    u32x4 gl[8]; u32x2 zp[2], t2[9]; float tf[16]; f32x4 ct, xs1[2], xs2[2]; float csv = 0.f, cs5 = 0.f;
    auto rdB = [&](int u, int j) { const int s = u / NFP, n = u % NFP; if (u < NU) bq[u % 3][j] = trC(ST0 + s, n, j); };
    auto rdX = [&](int s, int j) { xf[s][j >> 1][j & 1] = *(LAS bf16x8*)(((j & 1) ? aR1 : aR0) + (s & 3) * R_SZ + (j >> 1) * 2048); };
    auto slot = [&](auto SGc) __attribute__((always_inline)) {
      constexpr int sg = decltype(SGc)::value, s = sg - ST0;
      if (s == 1) { const int l = opq(lane0); const bool on = wm == 0 && pi < 9;
        __builtin_amdgcn_raw_buffer_store_b64((u32x2){__float_as_uint(csv), tag}, rsWs, on ? (NB * 16 + l) * 8 : OOB, csg + rot * 1152, SC1);
        if constexpr (H == 0) __builtin_amdgcn_raw_buffer_store_b64((u32x2){__float_as_uint(cs5), tag}, rsWs, (on && l < 16) ? (64 + l) * 8 : OOB, csg + rot * 1152, SC1); }
      if constexpr (H == 1) {
        if (sg == 8) {
          asm volatile("s_nop 7" : "+v"(xs1[0]), "+v"(xs2[0]), "+v"(xs1[1]), "+v"(xs2[1]));
          const int l = opq(lane0), r = l & 15, j = r & 3;
          LAS char* const o = lds + ((r >> 2) == (l >> 4) ? L_XSTAT + (32 * wm + r) * 4 : L_DUMMY);
#pragma unroll
          for (int it = 0; it < 2; ++it) { float s1 = xs1[it].x, s2 = xs2[it].x;
            s1 = j == 1 ? xs1[it].y : s1; s2 = j == 1 ? xs2[it].y : s2; s1 = j == 2 ? xs1[it].z : s1; s2 = j == 2 ? xs2[it].z : s2; s1 = j == 3 ? xs1[it].w : s1; s2 = j == 3 ? xs2[it].w : s2;
            const float mean = s1 * (1.0f / D), var = fmaxf(s2 * (1.0f / D) - mean * mean, 0.f);
            *(LAS float*)(o + it * 64) = -mean; *(LAS float*)(o + it * 64 + 512) = __builtin_amdgcn_rsqf(var + 1e-5f); } }
        if (s == 7) { const int t = min(wm * 64 + opq(lane0), 143); unsigned bad = 0;
          auto f = [&](int j) { return __uint_as_float(t2[j].x); };
          float c = (((f(0) + f(1)) + (f(2) + f(3))) + ((f(4) + f(5)) + (f(6) + f(7)))) + f(8);
#pragma unroll
          for (int j = 0; j < 9; ++j) bad |= t2[j].y ^ tag;
          if (UNLIKELY(!__all(bad == 0))) {
            _Pragma("unroll 1") for (unsigned sp = 0; !__all(bad == 0); ++sp) {
              STAMP(20);
              if (sp > SPIN_MAX) { set_flag(P.ws, W_TMO, 1u); break; }
              bad = 0;
#pragma unroll
              for (int j = 0; j < 9; ++j) { t2[j] = __builtin_amdgcn_raw_buffer_load_b64(rsWs, t * 8, csg + j * 1152, SC1); bad |= t2[j].y ^ tag; }
              c = (((f(0) + f(1)) + (f(2) + f(3))) + ((f(4) + f(5)) + (f(6) + f(7)))) + f(8);
            } }
          *(LAS float*)(lds + L_CS + PASS * 576 + t * 4) = c; }
        auto svo = [&]() { const int l = opq(lane0); return ((l >> 3) & 1) * 16384 + (32 * wm + 8 * (l >> 4) + (l & 7)) * 16; };
        if (sg == 12 || sg == 13) { const int vo = svo();
#pragma unroll
          for (int j = 4 * (sg - 12); j < 4 * (sg - 11); ++j) HLOAD(gl[j], vo, rsWs, rowb + C_STATS + j * 2048); }
        if (sg == 15) {
          asm volatile("s_waitcnt vmcnt(10) ; HIDDEN_WAIT %0 %1 %2 %3 %4 %5 %6 %7" : "+v"(gl[0]), "+v"(gl[1]), "+v"(gl[2]), "+v"(gl[3]), "+v"(gl[4]), "+v"(gl[5]), "+v"(gl[6]), "+v"(gl[7]) :: "memory");
          float s1, s2; unsigned bad;
          auto red = [&]() { auto a = [&](int j) { return __uint_as_float(gl[j].x); }; auto b = [&](int j) { return __uint_as_float(gl[j].z); }; bad = 0;
            s1 = ((a(0) + a(1)) + (a(2) + a(3))) + ((a(4) + a(5)) + (a(6) + a(7))); s2 = ((b(0) + b(1)) + (b(2) + b(3))) + ((b(4) + b(5)) + (b(6) + b(7)));
#pragma unroll
            for (int j = 0; j < 8; ++j) { bad |= gl[j].y ^ tag; bad |= gl[j].w ^ tag; } };
          red();
          if (UNLIKELY(!__all(bad == 0))) {
            _Pragma("unroll 1") for (unsigned sp = 0; !__all(bad == 0); ++sp) {
              STAMP(19);
              if (sp > SPIN_MAX) { set_flag(P.ws, W_TMO, 1u); break; }
              const int vo = svo();
#pragma unroll
              for (int j = 0; j < 8; ++j) gl[j] = __builtin_amdgcn_raw_buffer_load_b128(rsWs, vo, rowb + C_STATS + j * 2048, SC1);
              red();
            } }
          s1 = ror8sum(s1); s2 = ror8sum(s2);
          const float mu = s1 * (1.0f / FH), var = fmaxf(s2 * (1.0f / FH) - mu * mu, 0.f);
          tf[1] = __builtin_amdgcn_rsqf(var + 1e-5f); tf[0] = -mu * tf[1]; asm volatile("" : "+v"(tf[0]), "+v"(tf[1])); }
        if (sg == 16) {
          zm[0] = (f32x4){bcast16<0>(tf[0]), bcast16<1>(tf[0]), bcast16<2>(tf[0]), bcast16<3>(tf[0])}; zm[1] = (f32x4){bcast16<4>(tf[0]), bcast16<5>(tf[0]), bcast16<6>(tf[0]), bcast16<7>(tf[0])};
          zr2[0] = (f32x4){bcast16<0>(tf[1]), bcast16<1>(tf[1]), bcast16<2>(tf[1]), bcast16<3>(tf[1])}; zr2[1] = (f32x4){bcast16<4>(tf[1]), bcast16<5>(tf[1]), bcast16<6>(tf[1]), bcast16<7>(tf[1])};
          asm volatile("" : "+v"(zm[0]), "+v"(zm[1]), "+v"(zr2[0]), "+v"(zr2[1])); }
        if (sg == 17) { z2n_tile(0, gl[0]); z2n_tile(1, gl[1]); z2n_tile(2, gl[2]); }
        if (sg == 19) { const int t = wm * 64 + opq(lane0); u32x4 o;
#pragma unroll
          for (int k = 0; k < 4; ++k) o[k] = __float_as_uint((tf[4 * k] + tf[4 * k + 1]) + (tf[4 * k + 2] + tf[4 * k + 3]));
          st16<SC1>(o, rsWs, t < 180 ? t * 16 : OOB, colb + C_KVP + pi * KVP_STRIDE); }
        if (sg == 23) { vB[0] = opq(lane0) * 16; vB[1] = wm >= 2 ? OOB : vB[0]; }
        if (sg == 24) flags_dma((opq(lane0) & 15) * 4, colb + C_FLAGB);
        if (sg == 26) check_flags(colb + C_FLAGB + (lane0 & 15) * 4);
      }
    };
    auto front = [&](auto SGc) __attribute__((always_inline)) {
      constexpr int sg = decltype(SGc)::value, s = sg - ST0;
      if constexpr (H == 0) {
        if (s == 4) { const int vo = opq(lane0) * 16;
#pragma unroll
          for (int k = 0; k < 3; ++k) dma16<SC1>(rsWs, lds + L_LAND + (wm + 4 * k) * 1024, vo, csg + (wm + 4 * k) * 1024); }
        if (sg == 21) raise(colb + C_FLAGB + pi * 4);
        if (sg == 22) { vA[0] = rvoff(4096); vA[1] = vA[0] ^ 64; }
      } else {
        if (sg == 9) { const int r = wm * 64 + opq(lane0); const f32x2 a = __builtin_bit_cast(f32x2, zp[0]) + __builtin_bit_cast(f32x2, zp[1]);
          st16<SC1>((u32x4){__float_as_uint(a.x), tag, __float_as_uint(a.y), tag}, rsWs, r < 128 ? r * 16 : OOB, rowb + C_STATS + pj * 2048); }
        if (s == 6) { const int t = min(wm * 64 + opq(lane0), 143);
#pragma unroll
          for (int j = 0; j < 9; ++j) t2[j] = *(LAS u32x2*)(lds + L_LAND + j * 1152 + t * 8); }
        if (sg == 16) { gl[0] = z2_read(0); gl[1] = z2_read(1); gl[2] = z2_read(2); }
      }
    };
#pragma unroll
    for (int n = NFP; n < NF; ++n) { acc[n][0] = (f32x4){0, 0, 0, 0}; acc[n][1] = (f32x4){0, 0, 0, 0}; }
    mk_aC(0, PASS == 1);
    if (PASS == 0) { mk_aR(); rdX(0, 0); rdX(0, 1); rdX(0, 2); rdX(0, 3); }
#pragma unroll
    for (int j = 0; j < 8; ++j) rdB(j >> 2, j & 3);
    if (PASS == 0) ring_all(2);
    bf16x8 ones8 = __builtin_bit_cast(bf16x8, (u32x4){ONES, ONES, ONES, ONES}); asm volatile("" : "+v"(ones8));
    const int gq = opq(lane0) >> 4;
    SB();
    sfor<0, NU>([&](auto Uc) __attribute__((always_inline)) {
      constexpr int u = decltype(Uc)::value, s = u / NFP, n = u % NFP, sg = ST0 + s, d = u - (NFP - 2);
      if (n == NFP - 2) {
        slot(IC<sg>{});
        if constexpr (H == 1) waitv<allow1(sg)>();
        else { if (sg == 1 || sg == 2) waitv<6>(); else if (sg == 5) waitv<7>(); else if (sg < 7 || sg == 26) waitv<4>(); else if (sg == 7 || sg == 15 || sg == 24) waitv<0>(); }
        if ((sg == 8 && H == 1) || (sg == 18 && H == 0)) waitl0();
        bar();
        if (H == 1 && sg == 9) { const int r = (wm * 64 + opq(lane0)) & 127; zp[0] = *(LAS u32x2*)(lds + L_ZPART + r * 8); zp[1] = *(LAS u32x2*)(lds + L_ZPART + 1024 + r * 8); }
        if (H == 1 && sg == 18) zp[0] = *(LAS u32x2*)(lds + L_TAB + min(wm * 64 + opq(lane0), 179) * 16);
        SB();
      }
      const bf16x8 b0 = cat4(bq[u % 3][0], bq[u % 3][1]), b1 = cat4(bq[u % 3][2], bq[u % 3][3]);
#pragma unroll
      for (int j = 0; j < 4; ++j) { const int it = j >> 1, hh = j & 1; const bf16x8 &bb = hh ? b1 : b0, &oa = TR ? bb : xf[s][it][hh], &ob = TR ? xf[s][it][hh] : bb;
        if (s == 0 && hh == 0) mf0(acc[n][it], oa, ob); else mfa(acc[n][it], oa, ob);
        SB();
        rdB(u + 2, j);
        if (PASS == 0 && n == NFP - 2 && s < 8) rdX(s + 1, j);
        if (j == 3) {
          if (u >= 1 && u <= NFP) { asm volatile("s_nop 0" : "+v"(ct)); if (u - 1 < 4) csv = gq == u - 1 ? ct.x : csv; else cs5 = ct.x; asm volatile("" : "+v"(csv), "+v"(cs5)); }
          if (n == NFP - 2) front(IC<sg>{});
          if (H == 1 && sg == 19 && n == 0) {
#pragma unroll
            for (int k = 0; k < 4; ++k) { LAS char* const a = lds + L_KVS + ((k & 1) ? zp[0][k >> 1] >> 16 : zp[0][k >> 1] & 0xffffu);
#pragma unroll
              for (int w = 0; w < 4; ++w) tf[4 * k + w] = *(LAS float*)(a + w * 8192); } }
          if constexpr (d >= 0) ring_pos(ST0 + d / NFP + 3, d % NFP, NFP); }
        SB(); }
      if (s == 0) { mf0(ct, ones8, b0); mfa(ct, ones8, b1); }
      if (PASS == 0 && H == 1 && n < 2 && (s == 0 || !(ABL & 2))) {
#pragma unroll
        for (int it = 0; it < 2; ++it) { f32x4& xs = n ? xs2[it] : xs1[it]; const bf16x8 &o0 = n ? xf[s][it][0] : ones8, &o1 = n ? xf[s][it][1] : ones8;
          if (s == 0) mf0(xs, xf[s][it][0], o0); else mfa(xs, xf[s][it][0], o0); mfa(xs, xf[s][it][1], o1); } }
      SB();
    });
#pragma unroll
    for (int k = 2; k < NFP; ++k) ring_pos(ST0 + 11, k, NFP);
    accfence(acc);
    relane();
  };
  auto csum1 = [&](int ps, int col) { return *(LAS float*)(lds + L_CS + ps * 576 + col * 4); };
  auto rowstat = [&](f32x2 (&nm)[4], f32x2 (&rs)[4]) {
#pragma unroll
    for (int it = 0; it < 2; ++it) { const f32x4 a = *(LAS f32x4*)(lds + L_XSTAT + (32 * wm + 16 * it + 4 * g) * 4), b = *(LAS f32x4*)(lds + L_XSTAT + 512 + (32 * wm + 16 * it + 4 * g) * 4);
      nm[2 * it] = lo2(a); nm[2 * it + 1] = hi2(a); rs[2 * it] = lo2(b); rs[2 * it + 1] = hi2(b); } };
  auto affN = [&](int n, float c, const f32x2 (&nm)[4], const f32x2 (&rs)[4], f32x2 (&v)[4]) { const f32x2 c2 = {c, c};
    v[0] = fma2(nm[0], c2, lo2(acc[n][0])) * rs[0]; v[1] = fma2(nm[1], c2, hi2(acc[n][0])) * rs[1]; v[2] = fma2(nm[2], c2, lo2(acc[n][1])) * rs[2]; v[3] = fma2(nm[3], c2, hi2(acc[n][1])) * rs[3]; };

  if constexpr (H == 1) { if (!(ABL & 4)) __builtin_amdgcn_s_setprio(1); waitv<5>(); } else waitv<4>();
  bar();
  STAMP(1);
  pass(IC<0>{});
  STAMP(2);
  if constexpr (H == 0) {
    dma4(mkrs(P.wsp, S * S * 2), lds + L_DUMMY, lane * 128, (blockIdx.x * 4 + wm) * 8192);
    if (wm == 0) dma4(mkrs(P.bsp, S * 2), lds + L_BIAS, lane * 4, pi * 256);
  }
  {
    f32x2 nm[4], rs[4]; rowstat(nm, rs);
    LAS char* const sw = lds + L_STASH + (wm * 9 + NB) * 1024 + fr * 64 + (((g >> 1) ^ ((fr >> 2) & 1)) * 32) + (g & 1) * 16;
    LAS char* const sr = lds + L_STASH + (wm * 9 + NB) * 1024 + (4 * g + (fr >> 2)) * 64 + (g & 1) * 32 + (fr & 3) * 8;
    LAS char* const sr1 = lds + L_STASH + (wm * 9 + NB) * 1024 + (4 * g + (fr >> 2)) * 64 + ((g & 1) ^ 1) * 32 + (fr & 3) * 8;
    float cn[NF];
#pragma unroll
    for (int n = 0; n < NF; ++n) cn[n] = csum1(0, (NB + n) * 16 + fr);
#pragma unroll
    for (int n = 0; n < NF; ++n) { f32x2 v[4]; affN(n, cn[n], nm, rs, v);
      if constexpr (H == 0) {
#pragma unroll
        for (int i = (n * 7) / 5; i < ((n + 1) * 7) / 5; ++i) { const int k = i * 4 + wm, src = k / 9;
          st16<SC1>((u32x4){~0u, ~0u, ~0u, ~0u}, rsWs, k < 27 ? lane * 16 : OOB, rowb + C_PART + (pj * 4 + ((gc + 1 + src) & 3)) * PART_SRC + (k - 9 * src) * 1024); } }
      *(LAS u32x4*)(sw + n * 1024) = (u32x4){pk2v(gelu2(v[0])), pk2v(gelu2(v[1])), pk2v(gelu2(v[2])), pk2v(gelu2(v[3]))}; }
    asm volatile("" ::: "memory");
    float s1[2] = {0.f, 0.f}, s2[2] = {0.f, 0.f};
#pragma unroll
    for (int n = 0; n < NF; ++n)
#pragma unroll
      for (int ih = 0; ih < 2; ++ih) { const u32x2 t = __builtin_bit_cast(u32x2, __builtin_amdgcn_ds_read_tr16_b64_v4i16((LAS s16x4*)((ih ? sr1 : sr) + n * 1024)));
        s1[ih] = dot2(t.x, ONES, s1[ih]); s2[ih] = dot2(t.x, t.x, s2[ih]); s1[ih] = dot2(t.y, ONES, s1[ih]); s2[ih] = dot2(t.y, t.y, s2[ih]); }
    const float a0 = gsum(s1[0]), b0 = gsum(s2[0]), a1 = gsum(s1[1]), b1 = gsum(s2[1]);
    if (g < 2) *(LAS f32x2*)(lds + L_ZPART + (H * 128 + 32 * wm + 16 * g + fr) * 8) = g ? (f32x2){a1, b1} : (f32x2){a0, b0};
    waitl0();
  }
  STAMP(3);
  pass(IC<1>{});
  STAMP(4);
  u32x2 pq[3][2];
  f32x4 aq[3][2];
  if constexpr (H == 0) {
        { const int l = opq(lane0), li = (wm & 1) * 64 + l;
          if (!WARM_W3) {} else if (wm < 2) dma4(rsWout, lds + L_DUMMY, li < 81 ? li * 128 : OOB, blockIdx.x * 81 * 128);
          else if (wm == 2) dma4(rsWo, lds + L_DUMMY, l < 21 ? l * 128 : OOB, blockIdx.x * 21 * 128);
          codewarm(); }
    f32x2 nm[4], rs[4]; rowstat(nm, rs);
    float cn[5];
#pragma unroll
    for (int n = 0; n < 5; ++n) cn[n] = csum1(1, n * 16 + fr);
    *(LAS u32x4*)(lds + L_KVF + (wm * 64 + lane) * 16) = (u32x4){0, 0, 0, 0};
    *(LAS u32x2*)(lds + L_KVF + 4096 + (wm * 64 + lane) * 8) = (u32x2){0, 0};
    bf16x8 pkf[3], vf[3];
#pragma unroll
    for (int n = 0; n < 5; ++n) { f32x2 v[4]; affN(n, cn[n], nm, rs, v);
      if (n >= 2) { u32x4 w = {pk2v(v[0]), pk2v(v[1]), pk2v(v[2]), pk2v(v[3])};
        if (n == 4 && fr == 0) w = (u32x4){ONES, ONES, ONES, ONES};
        vf[n - 2] = __builtin_bit_cast(bf16x8, w); }
      if (n < 2 || n == 4) pkf[n == 4 ? 2 : n] = __builtin_bit_cast(bf16x8, (u32x4){pk2v(phi2(v[0])), pk2v(phi2(v[1])), pk2v(phi2(v[2])), pk2v(phi2(v[3]))});
    }
#pragma unroll
    for (int td = 0; td < 3; ++td)
#pragma unroll
      for (int te = 0; te < 3; ++te) { if (td == 2 && te == 0) continue;
        *(LAS f32x4*)(lds + L_KVS + (wm * 8 + td * 3 + te - (td == 2)) * 1024 + lane * 16) = mfma(pkf[td], vf[te], (f32x4){0, 0, 0, 0}); }
  } else {
    u32x4 z[6];
#pragma unroll
    for (int n = 0; n < 6; ++n) z[n] = z2_read(3 + n);
#pragma unroll
    for (int n = 0; n < 6; ++n) z2n_tile(3 + n, z[n]);
#pragma unroll
    for (int n = 0; n < 3; ++n) { aq[n][0] = acc[n][0]; aq[n][1] = acc[n][1]; }
  }
  STAMP(6);
  pass(IC<2>{});
  STAMP(7);
  u32x2 z1[NF][2];
  f32x4 ag[NF][2];
  {
    constexpr int NR = NF + 2;
    LAS char* const aZ = lds + H * 4096 + lane * 16;
    LAS char* aZt = lds + CT_OFF + lane * 16; asm volatile("" : "+v"(aZt));
    mk_aR();
    if constexpr (H == 0) vB[0] = lane * 16;
    f32x4 kvs = {0, 0, 0, 0}; u32x4 kl[2][2]; int kvo;
    if constexpr (H == 0) { const int t = wm * 64 + lane; kvo = t < 180 ? t * 16 : OOB; }
    bf16x8 fa[2][NR], fb[2][NR];
    auto rd1 = [&](bf16x8 (&f)[2][NR], int i, int slot) {
      const int h = i & 1; int ii = i >> 1; if (ii == 1) ii = 2; else if (ii == 2) ii = 1;
      if (ii < 2) f[h][ii] = *(LAS bf16x8*)((h ? aR1 : aR0) + slot * R_SZ + ii * 2048); else if (ii < 6) f[h][ii] = *(LAS bf16x8*)(aZ + slot * CM_SZ + (h * 8 + ii - 2) * 1024); else f[h][ii] = *(LAS bf16x8*)(aZt + slot * CT_SZ + h * 1024);
    };
    float zn[2], zr[2], za, zb, ta, tb; f32x2 zc;
    LAS char* const aCs = lds + L_CS + 576 + (NB * 16 + 4 * g) * 4;
#pragma unroll
    for (int it = 0; it < 2; ++it) { zn[it] = *(LAS float*)(lds + L_XSTAT + (32 * wm + 16 * it + fr) * 4); zr[it] = *(LAS float*)(lds + L_XSTAT + 512 + (32 * wm + 16 * it + fr) * 4); }
    float GC1 = 2.3022082f; asm volatile("" : "+v"(GC1));
    auto zop = [&](int e, int k) { const int n = e >> 2, it = (e >> 1) & 1, h = e & 1;
      if (k == 0) zc = *(LAS f32x2*)(aCs + 576 + n * 64 + h * 8);
      if (k == 3) asm volatile("v_fma_f32 %0, %2, %4, %5\n\tv_fma_f32 %1, %3, %4, %6" : "=&v"(za), "=&v"(zb) : "v"(zc.x), "v"(zc.y), "v"(zn[it]), "v"(acc[n][it][2 * h]), "v"(acc[n][it][2 * h + 1]));
      if (k == 4) asm volatile("v_mul_f32 %0, %0, %2\n\tv_mul_f32 %1, %1, %2" : "+v"(za), "+v"(zb) : "v"(zr[it]));
      if (k == 5) asm volatile("v_mul_f32 %0, %2, %2\n\tv_mul_f32 %1, %3, %3" : "=&v"(ta), "=&v"(tb) : "v"(za), "v"(zb));
      if (k == 6) asm volatile("v_fmamk_f32 %0, %0, 0x3dd2d3e8, %2\n\tv_fmamk_f32 %1, %1, 0x3dd2d3e8, %2" : "+v"(ta), "+v"(tb) : "v"(GC1));
      if (k == 7) asm volatile("v_mul_f32 %0, %0, %2\n\tv_mul_f32 %1, %1, %3" : "+v"(ta), "+v"(tb) : "v"(za), "v"(zb));
      if (k == 8) asm volatile("v_exp_f32 %0, %0" : "+v"(ta));
      if (k == 9) asm volatile("v_exp_f32 %0, %0" : "+v"(tb));
      if (k == 10) asm volatile("v_add_f32 %0, 1.0, %0\n\tv_add_f32 %1, 1.0, %1" : "+v"(ta), "+v"(tb));
      if (k == 11) asm volatile("v_rcp_f32 %0, %0" : "+v"(ta));
      if (k == 12) asm volatile("v_rcp_f32 %0, %0" : "+v"(tb));
      if (k == 13) asm volatile("v_fma_f32 %0, -%0, %2, %0\n\tv_fma_f32 %1, -%1, %3, %1" : "+v"(za), "+v"(zb) : "v"(ta), "v"(tb));
      if (k == 14) { unsigned o; asm volatile("v_cvt_pk_bf16_f32 %0, %1, %2" : "=v"(o) : "v"(za), "v"(zb)); z1[n][it][h] = o; } };
    auto qop = [&](int e, int k) { const int n = e >> 2, it = (e >> 1) & 1, h = e & 1;
      if (k == 0) zc = *(LAS f32x2*)(aCs + n * 64 + h * 8);
      if (k == 3) asm volatile("v_fma_f32 %0, %2, %4, %5\n\tv_fma_f32 %1, %3, %4, %6" : "=&v"(za), "=&v"(zb) : "v"(zc.x), "v"(zc.y), "v"(zn[it]), "v"(aq[n][it][2 * h]), "v"(aq[n][it][2 * h + 1]));
      if (k == 4) asm volatile("v_mul_f32 %0, %0, %2\n\tv_mul_f32 %1, %1, %2" : "+v"(za), "+v"(zb) : "v"(zr[it]));
      if (k == 5) asm volatile("v_min_f32 %0, 0, %2\n\tv_min_f32 %1, 0, %3" : "=&v"(ta), "=&v"(tb) : "v"(za), "v"(zb));
      if (k == 6) asm volatile("v_mul_f32 %0, 0x3fb8aa3b, %0\n\tv_mul_f32 %1, 0x3fb8aa3b, %1" : "+v"(ta), "+v"(tb));
      if (k == 7) asm volatile("v_exp_f32 %0, %0" : "+v"(ta));
      if (k == 8) asm volatile("v_exp_f32 %0, %0" : "+v"(tb));
      if (k == 9) asm volatile("v_max_f32 %0, 0, %0\n\tv_max_f32 %1, 0, %1" : "+v"(za), "+v"(zb));
      if (k == 10) asm volatile("v_add_f32 %0, %0, %2\n\tv_add_f32 %1, %1, %3" : "+v"(za), "+v"(zb) : "v"(ta), "v"(tb));
      if (k == 11) asm volatile("v_add_f32 %0, 0x358637bd, %0\n\tv_add_f32 %1, 0x358637bd, %1" : "+v"(za), "+v"(zb));
      if (k == 12) { unsigned o; asm volatile("v_cvt_pk_bf16_f32 %0, %1, %2" : "=v"(o) : "v"(za), "v"(zb)); pq[n][it][h] = o; } };
    auto kvscatter = [&]() { const int t = wm * 64 + opq(lane0); const u32x2 tb = *(LAS u32x2*)(lds + L_TAB + min(t, 179) * 16 + 8);
      const unsigned k01 = pk2v(lo2(kvs)), k23 = pk2v(hi2(kvs)), msk = t < 180 ? 0xffffu : 0u; LAS char* const a = lds + (t < 180 ? L_KVF : L_DUMMY);
      *(LAS u16*)(a + (tb.x & msk)) = (u16)k01; *(LAS u16*)(a + ((tb.x >> 16) & msk)) = (u16)(k01 >> 16); *(LAS u16*)(a + (tb.y & msk)) = (u16)k23; *(LAS u16*)(a + ((tb.y >> 16) & msk)) = (u16)(k23 >> 16); };
    auto ostep = [&](int it) {
      const int l = opq(lane0), r = l & 15, gg = l >> 4; bf16x8 kf[3][2];
#pragma unroll
      for (int en = 0; en < 3; ++en) { kf[en][0] = *(LAS bf16x8*)(lds + L_KVF + (en * 2) * 1024 + l * 16); kf[en][1] = *(LAS bf16x8*)(lds + L_KVF + (en * 2 + 1) * 1024 + l * 16); }
      { const bf16x8 b0 = cat(pq[0][it], pq[1][it]), b1 = cat(pq[2][it], (u32x2){0, 0}); f32x4 nm[3];
#pragma unroll
        for (int en = 0; en < 3; ++en) nm[en] = mfma(kf[en][1], b1, mfma(kf[en][0], b0, (f32x4){0, 0, 0, 0}));
        const float rA = __builtin_amdgcn_rcpf(__shfl(nm[2][0], 16 + r) + 1e-6f), rB = __builtin_amdgcn_rcpf(__shfl(nm[2][1], 16 + r) + 1e-6f);
        const float rM = gg == 0 ? rA : rB;
#pragma unroll
        for (int en = 0; en < 3; ++en) { const float r01 = en == 0 ? rA : en == 1 ? rM : rB, r23 = en == 0 ? rA : rB;
          const u32x2 o = {pk2v(lo2(nm[en]) * r01), pk2v(hi2(nm[en]) * r23)};
          const int oc4 = 9 * gu + 4 * en + gg;
          int vo = (oc4 >> 3) * 8192 + ((oc4 >> 2) & 1) * 512 + ((oc4 & 3) * 16 + r) * 8;
          if (en == 2) vo = gg != 0 ? OOB : vo;
          __builtin_amdgcn_raw_buffer_store_b64(o, rsWs, vo, yb + (2 * wm + it) * 1024, SC1); } } };
    auto step = [&](bf16x8 (&cur)[2][NR], bf16x8 (&nxt)[2][NR], auto Pc) {
      constexpr int p = decltype(Pc)::value, st = 30 + p, e = p - (H ? 12 : 1);
      auto vop = [&](int k) { if (H == 1 && p < 12) qop(p, k); else if (e >= 0 && e < 4 * NF) zop(e, k); };
      if constexpr (H == 1) { if (p == 14 || p == 15) ostep(p - 14); if (p == 20) cvoff(vA, 2); waitv<allow1(27 + p)>(); }
      else if (H == 0 && p >= 2 && p <= 9) { if (p <= 8) asm volatile("s_waitcnt vmcnt(6) ; HIDDEN_WAIT %0 %1" : "+v"(kl[p & 1][0]), "+v"(kl[p & 1][1]) :: "memory");
                                             else asm volatile("s_waitcnt vmcnt(4) ; HIDDEN_WAIT %0 %1" : "+v"(kl[p & 1][0]), "+v"(kl[p & 1][1]) :: "memory"); }
      else if (H == 0 && p == 1) waitv<6>();
      else if (H == 0 && (p == 18 || p == 20)) waitv<5>();
      else waitv<4>();
      if (p == 31) waitl0();
      bar();
      SB();
      if constexpr (H == 0) {
        if (p >= 2 && p <= 9) { kvs += __builtin_bit_cast(f32x4, kl[p & 1][0]) + __builtin_bit_cast(f32x4, kl[p & 1][1]); asm volatile("" : "+v"(kvs)); }
        if (p < 8) { HLOAD(kl[p & 1][0], kvo, rsWs, colb + C_KVP + 2 * p * KVP_STRIDE); HLOAD(kl[p & 1][1], kvo, rsWs, colb + C_KVP + (2 * p + 1) * KVP_STRIDE); }
        if (p == 10) kvscatter();
        if (p == 17) raise(rowb + C_FLAGO + pj * 4);
        if (p == 19) raise(W_EPOCH, blockIdx.x == 0);
        if (p == 22) codewarm();
        if (p == 27) flags_dma((opq(lane0) & 15) * 4, rowb + C_FLAGO);
        if (p == 28) check_flags(rowb + C_FLAGO + (lane0 & 15) * 4);
      }
      ring(st, 4, p + 3);
      SB();
      vop(0);
#pragma unroll
      for (int i = 0; i < 4 * NF; ++i) {
        const int n = i >> 2, m = (i >> 1) & 1, h = i & 1;
        if (p == 0 && h == 0) mf0(ag[n][m], cur[0][2 + n], cur[0][m]); else mfa(ag[n][m], cur[h][2 + n], cur[h][m]);
        SB();
        if (p != 31 && i < 2 * NR) rd1(nxt, i, p & 3);
        if (i > 0) vop(i);
        if ((i & 3) == 3 && i < 16) ring(st, i >> 2, p + 3);
        SB();
      }
    };
#pragma unroll
    for (int i = 0; i < 2 * NR; ++i) rd1(fa, i, 3);
    STAMP(9);
    sfor<0, 16>([&](auto Kc) __attribute__((always_inline)) { constexpr int k = decltype(Kc)::value; step(fa, fb, IC<2 * k>{}); step(fb, fa, IC<2 * k + 1>{}); if (k == 5) STAMP(8); });
    accfence(ag);
  }
  STAMP(10);
  relane();
  {
    u32x2 y[NF][2];
#pragma unroll
    for (int it = 0; it < 2; ++it) { const float b = blo(*(LAS u16*)(lds + L_BIAS + (32 * wm + 8 * (fr >> 2) + 4 * it + (fr & 3)) * 2));
#pragma unroll
      for (int n = 0; n < NF; ++n) y[n][it] = (u32x2){pk2v(unpk(z1[n][it].x) * (lo2(ag[n][it]) + b)), pk2v(unpk(z1[n][it].y) * (hi2(ag[n][it]) + b))}; }
    waitv<0>();
#pragma unroll
    for (int hh = 0; hh < 2; ++hh)
#pragma unroll
      for (int it = 0; it < 2; ++it) { const u32x4 v = __builtin_bit_cast(u32x4, cat(y[2 * hh][it], y[2 * hh + 1][it]));
        st16<SC1>(v, rsWs, lane * 16, yb + (4 + 2 * gu + H) * 16384 + hh * 8192 + (2 * wm + it) * 1024);
        *(LAS u32x4*)(lds + (H ? L_YOWN : R_OFF + 2 * R_SZ) + hh * 8192 + (2 * wm + it) * 1024 + lane * 16) = v; }
    if constexpr (H == 0) {
#pragma unroll
      for (int it = 0; it < 2; ++it) __builtin_amdgcn_raw_buffer_store_b64(y[4][it], rsWs, lane * 8, yb + 3 * 16384 + (gu >> 1) * 8192 + (2 * wm + it) * 1024 + (gu & 1) * 512, SC1);
    }
  }
  STAMP(11);
  {
    LAS char* const aY = lds + R_OFF + 2 * wm * 1024;
    bf16x8 rf[2][2][2]; s16x4 bq[3][4];
    mk_aC(gc + 1);
    auto rdB = [&](int u, int j) { const int q = u / NF, n = u % NF; if (u < 12 * NF && (q != 2 || j < 2)) bq[u % 3][j] = trC(59 + q, n, j); };
    auto rdY = [&](int q, int j) { const int it = j >> 1, hh = j & 1; LAS char* a = aY + (q == 4 ? L_YOWN - R_OFF : ((59 + q) & 3) * R_SZ) + hh * 8192 + it * 1024;
      if (q == 2) { if (hh == 0) rf[q & 1][it][0] = cat(*(LAS u32x2*)(a + lane * 8), (u32x2){0, 0}); }
      else if (q < 2 || q == 11) rf[q & 1][it][hh] = cat(*(LAS u32x2*)(a + lane * 8), *(LAS u32x2*)(a + 512 + lane * 8));
      else rf[q & 1][it][hh] = *(LAS bf16x8*)(a + lane * 16); };
    rdY(0, 0); rdY(0, 1); rdY(0, 2); rdY(0, 3);
#pragma unroll
    for (int j = 0; j < 8; ++j) rdB(j >> 2, j & 3);
    SB();
    sfor<0, 12 * NF>([&](auto Uc) __attribute__((always_inline)) {
      constexpr int u = decltype(Uc)::value, q = u / NF, n = u % NF;
      if (n == NF - 2 && q < 11) {
        if constexpr (H == 1) { if (q == 10) waitv<0>(); else if (q >= 1) waitv<5>(); }
        else { if (q == 1 || q == 3 || q == 10) waitv<0>(); else if (q >= 4) waitv<4>(); }
        bar();
        if constexpr (H == 0) {
          if (q == 1) raise(rowb + C_FLAGD + gc * 128 + gu * 4);
          if (q == 2) flags_dma((opq(lane0) & 3) * 4, rowb + C_FLAGD + gc * 128);
          if (q == 3) { STAMP(12); check_flags(rowb + C_FLAGD + gc * 128 + (lane0 & 3) * 4); STAMP(13); ring_all(64); }
          if (q == 4) STAMP(14);
        }
        SB();
      }
      const bf16x8 b0 = cat4(bq[u % 3][0], bq[u % 3][1]), b1 = q == 2 ? b0 : cat4(bq[u % 3][2], bq[u % 3][3]);
#pragma unroll
      for (int j = 0; j < 4; ++j) { const int it = j >> 1, hh = j & 1;
        if (q == 0 && hh == 0) mf0(acc[n][it], b0, rf[q & 1][it][hh]); else if (q != 2 || hh == 0) mfa(acc[n][it], hh ? b1 : b0, rf[q & 1][it][hh]);
        SB();
        rdB(u + 2, j);
        if (n == NF - 2 && q < 11) rdY(q + 1, j);
        if (j == 3) { const int d = u - (NF - 2); if (d >= 0) { const int qq = d / NF + 3; if (qq < 12 && (H == 1 || qq >= 6)) ring_pos(59 + qq, d % NF, NF); } }
        SB(); }
    });
    accfence(acc);
  }
  STAMP(15);
  relane();
  {
    const rsrc_t rsOut = mkrs(P.out + (size_t)pi * 128 * D, 128 * D * 2);
    auto packp = [&](const f32x4 (&a)[2]) { u32x4 v = {pk2v(lo2(a[0])), pk2v(hi2(a[0])), pk2v(lo2(a[1])), pk2v(hi2(a[1]))};
      v.x = min(v.x, 0xfffffffeu); v.y = min(v.y, 0xfffffffeu); v.z = min(v.z, 0xfffffffeu); v.w = min(v.w, 0xfffffffeu); return v; };
    constexpr int NK = H ? 1 : 2;
    f32x4 kp[NK][2]; int cb[NK];
#pragma unroll
    for (int it = 0; it < 2; ++it) { kp[0][it] = acc[3][it]; if constexpr (H == 0) kp[1][it] = acc[4][it]; }
    cb[0] = 256 * gu + 32 * (H * 4 + gc); if constexpr (H == 0) cb[1] = 1024 + 32 * gu;
    const int off1 = wm == gc ? 0 : OOB;
    int ro[2]; u32x2 xr[NK][2];
#pragma unroll
    for (int it = 0; it < 2; ++it) { ro[it] = (32 * wm + 8 * (fr >> 2) + 4 * it + (fr & 3)) * 1152 + 8 * g;
#pragma unroll
      for (int k = 0; k < NK; ++k) xr[k][it] = __builtin_amdgcn_raw_buffer_load_b64(rsX, ro[it] + (k ? off1 : 0), cb[k], 0); }
#pragma unroll
    for (int n = 0; n < NF; ++n) { if (n == 3) continue; const int dg = n < 4 ? (n + gc + 1) & 3 : wm, slot = n < 4 ? H * 4 + wm : 8;
      st16<SC1>(packp(acc[n]), rsWs, dg == gc ? OOB : lane * 16, rowb + C_PART + ((4 * dg + gu) * 4 + gc) * PART_SRC + slot * 1024); }
    const int pb = rowb + C_PART + pj * 4 * PART_SRC;
    u32x4 v[NK][3];
    __builtin_amdgcn_s_sleep(FIN_SLEEP);
    _Pragma("unroll 1") for (unsigned sp = 0;; ++sp) {
      unsigned mx = 0;
#pragma unroll
      for (int k = 0; k < NK; ++k)
#pragma unroll
        for (int j = 0; j < 3; ++j) { const int so = pb + ((gc + 1 + j) & 3) * PART_SRC + (k ? 8 : H * 4 + wm) * 1024;
          v[k][j] = __builtin_amdgcn_raw_buffer_load_b128(rsWs, lane * 16 + (k ? off1 : 0), so, SC1);
          mx = max(max(mx, max(v[k][j].x, v[k][j].y)), max(v[k][j].z, v[k][j].w)); }
      if (LIKELY(__all(mx != ~0u))) break;
      if (sp > SPIN_MAX) { set_flag(P.ws, W_TMO, 1u); break; }
      __builtin_amdgcn_s_sleep(FIN_POLL);
    }
#pragma unroll
    for (int k = 0; k < NK; ++k)
#pragma unroll
      for (int it = 0; it < 2; ++it) { f32x2 a = lo2(kp[k][it]) + unpk(xr[k][it].x), b = hi2(kp[k][it]) + unpk(xr[k][it].y);
#pragma unroll
        for (int j = 0; j < 3; ++j) { a += unpk(v[k][j][2 * it]); b += unpk(v[k][j][2 * it + 1]); }
        __builtin_amdgcn_raw_buffer_store_b64((u32x2){pk2v(a), pk2v(b)}, rsOut, ro[it] + (k ? off1 : 0), cb[k], SC1); }
  }
  if constexpr (H == 0) asm volatile(".Lmk_end:");
}

__global__ __launch_bounds__(512, 2) void fused(const u16* x, const u16* wqkv, const u16* wo, const u16* win, const u16* wsp, const u16* bsp, const u16* wout, u16* out, char* ws) {
  extern __shared__ __attribute__((aligned(16))) char smem[];
  LAS char* lds = (LAS char*)smem;
  const int lane = threadIdx.x & 63, wid = __builtin_amdgcn_readfirstlane(threadIdx.x >> 6);
  const int b = blockIdx.x, xc = b & 7, q = b >> 3;
  const int pi = 8 * (xc & 1) + (q & 7), pj = 4 * (xc >> 1) + (q >> 3);
  const unsigned epoch = *(const __attribute__((address_space(4))) unsigned*)(ws + W_EPOCH);
  const Ptrs P{x, wqkv, wo, win, wsp, bsp, wout, out, ws};
  unsigned long long pc; int clen;
  asm volatile("s_getpc_b64 %0\n.Lmk_pc:\n\ts_mov_b32 %1, .Lmk_end-.Lmk_pc" : "=s"(pc), "=s"(clen));
  const rsrc_t rsCode = mkrs((const void*)pc, clen);
  if (wid < 4) body<0>(P, lds, wid, lane, pi, pj, epoch, rsCode); else body<1>(P, lds, wid - 4, lane, pi, pj, epoch, rsCode);
}
}

extern "C" void kernel_launch(void* const* d_in, const int* in_sizes, int n_in, void* d_out, int out_size, void* d_ws, size_t ws_size, hipStream_t stream) {
  using mk::u16;
  (void)hipFuncSetAttribute((const void*)mk::fused, hipFuncAttributeMaxDynamicSharedMemorySize, mk::LDS_TOTAL);
  mk::fused<<<256, 512, mk::LDS_TOTAL, stream>>>((const u16*)d_in[0], (const u16*)d_in[1], (const u16*)d_in[2], (const u16*)d_in[4], (const u16*)d_in[5], (const u16*)d_in[6],
                                                 (const u16*)d_in[7], (u16*)d_out, (char*)d_ws);
}
```

```cpp
#include <hip/hip_runtime.h>
#include <hip/hip_bf16.h>
#include <type_traits>

namespace mk {
typedef unsigned short u16;
typedef short bf16x8 __attribute__((ext_vector_type(8)));
typedef short s16x4 __attribute__((ext_vector_type(4)));
typedef float f32x4 __attribute__((ext_vector_type(4)));
typedef float f32x2 __attribute__((ext_vector_type(2)));
typedef unsigned u32x4 __attribute__((ext_vector_type(4)));
typedef unsigned u32x3 __attribute__((ext_vector_type(3)));
typedef unsigned u32x2 __attribute__((ext_vector_type(2)));
typedef __bf16 bf16x2_t __attribute__((ext_vector_type(2)));
#define LAS __attribute__((address_space(3)))
#define GAS __attribute__((address_space(1)))
#define DI __device__ __forceinline__
template <int V> using IC = std::integral_constant<int, V>;
template <int B, int E, class F> __device__ __forceinline__ void sfor(F&& f) { if constexpr (B < E) { f(IC<B>{}); sfor<B + 1, E>(f); } }

constexpr int S = 2048, D = 576, FH = 2304;
constexpr int L_KVF = 0, L_ZPART = 6144, L_CS = 11520, L_TAB = L_CS + 1792;
constexpr int L_LAND0 = 0, L_YOWN = 0;
constexpr int L_BIAS = 16384, L_FLG = L_BIAS + 256, L_DUMMY = L_FLG + 1024, L_XSTAT = L_DUMMY + 1536;
static_assert(L_TAB + 3072 <= L_BIAS && L_XSTAT + 1024 <= 20480, "lds");
constexpr int C_SZ = 18432, CM_OFF = 20480, CM_SZ = 16384, CT_OFF = CM_OFF + 65536, CT_SZ = 2048;
constexpr int R_OFF = CT_OFF + 8192, R_SZ = 16384;
constexpr int L_STASH = R_OFF, L_KVS = R_OFF + 36864, L_XLAND = R_OFF + 4 * R_SZ;
constexpr int LDS_TOTAL = 163840;
static_assert(L_KVS + 32768 <= LDS_TOTAL, "lds");
constexpr int W_EPOCH = 0, W_TMO = 256, W_COL = 2 << 20, W_ROW = 18 << 20, W_END = 50 << 20;
constexpr int COL_SZ = 1 << 20, C_ZF = 0, C_KVP = 0x90000, C_CSG = 0xA0000, C_FLAGB = 0xB0000, C_WT = 0xC0000;
constexpr int ROW_SZ = 2 << 20, C_YF = 0, C_PART = 0xC0000, C_STATS = 0x150000, C_FLAGO = 0x158000, C_FLAGD = 0x158100, C_XST = 0x159000;
constexpr int KVP_STRIDE = 2944, YF_GRP = 12 * 16384, PART_SRC = 9216, OOB = 0x40000000;
constexpr int SC1 = 16;
constexpr unsigned SPIN_MAX = 1u << 18;
constexpr int csg_at(int ps) { return ps == 0 ? 5 : ps == 1 ? 1 : 4; }
constexpr int vpost1(int sg) { return sg == -1 ? 3 : sg == 2 ? 2 : sg == 9 ? 9 : sg == 13 || sg == 14 || sg == 17 ? 4 : sg == 11 ? 1 : sg == 18 ? 7 : sg == 19 || sg == 21 || sg == 26 ? 1 : sg == 27 + 15 ? 6 : (sg >= 27 + 1 && sg <= 27 + 8) ? 2 : sg == 27 + 27 ? 1 : 0; }
constexpr int npc(int st) { return st >= 9 && st < 18 ? 4 : 5; }
constexpr int allow1(int sg) { return npc(sg + 2) + vpost1(sg - 1) + vpost1(sg); }

template <int N> DI void waitv() { asm volatile("s_waitcnt vmcnt(%0)" ::"n"(N) : "memory"); }
DI void waitl0() { __builtin_amdgcn_s_waitcnt(0xC07F); asm volatile("" ::: "memory"); }
DI void bar() { asm volatile("" ::: "memory"); __builtin_amdgcn_s_barrier(); asm volatile("" ::: "memory"); }
#define SB() __builtin_amdgcn_sched_barrier(0)
DI f32x4 mfma(bf16x8 a, bf16x8 b, f32x4 c) { return __builtin_amdgcn_mfma_f32_16x16x32_bf16(a, b, c, 0, 0, 0); }
DI void mfa(f32x4& c, const bf16x8& a, const bf16x8& b) { asm volatile("v_mfma_f32_16x16x32_bf16 %0, %1, %2, %0" : "+v"(c) : "v"(a), "v"(b)); }
DI void mf0(f32x4& c, const bf16x8& a, const bf16x8& b) { asm volatile("v_mfma_f32_16x16x32_bf16 %0, %1, %2, 0" : "=&v"(c) : "v"(a), "v"(b)); }
DI void mfc(f32x4& d, const bf16x8& a, const bf16x8& b, const f32x4& c) { asm volatile("v_mfma_f32_16x16x32_bf16 %0, %1, %2, %3" : "=&v"(d) : "v"(a), "v"(b), "v"(c)); }
DI LAS char* pin(LAS char* p) { asm volatile("" : "+v"(p)); return p; }
DI unsigned tagor(unsigned v, unsigned tag, unsigned acc) { return __builtin_amdgcn_bitop3_b32(v, tag, acc, 0xBE); }
DI int opq(int v) { asm volatile("" : "+v"(v)); return v; }
template <int N> DI void accfence(f32x4 (&a)[N][2]) {
  if constexpr (N == 5) asm volatile("s_nop 7\n\ts_nop 1" : "+v"(a[0][0]), "+v"(a[0][1]), "+v"(a[1][0]), "+v"(a[1][1]), "+v"(a[2][0]), "+v"(a[2][1]), "+v"(a[3][0]), "+v"(a[3][1]), "+v"(a[4][0]), "+v"(a[4][1]));
  else asm volatile("s_nop 7\n\ts_nop 1" : "+v"(a[0][0]), "+v"(a[0][1]), "+v"(a[1][0]), "+v"(a[1][1]), "+v"(a[2][0]), "+v"(a[2][1]), "+v"(a[3][0]), "+v"(a[3][1]));
}
DI unsigned pk2(float lo, float hi) { f32x2 v = {lo, hi}; return __builtin_bit_cast(unsigned, __builtin_convertvector(v, bf16x2_t)); }
DI float blo(unsigned u) { return __uint_as_float(u << 16); }
DI float dot2(unsigned a, unsigned b, float c) { return __builtin_amdgcn_fdot2_f32_bf16(__builtin_bit_cast(bf16x2_t, a), __builtin_bit_cast(bf16x2_t, b), c, false); }
DI f32x2 gelu2(f32x2 v) {
  const f32x2 a = v * __builtin_elementwise_fma(v * v, (f32x2){0.10294324f, 0.10294324f}, (f32x2){2.3022082f, 2.3022082f});
  const f32x2 d = (f32x2){__builtin_amdgcn_exp2f(a.x), __builtin_amdgcn_exp2f(a.y)} + 1.0f;
  const f32x2 r = {__builtin_amdgcn_rcpf(d.x), __builtin_amdgcn_rcpf(d.y)};
  return __builtin_elementwise_fma(-v, r, v);
}
DI unsigned pk2v(f32x2 v) { return __builtin_bit_cast(unsigned, __builtin_convertvector(v, bf16x2_t)); }
DI f32x2 unpk(unsigned u) { return (f32x2){__uint_as_float(u << 16), __uint_as_float(u & 0xffff0000u)}; }
DI f32x2 lo2(f32x4 a) { return (f32x2){a.x, a.y}; }
DI f32x2 hi2(f32x4 a) { return (f32x2){a.z, a.w}; }
DI f32x2 fma2(f32x2 a, f32x2 b, f32x2 c) { return __builtin_elementwise_fma(a, b, c); }
DI f32x2 phi2(f32x2 t) {
  const f32x2 a = t * 1.44269504f, p = t + 1.0f;
  const f32x2 r = {t.x > 0.f ? p.x : __builtin_amdgcn_exp2f(a.x), t.y > 0.f ? p.y : __builtin_amdgcn_exp2f(a.y)};
  return r + 1e-6f;
}
template <int N> DI float bcast16(float v) { return __builtin_bit_cast(float, __builtin_amdgcn_update_dpp(0, __builtin_bit_cast(int, v), 0x150 + N, 0xf, 0xf, true)); }
template <int N> DI float rorsum(float v) { return v + __builtin_bit_cast(float, __builtin_amdgcn_update_dpp(0, __builtin_bit_cast(int, v), 0x120 + N, 0xf, 0xf, true)); }
DI float half32sum(float v) { v = rorsum<1>(rorsum<2>(rorsum<4>(rorsum<8>(v)))); const unsigned u = __float_as_uint(v); const auto r = __builtin_amdgcn_permlane16_swap(u, u, false, false); return __uint_as_float(r[0]) + __uint_as_float(r[1]); }
DI float ror8sum(float v) { return v + __builtin_bit_cast(float, __builtin_amdgcn_update_dpp(0, __builtin_bit_cast(int, v), 0x128, 0xf, 0xf, true)); }
typedef __amdgpu_buffer_rsrc_t rsrc_t;
DI rsrc_t mkrs(const void* p, int bytes) { return __builtin_amdgcn_make_buffer_rsrc((void*)p, 0, bytes, 0x00020000); }
template <int AUX> DI void dma16(rsrc_t rs, LAS char* dst, int voff, int soff) {
  const unsigned m = (unsigned)(size_t)dst;
  if constexpr (AUX == 0) asm volatile("s_mov_b32 m0, %0\n\ts_nop 0\n\tbuffer_load_dwordx4 %1, %2, %3 offen lds" ::"s"(m), "v"(voff), "s"(rs), "s"(soff) : "memory");
  else asm volatile("s_mov_b32 m0, %0\n\ts_nop 0\n\tbuffer_load_dwordx4 %1, %2, %3 offen sc1 lds" ::"s"(m), "v"(voff), "s"(rs), "s"(soff) : "memory");
}
DI void dma4(rsrc_t rs, LAS char* dst, int voff, int soff) {
  const unsigned m = (unsigned)(size_t)dst;
  asm volatile("s_mov_b32 m0, %0\n\ts_nop 3\n\tbuffer_load_dword %1, %2, %3 offen sc1 lds" ::"s"(m), "v"(voff), "s"(rs), "s"(soff) : "memory");
}
template <int AUX> DI void st16(u32x4 v, rsrc_t rs, int voff, int soff) { asm volatile("" : "+v"(v)); __builtin_amdgcn_raw_buffer_store_b128(v, rs, voff, soff, AUX); asm volatile("s_nop 1" ::"v"(v) : "memory"); }
#define UNLIKELY(x) __builtin_expect(!!(x), 0)
#define HLOAD(dst, voff, rs, soff) asm volatile("buffer_load_dwordx4 %0, %1, %2, %3 offen sc1 ; HIDDEN_LOAD" : "=v"(dst) : "v"(voff), "s"(rs), "s"(soff) : "memory")
DI s16x4 tr1(LAS char* a) { return __builtin_amdgcn_ds_read_tr16_b64_v4i16((LAS s16x4*)a); }
DI bf16x8 cat4(s16x4 a, s16x4 b) { return __builtin_shufflevector(a, b, 0, 1, 2, 3, 4, 5, 6, 7); }
DI bf16x8 cat(u32x2 a, u32x2 b) { u32x4 v = {a.x, a.y, b.x, b.y}; return __builtin_bit_cast(bf16x8, v); }

struct Ptrs { const u16 *x, *wqkv, *wo, *win, *wsp, *bsp, *wout; u16* out; char* ws; };

DI void poll_flags(char* ws, int off, bool active, unsigned tag) {
  GAS unsigned* p = (GAS unsigned*)(ws + off);
  _Pragma("unroll 1") for (unsigned sp = 0;; ++sp) {
    const unsigned v = __hip_atomic_load(p, __ATOMIC_RELAXED, __HIP_MEMORY_SCOPE_AGENT);
    if (__all(!active || v == tag)) break;
    if (sp > SPIN_MAX) { __hip_atomic_store((GAS unsigned*)(ws + W_TMO), 1u, __ATOMIC_RELAXED, __HIP_MEMORY_SCOPE_AGENT); break; }
    __builtin_amdgcn_s_sleep(2);
  }
  asm volatile("" ::: "memory");
}
DI void set_flag(char* ws, int off, unsigned tag) { __hip_atomic_store((GAS unsigned*)(ws + off), tag, __ATOMIC_RELAXED, __HIP_MEMORY_SCOPE_AGENT); }

template <int H>
DI void body(const Ptrs& P, LAS char* lds, const int wm, const int lane0, const int pi, const int pj) {
  constexpr int NF = H ? 4 : 5, NB = H ? 5 : 0;
  int lane = lane0, fr = lane & 15, g = lane >> 4;
  auto relane = [&]() { lane = opq(lane0); fr = lane & 15; g = lane >> 4; };
  const int gc = pj >> 2, gu = pj & 3, qo = 4 * (pj & 1);
  const int rot = (pi + 2 * pj) % 9;
  const rsrc_t rsX = mkrs(P.x + (size_t)pi * 128 * D, 128 * D * 2), rsWin = mkrs(P.win, D * 2 * FH * 2), rsQkv = mkrs(P.wqkv, D * 3 * D * 2);
  const rsrc_t rsSp = mkrs(P.wsp + (size_t)pi * 128 * S, 128 * S * 2), rsWs = mkrs(P.ws, W_END);
  const rsrc_t rsWout = mkrs(P.wout, FH * D * 2), rsWo = mkrs(P.wo, D * D * 2);
  const int colb = W_COL + pj * COL_SZ, rowb = W_ROW + pi * ROW_SZ, yb = rowb + C_YF + gc * YF_GRP;

  const int l16 = opq(lane0 * 16);
  auto rvoff = [&](int stride) { const int l = opq(lane0); return (8 * (l >> 5) + ((l >> 3) & 3)) * stride + ((l & 7) ^ (l >> 4)) * 16; };
  int vA[2], vB[2];
  auto cvoff = [&](int (&v)[2], int kind) {
    const int l = opq(lane0), stride = kind == 0 ? 9216 : kind == 1 ? 3456 : 1152, t = kind == 0 ? pj : gu, b0 = 72 * pj - 8 * (pj & 1);
    const int sc = (l & 15) ^ ((4 * (wm & 1) + (l >> 4)) << 1), rw = (kind == 2 ? 16 * (wm >> 1) + 4 * (wm & 1) : 8 * (wm & 1) + 4 * (wm >> 1)) + (l >> 4);
    int add = 256 * t;
    if (kind == 1) { add = 1152; add = sc >= 4 ? 2304 - 64 : add; add = sc >= 8 ? 1152 - 64 : add; add = sc >= 9 ? 2304 - 80 : add; add = sc >= 10 ? -160 : add; add = sc >= 15 ? OOB : add; add += b0; }
    v[0] = rw * stride + 16 * sc + add;
    const int Pp = 32 * wm + (l >> 1), k = kind == 2 ? (Pp & ~24) | ((Pp & 8) << 1) | ((Pp & 16) >> 1) : (Pp & ~12) | ((Pp & 4) << 1) | ((Pp & 8) >> 1);
    const int tb = (l & 1) * 16 + (kind == 0 ? 4096 : 1024) + 32 * t;
    v[1] = (wm >= 2 || kind == 1) ? OOB : k * stride + tb;
    if (kind != 0) asm volatile("" : "+v"(v[0])); else asm volatile("" : "+v"(v[0]), "+v"(v[1]));
  };
  auto ksl = [&](int s) { int k = rot + s; return k >= 9 ? k - 9 : k; };
  auto kord = [&](int p) { return p < 2 ? 2 * pi + p : (p - 2 < 2 * pi ? p - 2 : p); };
  auto g3blk = [&](int q) { return q < 5 ? gu : (gu + 1 + (q - 5) % 3) & 3; };
  auto g3stage = [&](int q) { return q < 3 ? q : q == 11 ? 3 : 4 + 2 * g3blk(q) + (q == 4 || q >= 8); };
  auto ring = [&](int st, int idx, int dyn = 0) {
    const int slot = st & 3;
    if constexpr (H == 1) {
      LAS char* dst = lds + (idx < 4 ? CM_OFF + slot * CM_SZ + (wm + 4 * idx) * 1024 : wm >= 2 ? L_DUMMY : CT_OFF + slot * CT_SZ + wm * 1024);
      const int tl = idx == 4, r1 = tl ? 0 : 16 * idx, r3 = tl ? 0 : 32 * (idx >> 1) + 8 * (idx & 1);
      if (st < 9) dma16<0>(rsWin, dst, vA[tl], 4608 + (ksl(st) * 64 + r1) * 9216);
      else if (st < 18) { if (!tl) dma16<0>(rsQkv, dst, vB[0], (ksl(st - 9) * 64 + r1) * 3456); }
      else if (st < 27) dma16<0>(rsWin, dst, vA[tl], (ksl(st - 18) * 64 + r1) * 9216);
      else if (st < 59) dma16<SC1>(rsWs, dst, tl ? vB[1] : l16, colb + C_ZF + kord(dyn) * C_SZ + (wm + 4 * idx) * 1024);
      else if (tl) dma16<SC1>(rsWs, dst, vB[1], colb + C_WT + g3stage(st - 59) * 2048 + wm * 1024);
      else { const int q = st - 59;
        if (q < 2) dma16<0>(rsWo, dst, vA[tl], (144 * gc + 64 * q + r3) * 1152);
        else if (q == 2) dma16<0>(rsWo, dst, vA[tl] + (144 * gc + 128 + r3) * 1152, 0);
        else if (q == 11) dma16<0>(rsWout, dst, vA[tl], (2048 + 64 * gc + r3) * 1152);
        else dma16<0>(rsWout, dst, vA[tl], (512 * gc + 128 * g3blk(q) + 32 * (q == 4 || q >= 8) + r3 + 32 * (idx >> 1)) * 1152); }
    } else {
      if (idx == 4) return;
      LAS char* dst = lds + R_OFF + slot * R_SZ + (4 * wm + idx) * 1024;
      const int rb = 32 * wm + 16 * (idx & 1) + 4 * (idx >> 1);
      if (st < 9) dma16<0>(rsX, dst, vA[idx & 1], rb * 1152 + ksl(st) * 128);
      else if (st < 27) return;
      else if (st < 59) dma16<0>(rsSp, dst, vA[idx & 1], rb * 4096 + kord(dyn) * 128);
      else dma16<SC1>(rsWs, dst, l16, yb + g3stage(st - 59) * 16384 + (4 * wm + idx) * 1024);
    }
  };
  auto ring_all = [&](int st) { ring(st, 4); ring(st, 0); ring(st, 1); ring(st, 2); ring(st, 3); };
  auto ring_pos = [&](int st, int k, int nf) {
    const int dyn = st - 27;
    if (nf == 5) { ring(st, k == 0 ? 4 : k - 1, dyn); }
    else if (nf == 4) { if (k == 0) ring(st, 4, dyn); ring(st, k, dyn); }
    else { if (k == 0) { ring(st, 4, dyn); ring(st, 0, dyn); } else if (k == 1) { ring(st, 1, dyn); ring(st, 2, dyn); } else ring(st, 3, dyn); }
  };
  if constexpr (H == 1) cvoff(vA, 0); else { vA[0] = rvoff(1152); vA[1] = vA[0] ^ 64; }
  u32x3 xw[3];
  ring_all(0);
  if constexpr (H == 1) ring_all(1);
  if constexpr (H == 1) { const int vo = (8 * pj + 2 * wm + (lane >> 5)) * 1152 + (lane & 31) * 12;
#pragma unroll
    for (int k = 0; k < 3; ++k) asm volatile("buffer_load_dwordx3 %0, %1, %2, 0 offen offset:%3 ; HIDDEN_LOAD" : "=v"(xw[k]) : "v"(vo), "s"(rsX), "n"(k * 384) : "memory"); }
  if constexpr (H == 1) cvoff(vB, 1);
  else {
    const int t = wm * 64 + lane;
    const int rd = (t * 205) >> 10, e0 = 4 * (t - 5 * rd), hd = rd >= 18, dc = qo + rd;
    const int gb = (dc >> 4) * 3072 - (dc >> 5) * 1024 + ((dc >> 2) & 3) * 256 + (dc & 3) * 4, sb = (dc >> 5) * 1024 + ((dc >> 2) & 3) * 256 + ((dc >> 4) & 1) * 8 + (dc & 3) * 2;
    unsigned go[4], so[4];
#pragma unroll
    for (int k = 0; k < 4; ++k) { const int e = e0 + k, ce = qo + 18 * hd + e; int off = (ce >> 4) * 1024 + (ce & 15) * 16; off = ce >= 32 ? off + 128 : off; off = e >= 18 ? 2048 : off;
      int ep = 18 * hd + e; ep = e == 18 ? 36 + hd : ep; ep = e == 19 ? 40 : ep;
      go[k] = gb + off; so[k] = sb + (ep >> 4) * 2048 + (ep & 15) * 16; }
    if (t < 180) *(LAS u32x4*)(lds + L_TAB + t * 16) = (u32x4){go[0] | (go[1] << 16), go[2] | (go[3] << 16), so[0] | (so[1] << 16), so[2] | (so[3] << 16)};
  }
  if constexpr (H == 0) ring_all(1);
  LAS char *aR0, *aR1, *aC[NF + 1];
  auto mk_aR = [&]() { const int l = opq(lane0), r = l & 15, o = (32 * wm + r) * 128 + (((l >> 4) ^ ((r >> 1) & 7)) * 16); aR0 = pin(lds + R_OFF + o); aR1 = pin(lds + R_OFF + (o ^ 64)); };
  auto mk_aC = [&](int rotn) { const int l = opq(lane0), r0 = 16 * (l >> 5) + ((l >> 2) & 7);
#pragma unroll
    for (int n = 0; n < NF + 1 - H; ++n) aC[n] = pin(lds + (n != 4 ? CM_OFF + r0 * 256 + (((n == 5 ? 4 : 4 * H + ((n + rotn) & 3)) ^ (r0 & 7)) * 32) : CT_OFF + r0 * 32) + (l & 3) * 8); };
  auto trC = [&](int st, int n, int j) { const int m = (st >= 9 && st < 18) ? (H ? n + 1 : n == 4 ? 5 : n) : n;
    return tr1(aC[m] + (m != 4 ? (st & 3) * CM_SZ + (j >> 1) * 8192 + (j & 1) * 2048 : (st & 3) * CT_SZ + (j >> 1) * 1024 + (j & 1) * 256)); };
  mk_aC(0); mk_aR();
  unsigned epoch;
  asm volatile("s_load_dword %0, %1, 0x0\n\ts_waitcnt lgkmcnt(0)" : "=s"(epoch) : "s"(P.ws) : "memory");
  const unsigned tag = epoch * 0x9E3779B1u + 0x7F4A7C15u;
  unsigned ONES = 0x3f803f80u; asm volatile("" : "+s"(ONES));
  float xnm = 0.f, xrs = 0.f;
  auto raise = [&](int off, bool on = true) { __builtin_amdgcn_raw_buffer_store_b32(tag, rsWs, (opq(lane0) == 0 && wm == 0 && on) ? 0 : OOB, off, SC1); };
  auto flags_dma = [&](int voff, int soff) { dma4(rsWs, lds + L_FLG + wm * 256, voff, soff); };
  auto check_flags = [&](int off) { const unsigned v = *(LAS unsigned*)(lds + L_FLG + wm * 256 + opq(lane0) * 4);
    if (UNLIKELY(!__all(v == tag))) { poll_flags(P.ws, off, true, tag); } };

  auto sentinel = [&](int i) { const int k = i * 4 + wm, src = k / 9;
    st16<SC1>((u32x4){~0u, ~0u, ~0u, ~0u}, rsWs, k < 27 ? l16 : OOB, rowb + C_PART + (pj * 4 + ((gc + 1 + src) & 3)) * PART_SRC + (k - 9 * src) * 1024); };
  auto cs_early = [&](int ps) { const int l = opq(lane0), q = l >> 4, st = 9 * ps; const bf16x8 o8 = __builtin_bit_cast(bf16x8, (u32x4){ONES, ONES, ONES, ONES}); float c[3];
#pragma unroll
    for (int k = 0; k < 3; ++k) { if (k == 2 && ps == 1) { c[k] = 0.f; continue; }
      LAS char* const a = k < 2 ? (LAS char*)((unsigned)(size_t)aC[0] ^ (((2 * wm + k) ^ 4) << 5)) + (st & 3) * CM_SZ : lds + CT_OFF + (st & 3) * CT_SZ + (16 * (l >> 5) + ((l >> 2) & 7)) * 32 + (l & 3) * 8;
      const int h = k < 2 ? 8192 : 1024, t = k < 2 ? 2048 : 256;
      c[k] = mfma(o8, cat4(tr1(a + h), tr1(a + h + t)), mfma(o8, cat4(tr1(a), tr1(a + t)), (f32x4){0, 0, 0, 0})).x; }
    const int sl = 2 * wm + q, fg = q == 2 ? (ps == 1 ? 8 : 4) : sl + (ps == 2 && sl >= 4);
    __builtin_amdgcn_raw_buffer_store_b64((u32x2){__float_as_uint(q == 0 ? c[0] : q == 1 ? c[1] : c[2]), tag}, rsWs, (pi < 9 && (q < 2 || (q == 2 && wm == 0))) ? (fg * 16 + (l & 15)) * 8 : OOB, colb + C_CSG + ps * 12288 + rot * 1152, SC1); };
  constexpr int NK = H ? 1 : 2;
  int cb[NK], ro; u32x4 xr[NK];
  cb[0] = 256 * gu + 32 * (H * 4 + gc); if constexpr (H == 0) cb[1] = 1024 + 32 * gu;
  const int off1 = wm == gc ? 0 : OOB;
  auto mk_ro = [&]() { const int l = opq(lane0); return (32 * wm + 8 * ((l >> 2) & 3) + 4 * ((l >> 4) & 1) + (l & 3)) * 1152 + 16 * (l >> 5); };
  auto resid = [&]() { const int o = mk_ro();
#pragma unroll
    for (int k = 0; k < NK; ++k) asm volatile("buffer_load_dwordx4 %0, %1, %2, %3 offen ; HIDDEN_LOAD" : "=v"(xr[k]) : "v"(o + (k ? off1 : 0)), "s"(rsX), "s"(cb[k]) : "memory"); };
  auto resid_in = [&]() {
#pragma unroll
    for (int k = 0; k < NK; ++k) asm volatile("; HIDDEN_WAIT %0 (covered by the wait of barrier 24)" : "+v"(xr[k]) :: "memory"); };
  u32x4 wt;
  bf16x8 xf[9][2][2];
  f32x4 acc[NF][2];
  f32x4 zm[2], zr2[2];
  LAS char* zsw;
  auto z2_read = [&](int n) { return *(LAS u32x4*)(zsw + n * 1024); };
  auto z2n_tile = [&](int n, u32x4 z) {
    const u32x4 o = {pk2v(fma2(unpk(z.x), lo2(zr2[0]), lo2(zm[0]))), pk2v(fma2(unpk(z.y), hi2(zr2[0]), hi2(zm[0]))), pk2v(fma2(unpk(z.z), lo2(zr2[1]), lo2(zm[1]))), pk2v(fma2(unpk(z.w), hi2(zr2[1]), hi2(zm[1])))};
    st16<SC1>(o, rsWs, l16, colb + C_ZF + (2 * pi + (wm >> 1)) * C_SZ + (n == 4 ? 16 + (wm & 1) : (wm & 1) * 8 + (n < 4 ? n : n - 1)) * 1024); };

  auto pass = [&](auto PASSc, auto&& tailjob) {
    constexpr int PASS = decltype(PASSc)::value;
    constexpr int NFP = (PASS == 1 && H == 1) ? 3 : NF, ST0 = 9 * PASS, NU = 9 * NFP;
    constexpr bool TR = (PASS == 2) || (PASS == 1 && H == 1);
    constexpr int L_LAND = PASS == 0 ? L_LAND0 : PASS == 1 ? L_KVS : R_OFF + 2 * R_SZ;
    const int csg = colb + C_CSG + PASS * 12288;
    s16x4 bq[3][4];
    u32x4 gl[8]; u32x2 zp[2], t2[9]; float tf[16]; f32x4 ct; float csv = 0.f, cs5 = 0.f;
    auto rdB = [&](int u, int j) { const int s = u / NFP, n = u % NFP; if (u < NU) bq[u % 3][j] = trC(ST0 + s, n, j); };
    auto rdX = [&](int s, int j) { xf[s][j >> 1][j & 1] = *(LAS bf16x8*)(((j & 1) ? aR1 : aR0) + (s & 3) * R_SZ + (j >> 1) * 2048); };
    auto slot = [&](auto SGc) __attribute__((always_inline)) {
      constexpr int sg = decltype(SGc)::value, s = sg - ST0;
      if (sg == 2) { const int l = opq(lane0); const bool on = wm == 0 && pi < 9;
        __builtin_amdgcn_raw_buffer_store_b64((u32x2){__float_as_uint(csv), tag}, rsWs, on ? (NB * 16 + l) * 8 : OOB, csg + rot * 1152, SC1);
        if constexpr (H == 0) __builtin_amdgcn_raw_buffer_store_b64((u32x2){__float_as_uint(cs5), tag}, rsWs, (on && l < 16) ? (64 + l) * 8 : OOB, csg + rot * 1152, SC1); }
      if constexpr (H == 1) {
        if (sg == 1) { float s1 = 0.f;
          asm volatile("s_waitcnt vmcnt(10) ; HIDDEN_WAIT %0 %1 %2" : "+v"(xw[0]), "+v"(xw[1]), "+v"(xw[2]) :: "memory");
#pragma unroll
          for (int k = 0; k < 9; ++k) s1 = dot2(xw[k / 3][k % 3], ONES, s1);
          xnm = half32sum(s1) * (-1.0f / D); asm volatile("" : "+v"(xnm)); }
        if (sg == 2) { float s2 = 0.f;
#pragma unroll
          for (int k = 0; k < 9; ++k) { const f32x2 d = unpk(xw[k / 3][k % 3]) + xnm; s2 = __builtin_fmaf(d.x, d.x, __builtin_fmaf(d.y, d.y, s2)); }
          xrs = __builtin_amdgcn_rsqf(half32sum(s2) * (1.0f / D) + 1e-5f);
          const int l = opq(lane0); st16<SC1>((u32x4){__float_as_uint(xnm), tag, __float_as_uint(xrs), tag}, rsWs, (l & 31) == 0 ? (l >> 5) * 16 : OOB, rowb + C_XST + (8 * pj + 2 * wm) * 16); }
        if (sg == 8) { const int a = (wm * 64 + opq(lane0)) & 127;
          unsigned bad = tagor(gl[0].y, tag, gl[0].w ^ tag);
          if (UNLIKELY(!__all(bad == 0))) {
            _Pragma("unroll 1") for (unsigned sp = 0; !__all(bad == 0); ++sp) {
              if (sp > SPIN_MAX) { set_flag(P.ws, W_TMO, 1u); break; }
              __builtin_amdgcn_s_sleep(1); asm volatile("" ::: "memory");
              gl[0] = __builtin_amdgcn_raw_buffer_load_b128(rsWs, a * 16, rowb + C_XST, SC1); bad = tagor(gl[0].y, tag, gl[0].w ^ tag);
            } }
          LAS char* const o = lds + L_XSTAT + ((a & ~28) | ((a & 4) << 2) | ((a >> 1) & 12)) * 4;
          *(LAS unsigned*)o = gl[0].x; *(LAS unsigned*)(o + 512) = gl[0].z; }
        if (s == csg_at(PASS) + 3) { const int t = min(wm * 64 + opq(lane0), 143); unsigned bad = 0;
          auto f = [&](int j) { return __uint_as_float(t2[j].x); };
          float c = (((f(0) + f(1)) + (f(2) + f(3))) + ((f(4) + f(5)) + (f(6) + f(7)))) + f(8);
#pragma unroll
          for (int j = 0; j < 9; ++j) bad = tagor(t2[j].y, tag, bad);
          if (UNLIKELY(!__all(bad == 0))) {
            _Pragma("unroll 1") for (unsigned sp = 0; !__all(bad == 0); ++sp) {
              if (sp > SPIN_MAX) { set_flag(P.ws, W_TMO, 1u); break; }
              __builtin_amdgcn_s_sleep(1); asm volatile("" ::: "memory");
              bad = 0;
#pragma unroll
              for (int j = 0; j < 9; ++j) { t2[j] = __builtin_amdgcn_raw_buffer_load_b64(rsWs, t * 8, csg + j * 1152, SC1); bad = tagor(t2[j].y, tag, bad); }
              c = (((f(0) + f(1)) + (f(2) + f(3))) + ((f(4) + f(5)) + (f(6) + f(7)))) + f(8);
            } }
          *(LAS float*)(lds + L_CS + PASS * 576 + t * 4) = c; }
        if (sg == 11) {
          const int t = wm * 64 + opq(lane0), R = 48 * pi + (t >> 1), st = R >> 6, Pp = R & 63, k = (Pp & ~24) | ((Pp & 8) << 1) | ((Pp & 16) >> 1);
          int row = 512 * gc + 128 * ((st - 4) >> 1) + 32 * (st & 1) + k + (k & 32); row = st == 3 ? 2048 + 64 * gc + k : row; row = st < 3 ? 144 * gc + 64 * st + k : row;
          const int vo = t < 96 ? row * 1152 + 1024 + 32 * gu + (t & 1) * 16 : OOB; const rsrc_t rs = mkrs(pi < 4 ? P.wo : P.wout, pi < 4 ? D * D * 2 : FH * D * 2);
          asm volatile("s_nop 4\n\tbuffer_load_dwordx4 %0, %1, %2, 0 offen ; HIDDEN_LOAD" : "=v"(wt) : "v"(vo), "s"(rs) : "memory"); }
        auto svo = [&]() { const int l = opq(lane0); return ((l >> 3) & 1) * 16384 + (32 * wm + 8 * (l >> 4) + (l & 7)) * 16; };
        if (sg == 13 || sg == 14) { const int vo = svo();
#pragma unroll
          for (int j = 4 * (sg - 13); j < 4 * (sg - 12); ++j) HLOAD(gl[j], vo, rsWs, rowb + C_STATS + j * 2048); }
        if (sg == 16) {
          asm volatile("s_waitcnt vmcnt(9) ; HIDDEN_WAIT %0 %1 %2 %3 %4 %5 %6 %7" : "+v"(gl[0]), "+v"(gl[1]), "+v"(gl[2]), "+v"(gl[3]), "+v"(gl[4]), "+v"(gl[5]), "+v"(gl[6]), "+v"(gl[7]) :: "memory");
          float s1, s2; unsigned bad;
          auto red = [&]() { auto a = [&](int j) { return __uint_as_float(gl[j].x); }; auto b = [&](int j) { return __uint_as_float(gl[j].z); }; bad = 0;
            s1 = ((a(0) + a(1)) + (a(2) + a(3))) + ((a(4) + a(5)) + (a(6) + a(7))); s2 = ((b(0) + b(1)) + (b(2) + b(3))) + ((b(4) + b(5)) + (b(6) + b(7)));
#pragma unroll
            for (int j = 0; j < 8; ++j) { bad = tagor(gl[j].y, tag, bad); bad = tagor(gl[j].w, tag, bad); } };
          red();
          if (UNLIKELY(!__all(bad == 0))) {
            _Pragma("unroll 1") for (unsigned sp = 0; !__all(bad == 0); ++sp) {
              if (sp > SPIN_MAX) { set_flag(P.ws, W_TMO, 1u); break; }
              __builtin_amdgcn_s_sleep(1); asm volatile("" ::: "memory");
              const int vo = svo();
#pragma unroll
              for (int j = 0; j < 8; ++j) gl[j] = __builtin_amdgcn_raw_buffer_load_b128(rsWs, vo, rowb + C_STATS + j * 2048, SC1);
              red();
            } }
          s1 = ror8sum(s1); s2 = ror8sum(s2);
          const float mu = s1 * (1.0f / FH), var = fmaxf(s2 * (1.0f / FH) - mu * mu, 0.f);
          tf[1] = __builtin_amdgcn_rsqf(var + 1e-5f); tf[0] = -mu * tf[1]; asm volatile("" : "+v"(tf[0]), "+v"(tf[1])); }
        if (sg == 17) {
          zm[0] = (f32x4){bcast16<0>(tf[0]), bcast16<1>(tf[0]), bcast16<2>(tf[0]), bcast16<3>(tf[0])}; zm[1] = (f32x4){bcast16<4>(tf[0]), bcast16<5>(tf[0]), bcast16<6>(tf[0]), bcast16<7>(tf[0])};
          zr2[0] = (f32x4){bcast16<0>(tf[1]), bcast16<1>(tf[1]), bcast16<2>(tf[1]), bcast16<3>(tf[1])}; zr2[1] = (f32x4){bcast16<4>(tf[1]), bcast16<5>(tf[1]), bcast16<6>(tf[1]), bcast16<7>(tf[1])};
          asm volatile("" : "+v"(zm[0]), "+v"(zm[1]), "+v"(zr2[0]), "+v"(zr2[1])); }
        if (sg == 17) { asm volatile("; HIDDEN_WAIT %0 (covered by the wait of barrier 13)" : "+v"(wt) :: "memory");
          st16<SC1>(wt, rsWs, wm * 1024 + l16 < 1536 ? wm * 1024 + l16 : OOB, colb + C_WT + pi * 1536); }
        if (sg == 17) { z2n_tile(0, gl[0]); z2n_tile(1, gl[1]); z2n_tile(2, gl[2]); }
        if (sg == 19) { const int t = wm * 64 + opq(lane0); u32x4 o;
#pragma unroll
          for (int k = 0; k < 4; ++k) o[k] = __float_as_uint((tf[4 * k] + tf[4 * k + 1]) + (tf[4 * k + 2] + tf[4 * k + 3]));
          st16<SC1>(o, rsWs, t < 180 ? t * 16 : OOB, colb + C_KVP + pi * KVP_STRIDE); }
        if (sg == 21) resid();
        if (sg == 25) resid_in();
        if (sg == 23) vB[1] = wm >= 2 ? OOB : l16;
        if (sg == 26) flags_dma((opq(lane0) & 15) * 4, colb + C_FLAGB);
      }
    };
    auto front = [&](auto SGc) __attribute__((always_inline)) {
      constexpr int sg = decltype(SGc)::value, s = sg - ST0;
      if constexpr (H == 0) {
        if (s == csg_at(PASS)) { const int vo = l16;
#pragma unroll
          for (int k = 0; k < 3; ++k) dma16<SC1>(rsWs, lds + ((k == 2 && wm == 3) ? L_DUMMY : L_LAND + (wm + 4 * k) * 1024), vo, csg + (wm + 4 * k) * 1024); }
        if (sg == 5) dma16<SC1>(rsWs, lds + L_XLAND + wm * 1024, l16, rowb + C_XST + (wm & 1) * 1024);
        if (sg == 21) raise(colb + C_FLAGB + pi * 4);
        if (sg == 22) { vA[0] = rvoff(4096); vA[1] = vA[0] ^ 64; }
        if (sg == 19) resid();
        if (sg == 24) resid_in();
      } else {
        if (s == csg_at(PASS) + 2) { LAS char* const a = pin(lds + L_LAND + min(wm * 512 + (l16 >> 1), 143 * 8));
#pragma unroll
          for (int j = 0; j < 9; ++j) t2[j] = *(LAS u32x2*)(a + j * 1152); }
        if (sg == 7) gl[0] = *(LAS u32x4*)(lds + L_XLAND + wm * 1024 + l16);
        if (sg == 16) { gl[0] = z2_read(0); gl[1] = z2_read(1); gl[2] = z2_read(2); }
      }
    };
#pragma unroll
    for (int n = NFP; n < NF; ++n) { acc[n][0] = (f32x4){0, 0, 0, 0}; acc[n][1] = (f32x4){0, 0, 0, 0}; }
    if (PASS == 0) { rdX(0, 0); rdX(0, 1); rdX(0, 2); rdX(0, 3); }
#pragma unroll
    for (int j = 0; j < 8; ++j) rdB(j >> 2, j & 3);
    if (PASS == 0) ring_all(2);
    bf16x8 ones8; int gq;
    if (PASS == 0) { ones8 = __builtin_bit_cast(bf16x8, (u32x4){ONES, ONES, ONES, ONES}); asm volatile("" : "+v"(ones8)); gq = opq(lane0) >> 4; }
    SB();
    sfor<0, NU>([&](auto Uc) __attribute__((always_inline)) {
      constexpr int u = decltype(Uc)::value, s = u / NFP, n = u % NFP, sg = ST0 + s, d = u - (NFP - 2);
      if (n == NFP - 2) {
        slot(IC<sg>{});
        if constexpr (H == 1) waitv<allow1(sg)>();
        else { if (sg == 2 || sg == 3) waitv<6>(); else if (sg == 6) waitv<8>(); else if (sg < 7 || sg == 26) waitv<4>(); else if (s == csg_at(PASS) + 2) waitv<0>(); }
        if ((sg == 8 && H == 1) || (sg == 18 && H == 0)) waitl0();
        bar();
        if (H == 1 && sg == 18) zp[0] = *(LAS u32x2*)(lds + L_TAB + min(wm * 64 + opq(lane0), 179) * 16);
        SB();
      }
      const bf16x8 b0 = cat4(bq[u % 3][0], bq[u % 3][1]), b1 = cat4(bq[u % 3][2], bq[u % 3][3]);
#pragma unroll
      for (int j = 0; j < 4; ++j) { const int it = j >> 1, hh = j & 1; const bf16x8 &bb = hh ? b1 : b0, &oa = TR ? bb : xf[s][it][hh], &ob = TR ? xf[s][it][hh] : bb;
        if (s == 0 && hh == 0) mf0(acc[n][it], oa, ob); else mfa(acc[n][it], oa, ob);
        SB();
        rdB(u + 2, j);
        if (PASS == 0 && n == NFP - 2 && s < 8) rdX(s + 1, j);
        if (j == 3) {
          if (PASS == 0 && u >= 1 && u <= NFP) { asm volatile("s_nop 0" : "+v"(ct)); if (u - 1 < 4) csv = gq == u - 1 ? ct.x : csv; else cs5 = ct.x; asm volatile("" : "+v"(csv), "+v"(cs5)); }
          if (n == NFP - 2) front(IC<sg>{});
          if (H == 1 && sg == 19 && n == 0) {
#pragma unroll
            for (int k = 0; k < 4; ++k) { LAS char* const a = pin(lds + L_KVS + ((k & 1) ? zp[0][k >> 1] >> 16 : zp[0][k >> 1] & 0xffffu));
#pragma unroll
              for (int w = 0; w < 4; ++w) tf[4 * k + w] = *(LAS float*)(a + w * 8192); } }
          if constexpr (d >= 0 && !(H == 1 && ST0 + d / NFP == 26)) ring_pos(ST0 + d / NFP + 3, d % NFP, NFP); }
        if (u >= NU - 2) tailjob((u - (NU - 2)) * 4 + j);
        SB(); }
      if (sg == 0) { mf0(ct, ones8, b0); mfa(ct, ones8, b1); }
      SB();
    });
#pragma unroll
    for (int k = 2; k < NFP; ++k) if (!(H == 1 && PASS == 2)) ring_pos(ST0 + 11, k, NFP);
    accfence(acc);
    relane();
  };
  f32x2 nm[4], rs[4]; float cn[NF];
  auto tables = [&](int ps, int k) {
    if (k == 0) { LAS char* const a = pin(lds + L_XSTAT + wm * 128 + (opq(lane0) & 48));
#pragma unroll
      for (int it = 0; it < 2; ++it) { const f32x4 m = *(LAS f32x4*)(a + it * 64), r = *(LAS f32x4*)(a + 512 + it * 64); nm[2 * it] = lo2(m); nm[2 * it + 1] = hi2(m); rs[2 * it] = lo2(r); rs[2 * it + 1] = hi2(r); } }
    if (k == 4) { LAS char* const a = pin(lds + L_CS + ((l16 >> 2) & 60));
#pragma unroll
      for (int n = 0; n < NF; ++n) cn[n] = *(LAS float*)(a + ps * 576 + (NB + n) * 64); } };
  auto affN = [&](int n, f32x2 (&v)[4]) { const f32x2 c2 = {cn[n], cn[n]};
    v[0] = fma2(nm[0], c2, lo2(acc[n][0])) * rs[0]; v[1] = fma2(nm[1], c2, hi2(acc[n][0])) * rs[1]; v[2] = fma2(nm[2], c2, lo2(acc[n][1])) * rs[2]; v[3] = fma2(nm[3], c2, hi2(acc[n][1])) * rs[3]; };

  if constexpr (H == 1) { __builtin_amdgcn_s_setprio(1); waitv<8>(); } else waitv<4>();
  bar();
  pass(IC<0>{}, [&](int k) { tables(0, k); });
  if constexpr (H == 0) {
    dma4(mkrs(P.wsp, S * S * 2), lds + L_DUMMY, lane * 128, (blockIdx.x * 4 + wm) * 8192);
    if (wm == 0) dma4(mkrs(P.bsp, S * 2), lds + L_BIAS, lane * 4, pi * 256);
  }
  {
    zsw = pin(lds + L_STASH + wm * 9216 + fr * 64 + (((g >> 1) ^ ((fr >> 2) & 1)) * 32) + (g & 1) * 16);
    LAS char* const sw = zsw + NB * 1024;
    const int sro = L_STASH + (wm * 9 + NB) * 1024 + (lane >> 2) * 64 + (g & 1) * 32 + (fr & 3) * 8;
    LAS char* const sr = pin(lds + sro);
    LAS char* const sr1 = pin(lds + (sro ^ 32));
#pragma unroll
    for (int n = 0; n < NF; ++n) { f32x2 v[4]; affN(n, v);
      *(LAS u32x4*)(sw + n * 1024) = (u32x4){pk2v(gelu2(v[0])), pk2v(gelu2(v[1])), pk2v(gelu2(v[2])), pk2v(gelu2(v[3]))}; }
    asm volatile("" ::: "memory");
    float s1[2] = {0.f, 0.f}, s2[2] = {0.f, 0.f};
#pragma unroll
    for (int n = 0; n < NF; ++n)
#pragma unroll
      for (int ih = 0; ih < 2; ++ih) { const u32x2 t = __builtin_bit_cast(u32x2, __builtin_amdgcn_ds_read_tr16_b64_v4i16((LAS s16x4*)((ih ? sr1 : sr) + n * 1024)));
        s1[ih] = dot2(t.x, ONES, s1[ih]); s2[ih] = dot2(t.x, t.x, s2[ih]); s1[ih] = dot2(t.y, ONES, s1[ih]); s2[ih] = dot2(t.y, t.y, s2[ih]); }
    auto rs16 = [](float x, float y) { const auto r = __builtin_amdgcn_permlane16_swap(__float_as_uint(x), __float_as_uint(y), false, false); return __uint_as_float(r[0]) + __uint_as_float(r[1]); };
    const auto r = __builtin_amdgcn_permlane32_swap(__float_as_uint(rs16(s1[0], s1[1])), __float_as_uint(rs16(s2[0], s2[1])), false, false);
    *(LAS float*)(lds + L_ZPART + (H * 128 + 32 * wm + (lane & 31)) * 8 + (lane >> 5) * 4) = __uint_as_float(r[0]) + __uint_as_float(r[1]);
    if constexpr (H == 1) {
      cs_early(1);
#pragma unroll
      for (int i = 0; i < 7; ++i) sentinel(i); }
    waitl0();
  }
  bar();
  if constexpr (H == 1) { const int r = wm * 64 + lane; const f32x2 a = *(LAS f32x2*)(lds + L_ZPART + (r & 127) * 8) + *(LAS f32x2*)(lds + L_ZPART + 1024 + (r & 127) * 8);
    st16<SC1>((u32x4){__float_as_uint(a.x), tag, __float_as_uint(a.y), tag}, rsWs, r < 128 ? r * 16 : OOB, rowb + C_STATS + pj * 2048); }
  pass(IC<1>{}, [&](int k) { if constexpr (H == 0) tables(1, k); });
  auto w3warm = [&]() { const int l = opq(lane0), li = (wm & 1) * 64 + l;
    if (wm < 2) dma4(rsWout, lds + L_DUMMY, li < 81 ? li * 128 : OOB, blockIdx.x * 81 * 128);
    else if (wm == 2) dma4(rsWo, lds + L_DUMMY, l < 21 ? (blockIdx.x * 21 + l) * 128 : OOB, 0); };
  u32x2 pq[3][2];
  f32x4 aq[3][2];
  if constexpr (H == 0) {
    w3warm();
    *(LAS u32x4*)(lds + L_KVF + (wm * 64 + lane) * 16) = (u32x4){0, 0, 0, 0};
    *(LAS u32x2*)(lds + L_KVF + 4096 + (wm * 64 + lane) * 8) = (u32x2){0, 0};
    LAS char* const kw = pin(lds + L_KVS + wm * 8192 + l16);
    bf16x8 pkf[3], vf[3];
#pragma unroll
    for (int n = 0; n < 5; ++n) { f32x2 v[4]; affN(n, v);
      if (n >= 2) { u32x4 w = {pk2v(v[0]), pk2v(v[1]), pk2v(v[2]), pk2v(v[3])};
        if (n == 4 && fr == 0) w = (u32x4){ONES, ONES, ONES, ONES};
        vf[n - 2] = __builtin_bit_cast(bf16x8, w); }
      if (n < 2 || n == 4) pkf[n == 4 ? 2 : n] = __builtin_bit_cast(bf16x8, (u32x4){pk2v(phi2(v[0])), pk2v(phi2(v[1])), pk2v(phi2(v[2])), pk2v(phi2(v[3]))});
    }
#pragma unroll
    for (int td = 0; td < 3; ++td)
#pragma unroll
      for (int te = 0; te < 3; ++te) { if (td == 2 && te == 0) continue;
        *(LAS f32x4*)(kw + (td * 3 + te - (td == 2)) * 1024) = mfma(pkf[td], vf[te], (f32x4){0, 0, 0, 0}); }
  } else {
    u32x4 z[6];
#pragma unroll
    for (int n = 0; n < 6; ++n) z[n] = z2_read(3 + n);
#pragma unroll
    for (int n = 0; n < 6; ++n) z2n_tile(3 + n, z[n]);
    cs_early(2);
#pragma unroll
    for (int n = 0; n < 3; ++n) { aq[n][0] = acc[n][0]; aq[n][1] = acc[n][1]; }
  }
  constexpr int NR = NF + 2;
  LAS char *aZ, *aZt;
    bf16x8 fa[2][NR], fb[2][NR];
    auto rd1 = [&](bf16x8 (&f)[2][NR], int i, int slot) {
      const int h = i & 1; int ii = i >> 1; if (ii == 1) ii = 2; else if (ii == 2) ii = 1;
      if (ii < 2) f[h][ii] = *(LAS bf16x8*)((h ? aR1 : aR0) + slot * R_SZ + ii * 2048); else if (ii < 6) f[h][ii] = *(LAS bf16x8*)(aZ + slot * CM_SZ + (h * 8 + ii - 2) * 1024); else f[h][ii] = *(LAS bf16x8*)(aZt + slot * CT_SZ + h * 1024);
    };
  pass(IC<2>{}, [&](int k) { if (k == 0) { aZ = pin(lds + CM_OFF + H * 4096 + l16); aZt = pin(lds + CT_OFF + l16); mk_aR(); }
    if (2 * k < 2 * NR) rd1(fa, 2 * k, 3); if (2 * k + 1 < 2 * NR) rd1(fa, 2 * k + 1, 3); });
  u32x2 y[NF][2];
  auto ystore = [&](int k) { const int hh = (k >> 1) & 1, it = k & 1;
    if (k < 4) st16<SC1>(__builtin_bit_cast(u32x4, cat(y[2 * hh][it], y[2 * hh + 1][it])), rsWs, l16, yb + (4 + 2 * gu + hh) * 16384 + H * 8192 + (2 * wm + it) * 1024);
    else __builtin_amdgcn_raw_buffer_store_b64(y[NF - 1][it], rsWs, l16 >> 1, yb + 3 * 16384 + (gu >> 1) * 8192 + (2 * wm + it) * 1024 + (gu & 1) * 512, SC1); };
  f32x4 ag[NF][2];
  {
    f32x4 kvs = {0, 0, 0, 0}; u32x4 kl[2][2]; int kvo;
    if constexpr (H == 1) { const int t = wm * 64 + lane; kvo = t < 180 ? t * 16 : OOB; }
    float zn[2], zr[2], za, zb, ta, tb, z1[NF][2][4]; f32x2 zc; f32x4 bv[2];
    LAS char* const aCs = lds + L_CS + 576 + (NB * 16 + 4 * g) * 4;
#pragma unroll
    for (int it = 0; it < 2; ++it) { zn[it] = *(LAS float*)(lds + L_XSTAT + (32 * wm + 16 * it + fr) * 4); zr[it] = *(LAS float*)(lds + L_XSTAT + 512 + (32 * wm + 16 * it + fr) * 4);
      const float b = blo(*(LAS u16*)(lds + L_BIAS + (32 * wm + 8 * (fr >> 2) + 4 * it + (fr & 3)) * 2)); bv[it] = (f32x4){b, b, b, b}; asm volatile("" : "+v"(bv[it])); }
    float GC1 = 2.3022082f; asm volatile("" : "+v"(GC1));
    auto zop = [&](int e, int k) { const int n = e >> 2, it = (e >> 1) & 1, h = e & 1;
      if (k == 0) zc = *(LAS f32x2*)(aCs + 576 + n * 64 + h * 8);
      if (k == 3) asm volatile("v_fma_f32 %0, %2, %4, %5\n\tv_fma_f32 %1, %3, %4, %6" : "=&v"(za), "=&v"(zb) : "v"(zc.x), "v"(zc.y), "v"(zn[it]), "v"(acc[n][it][2 * h]), "v"(acc[n][it][2 * h + 1]));
      if (k == 4) asm volatile("v_mul_f32 %0, %0, %2\n\tv_mul_f32 %1, %1, %2" : "+v"(za), "+v"(zb) : "v"(zr[it]));
      if (k == 5) asm volatile("v_mul_f32 %0, %2, %2\n\tv_mul_f32 %1, %3, %3" : "=&v"(ta), "=&v"(tb) : "v"(za), "v"(zb));
      if (k == 6) asm volatile("v_fmamk_f32 %0, %0, 0x3dd2d3e8, %2\n\tv_fmamk_f32 %1, %1, 0x3dd2d3e8, %2" : "+v"(ta), "+v"(tb) : "v"(GC1));
      if (k == 7) asm volatile("v_mul_f32 %0, %0, %2\n\tv_mul_f32 %1, %1, %3" : "+v"(ta), "+v"(tb) : "v"(za), "v"(zb));
      if (k == 8) asm volatile("v_exp_f32 %0, %0" : "+v"(ta));
      if (k == 9) asm volatile("v_exp_f32 %0, %0" : "+v"(tb));
      if (k == 10) asm volatile("v_add_f32 %0, 1.0, %0\n\tv_add_f32 %1, 1.0, %1" : "+v"(ta), "+v"(tb));
      if (k == 11) asm volatile("v_rcp_f32 %0, %0" : "+v"(ta));
      if (k == 12) asm volatile("v_rcp_f32 %0, %0" : "+v"(tb));
      if (k == 13) { float oa, ob; asm volatile("v_fma_f32 %0, -%2, %4, %2\n\tv_fma_f32 %1, -%3, %5, %3" : "=&v"(oa), "=&v"(ob) : "v"(za), "v"(zb), "v"(ta), "v"(tb)); z1[n][it][2 * h] = oa; z1[n][it][2 * h + 1] = ob; } };
    auto ytile = [&](int n, int it) { float t0, t1, t2, t3;
      asm volatile("v_mul_f32 %0, %4, %8\n\tv_mul_f32 %1, %5, %9\n\tv_mul_f32 %2, %6, %10\n\tv_mul_f32 %3, %7, %11" : "=&v"(t0), "=&v"(t1), "=&v"(t2), "=&v"(t3)
                   : "v"(z1[n][it][0]), "v"(z1[n][it][1]), "v"(z1[n][it][2]), "v"(z1[n][it][3]), "v"(ag[n][it].x), "v"(ag[n][it].y), "v"(ag[n][it].z), "v"(ag[n][it].w));
      y[n][it] = (u32x2){pk2(t0, t1), pk2(t2, t3)};
      asm volatile("" : "+v"(y[n][it]));
      if (n < 4 && (n & 1)) *(LAS u32x4*)(lds + ((n >> 1) ? L_YOWN : R_OFF + 2 * R_SZ) + H * 8192 + (2 * wm + it) * 1024 + l16) = __builtin_bit_cast(u32x4, cat(y[n - 1][it], y[n][it]));
      if (n == 1) ystore(it); };
    auto qop = [&](int e, int k) { const int n = e >> 2, it = (e >> 1) & 1, h = e & 1;
      if (k == 0) zc = *(LAS f32x2*)(aCs + n * 64 + h * 8);
      if (k == 3) asm volatile("v_fma_f32 %0, %2, %4, %5\n\tv_fma_f32 %1, %3, %4, %6" : "=&v"(za), "=&v"(zb) : "v"(zc.x), "v"(zc.y), "v"(zn[it]), "v"(aq[n][it][2 * h]), "v"(aq[n][it][2 * h + 1]));
      if (k == 4) asm volatile("v_mul_f32 %0, %0, %2\n\tv_mul_f32 %1, %1, %2" : "+v"(za), "+v"(zb) : "v"(zr[it]));
      if (k == 5) asm volatile("v_min_f32 %0, 0, %2\n\tv_min_f32 %1, 0, %3" : "=&v"(ta), "=&v"(tb) : "v"(za), "v"(zb));
      if (k == 6) asm volatile("v_mul_f32 %0, 0x3fb8aa3b, %0\n\tv_mul_f32 %1, 0x3fb8aa3b, %1" : "+v"(ta), "+v"(tb));
      if (k == 7) asm volatile("v_exp_f32 %0, %0" : "+v"(ta));
      if (k == 8) asm volatile("v_exp_f32 %0, %0" : "+v"(tb));
      if (k == 9) asm volatile("v_max_f32 %0, 0, %0\n\tv_max_f32 %1, 0, %1" : "+v"(za), "+v"(zb));
      if (k == 10) asm volatile("v_add_f32 %0, %0, %2\n\tv_add_f32 %1, %1, %3" : "+v"(za), "+v"(zb) : "v"(ta), "v"(tb));
      if (k == 11) asm volatile("v_add_f32 %0, 0x358637bd, %0\n\tv_add_f32 %1, 0x358637bd, %1" : "+v"(za), "+v"(zb));
      if (k == 12) { unsigned o; asm volatile("v_cvt_pk_bf16_f32 %0, %1, %2" : "=v"(o) : "v"(za), "v"(zb)); pq[n][it][h] = o; } };
    auto kvscatter = [&]() { const int t = wm * 64 + opq(lane0); const u32x2 tb = *(LAS u32x2*)(lds + L_TAB + min(t, 179) * 16 + 8);
      const unsigned k01 = pk2v(lo2(kvs)), k23 = pk2v(hi2(kvs)), msk = t < 180 ? 0xffffu : 0u; LAS char* const a = lds + (t < 180 ? L_KVF : L_DUMMY);
      *(LAS u16*)(a + (tb.x & msk)) = (u16)k01; *(LAS u16*)(a + ((tb.x >> 16) & msk)) = (u16)(k01 >> 16); *(LAS u16*)(a + (tb.y & msk)) = (u16)k23; *(LAS u16*)(a + ((tb.y >> 16) & msk)) = (u16)(k23 >> 16); };
    bf16x8 kf[3][2]; f32x4 om[2][3];
    auto oread = [&]() {
#pragma unroll
      for (int en = 0; en < 6; ++en) kf[en >> 1][en & 1] = *(LAS bf16x8*)(lds + L_KVF + en * 1024 + l16); };
    auto omm = [&]() {
#pragma unroll
      for (int it = 0; it < 2; ++it) { const bf16x8 b0 = cat(pq[0][it], pq[1][it]), b1 = cat(pq[2][it], (u32x2){0, 0});
#pragma unroll
        for (int en = 0; en < 3; ++en) { om[it][en] = mfma(kf[en][1], b1, mfma(kf[en][0], b0, (f32x4){0, 0, 0, 0})); asm volatile("" : "+v"(om[it][en])); } } };
    auto row1 = [&](float v) { const unsigned u = __float_as_uint(v), a = __builtin_amdgcn_permlane16_swap(u, u, false, false)[1]; return __uint_as_float(__builtin_amdgcn_permlane32_swap(a, a, false, false)[0]); };
    auto ostep = [&](int it) { const int l = opq(lane0), r = l & 15, gg = l >> 4;
      const float rA = __builtin_amdgcn_rcpf(row1(om[it][2][0]) + 1e-6f), rB = __builtin_amdgcn_rcpf(row1(om[it][2][1]) + 1e-6f), rM = gg == 0 ? rA : rB;
#pragma unroll
      for (int en = 0; en < 3; ++en) { const float r01 = en == 0 ? rA : en == 1 ? rM : rB, r23 = en == 0 ? rA : rB;
        const u32x2 o = {pk2v(lo2(om[it][en]) * r01), pk2v(hi2(om[it][en]) * r23)};
        const int oc4 = 9 * gu + 4 * en + gg;
        int vo = (oc4 >> 3) * 8192 + (oc4 & 7) * 128 + r * 8;
        if (en == 2) vo = gg != 0 ? OOB : vo;
        __builtin_amdgcn_raw_buffer_store_b64(o, rsWs, vo, yb + (2 * wm + it) * 1024, SC1); } };
    auto step = [&](bf16x8 (&cur)[2][NR], bf16x8 (&nxt)[2][NR], auto Pc) {
      constexpr int p = decltype(Pc)::value, st = 30 + p, e = p - (H ? 12 : 1);
      auto vop = [&](int k) { if (H == 1 && p < 12) qop(p, k); else if (e >= 0 && e < 4 * NF) zop(e, k); };
      if constexpr (H == 1) {
        if (p == 0) { waitv<0>(); check_flags(colb + C_FLAGB + (lane0 & 15) * 4);
#pragma unroll
          for (int k = 0; k < 5; ++k) ring(29, k, 2); }
        if (p >= 3 && p <= 10) { if (p <= 9) asm volatile("s_waitcnt vmcnt(12) ; HIDDEN_WAIT %0 %1" : "+v"(kl[p & 1][0]), "+v"(kl[p & 1][1]) :: "memory");
                                 else asm volatile("s_waitcnt vmcnt(10) ; HIDDEN_WAIT %0 %1" : "+v"(kl[p & 1][0]), "+v"(kl[p & 1][1]) :: "memory");
          kvs += __builtin_bit_cast(f32x4, kl[p & 1][0]) + __builtin_bit_cast(f32x4, kl[p & 1][1]); asm volatile("" : "+v"(kvs)); }
        if (p >= 1 && p <= 8) { HLOAD(kl[p & 1][0], kvo, rsWs, colb + C_KVP + (2 * p - 2) * KVP_STRIDE); HLOAD(kl[p & 1][1], kvo, rsWs, colb + C_KVP + (2 * p - 1) * KVP_STRIDE); }
        if (p == 11) kvscatter();
        if (p == 13) oread(); if (p == 14) omm(); if (p == 15) { ostep(0); ostep(1); } if (p == 20) cvoff(vA, 2);
        if (p == 27) flags_dma((opq(lane0) & 15) * 4, rowb + C_FLAGO);
        if (p == 29) { waitv<10>(); check_flags(rowb + C_FLAGO + (lane0 & 15) * 4); }
        waitv<allow1(27 + p)>();
      } else { if (p == 18) waitv<6>(); else waitv<4>(); }
      if (p == 31) waitl0();
      bar();
      SB();
      if constexpr (H == 0) {
        if (p == 17) raise(rowb + C_FLAGO + pj * 4);
        if (p == 17) raise(W_EPOCH, blockIdx.x == 0);
      }
      if (p == 31) {
        if constexpr (H == 1) { ring(61, 4); ring(61, 0); ring(61, 1); ring_all(62); }
        else { dma16<SC1>(rsWs, lds + R_OFF + R_SZ + wm * 2048, l16, yb + 2 * 16384 + wm * 2048); dma16<SC1>(rsWs, lds + R_OFF + R_SZ + wm * 2048 + 1024, l16, yb + 2 * 16384 + wm * 2048 + 1024); } } else ring(st, 4, p + 3);
      SB();
      vop(0);
#pragma unroll
      for (int i = 0; i < 4 * NF; ++i) {
        const int n = i >> 2, m = (i >> 1) & 1, h = i & 1;
        if (p == 0 && h == 0) mfc(ag[n][m], cur[0][2 + n], cur[0][m], bv[m]); else mfa(ag[n][m], cur[h][2 + n], cur[h][m]);
        SB();
        if (p != 31 && i < 2 * NR) rd1(nxt, i, p & 3);
        if (i > 0) vop(i);
        if (p == 31 && i >= 5 && (i & 1)) { asm volatile("s_nop 3" : "+v"(ag[(i - 5) >> 2][((i - 5) >> 1) & 1])); ytile((i - 5) >> 2, ((i - 5) >> 1) & 1); }
        if ((i & 3) == 3 && i < 16 && p != 31) ring(st, i >> 2, p + 3);
        SB();
      }
    };
    sfor<0, 16>([&](auto Kc) __attribute__((always_inline)) { constexpr int k = decltype(Kc)::value; step(fa, fb, IC<2 * k>{}); step(fb, fa, IC<2 * k + 1>{}); });
    accfence(ag);
    ytile(NF - 1, 0); ytile(NF - 1, 1);
  }
  relane();
  {
    LAS char *const aY = pin(lds + R_OFF + 2 * wm * 1024 + l16), *const aYh = pin(lds + R_OFF + 2 * wm * 1024 + (l16 >> 1)), *const aYo = pin(lds + L_YOWN + 2 * wm * 1024 + l16);
    bf16x8 rf[2][2][2]; s16x4 bq[3][4];
    mk_aC(gc + 1);
    constexpr int UL = 10 * NF;
    auto uq = [](int u) constexpr { const int t = u - UL; return u < UL ? u / NF : t < 4 ? 10 + (t >> 1) : 10 + (t & 1); };
    auto un = [](int u) constexpr { const int t = u - UL, f = t < 4 ? (t & 1) : (t >> 1); return u < UL ? u % NF : (NF == 5 && f >= 3) ? 7 - f : f; };
    auto rdB = [&](int u, int j) { if (u < 12 * NF && (uq(u) != 2 || j < 2)) bq[u % 3][j] = trC(59 + uq(u), un(u), j); };
    auto rdY = [&](int q, int j) { const int it = j >> 1, hh = j & 1, o = hh * 8192 + it * 1024, so = ((59 + q) & 3) * R_SZ + o;
      if (q == 2) { if (hh == 0) rf[q & 1][it][0] = cat(*(LAS u32x2*)(aYh + so), (u32x2){0, 0}); }
      else if (q < 2 || q == 11) rf[q & 1][it][hh] = cat(*(LAS u32x2*)(aYh + so), *(LAS u32x2*)(aYh + so + 512));
      else rf[q & 1][it][hh] = *(LAS bf16x8*)(q == 4 ? aYo + o : aY + so); };
    rdY(0, 0); rdY(0, 1); rdY(0, 2); rdY(0, 3);
#pragma unroll
    for (int j = 0; j < 8; ++j) rdB(j >> 2, j & 3);
    SB();
    auto sendp = [&](int n) { asm volatile("s_nop 3" : "+v"(acc[n][0]), "+v"(acc[n][1]));
      u32x4 v = {pk2v(lo2(acc[n][0])), pk2v(hi2(acc[n][0])), pk2v(lo2(acc[n][1])), pk2v(hi2(acc[n][1]))};
      v.x = min(v.x, 0xfffffffeu); v.y = min(v.y, 0xfffffffeu); v.z = min(v.z, 0xfffffffeu); v.w = min(v.w, 0xfffffffeu);
      const int dg = n < 4 ? (n + gc + 1) & 3 : wm, slot = n < 4 ? H * 4 + wm : 8;
      st16<SC1>(v, rsWs, dg == gc ? OOB : l16, rowb + C_PART + ((4 * dg + gu) * 4 + gc) * PART_SRC + slot * 1024); };
    sfor<0, 12 * NF>([&](auto Uc) __attribute__((always_inline)) {
      constexpr int u = decltype(Uc)::value, q = uq(u), n = un(u), bq_ = u < UL ? (n == NF - 2 ? q : -1) : (u == UL ? 10 : -1);
      if (bq_ >= 0) {
        if constexpr (H == 1) { if (bq_ == 0 || bq_ == 10) waitv<0>(); else if (bq_ >= 3) waitv<5>();
          if (bq_ == 3 || bq_ == 5) check_flags(rowb + C_FLAGD + gc * 128 + (bq_ - 3) * 8 + (lane0 & 3) * 4); }
        else { if (bq_ == 0 || bq_ == 3 || bq_ == 10) waitv<0>(); else if (bq_ == 4 || bq_ == 5) waitv<5>(); else if (bq_ >= 6) waitv<4>(); }
        bar();
        if constexpr (H == 0) {
          if (bq_ == 0) { raise(rowb + C_FLAGD + gc * 128 + gu * 4); }
          if (bq_ == 3) { ring_all(64); ring_all(65); raise(rowb + C_FLAGD + gc * 128 + 16 + gu * 4); }
        }
        if (bq_ == 0) { ystore(2); ystore(3); if constexpr (H == 0) { ystore(4); ystore(5); } }
        if (H == 1 && (bq_ == 2 || bq_ == 4)) flags_dma((opq(lane0) & 3) * 4, rowb + C_FLAGD + gc * 128 + (bq_ - 2) * 8);
        if (bq_ == 8) ring_all(70);
        if (bq_ == 10) ro = mk_ro();
        SB();
      }
      const bf16x8 b0 = cat4(bq[u % 3][0], bq[u % 3][1]), b1 = q == 2 ? b0 : cat4(bq[u % 3][2], bq[u % 3][3]);
#pragma unroll
      for (int j = 0; j < 4; ++j) { const int it = j >> 1, hh = j & 1;
        if (q == 0 && hh == 0) mf0(acc[n][it], b0, rf[q & 1][it][hh]); else if (q != 2 || hh == 0) mfa(acc[n][it], hh ? b1 : b0, rf[q & 1][it][hh]);
        SB();
        rdB(u + 2, j);
        if (bq_ >= 0) rdY(bq_ + 1, j);
        if (j == 3) { const int d = u - (NF - 2); if (d >= 0) { const int qq = d / NF + 3; if (qq < 11 && qq >= (H ? 4 : 7)) ring_pos(59 + qq, d % NF, NF); } }
        if (j == 2 && u > UL && uq(u - 1) == 11) sendp(un(u - 1));
        SB(); }
    });
    accfence(acc);
  }
  {
    __builtin_amdgcn_s_setprio(0);
    const rsrc_t rsOut = mkrs(P.out + (size_t)pi * 128 * D, 128 * D * 2);
    f32x4 kp[NK][2];
#pragma unroll
    for (int it = 0; it < 2; ++it) { kp[0][it] = acc[3][it]; if constexpr (H == 0) kp[1][it] = acc[4][it]; }
    const int pb = rowb + C_PART + pj * 4 * PART_SRC;
    u32x4 v[NK][3];
    __builtin_amdgcn_s_sleep(2);
    _Pragma("unroll 1") for (unsigned sp = 0;; ++sp) {
      unsigned mx = 0;
#pragma unroll
      for (int k = 0; k < NK; ++k)
#pragma unroll
        for (int j = 0; j < 3; ++j) { const int so = pb + ((gc + 1 + j) & 3) * PART_SRC + (k ? 8 : H * 4 + wm) * 1024;
          v[k][j] = __builtin_amdgcn_raw_buffer_load_b128(rsWs, l16 + (k ? off1 : 0), so, SC1);
          mx = max(max(mx, max(v[k][j].x, v[k][j].y)), max(v[k][j].z, v[k][j].w)); }
      const bool done = __all(mx != ~0u) || sp > SPIN_MAX;
      const int rok = done ? ro : OOB;
#pragma unroll
      for (int k = 0; k < NK; ++k) { f32x2 a[2][2];
#pragma unroll
        for (int it = 0; it < 2; ++it) { a[it][0] = lo2(kp[k][it]); a[it][1] = hi2(kp[k][it]);
#pragma unroll
          for (int j = 0; j < 3; ++j) { a[it][0] += unpk(v[k][j][2 * it]); a[it][1] += unpk(v[k][j][2 * it + 1]); } }
#pragma unroll
        for (int e = 0; e < 4; ++e) { const auto r = __builtin_amdgcn_permlane16_swap(__float_as_uint(a[0][e >> 1][e & 1]), __float_as_uint(a[1][e >> 1][e & 1]), false, false);
          a[0][e >> 1][e & 1] = __uint_as_float(r[0]); a[1][e >> 1][e & 1] = __uint_as_float(r[1]); }
        st16<SC1>((u32x4){pk2v(a[0][0] + unpk(xr[k].x)), pk2v(a[0][1] + unpk(xr[k].y)), pk2v(a[1][0] + unpk(xr[k].z)), pk2v(a[1][1] + unpk(xr[k].w))}, rsOut, rok | (k ? off1 : 0), cb[k]); }
      if (done) break;
      __builtin_amdgcn_s_sleep(4);
    }
  }
  if constexpr (H == 0) asm volatile(".Lmk_end:");
}

__global__ __launch_bounds__(512, 2) void fused(const u16* x, const u16* wqkv, const u16* wo, const u16* win, const u16* wsp, const u16* bsp, const u16* wout, u16* out, char* ws) {
  extern __shared__ __attribute__((aligned(16))) char smem[];
  LAS char* lds = (LAS char*)smem;
  const int lane = threadIdx.x & 63, wid = __builtin_amdgcn_readfirstlane(threadIdx.x >> 6);
  const int b = blockIdx.x, xc = b & 7, q = b >> 3;
  const int pi = 8 * (xc & 1) + (q & 7), pj = 4 * (xc >> 1) + (q >> 3);
  const Ptrs P{x, wqkv, wo, win, wsp, bsp, wout, out, ws};
  unsigned long long pc; int clen;
  asm volatile("s_getpc_b64 %0\n.Lmk_pc:\n\ts_mov_b32 %1, .Lmk_end-.Lmk_pc" : "=s"(pc), "=s"(clen));
  const rsrc_t rsCode = mkrs((const void*)pc, clen);
  dma4(rsCode, lds + L_DUMMY, wid == 7 ? lane * 128 + (q & 15) * 8192 : OOB, 0);
  if (wid < 4) body<0>(P, lds, wid, lane, pi, pj); else body<1>(P, lds, wid - 4, lane, pi, pj);
}
}

extern "C" void kernel_launch(void* const* d_in, const int* in_sizes, int n_in, void* d_out, int out_size, void* d_ws, size_t ws_size, hipStream_t stream) {
  using mk::u16;
  (void)hipFuncSetAttribute((const void*)mk::fused, hipFuncAttributeMaxDynamicSharedMemorySize, mk::LDS_TOTAL);
  mk::fused<<<256, 512, mk::LDS_TOTAL, stream>>>((const u16*)d_in[0], (const u16*)d_in[1], (const u16*)d_in[2], (const u16*)d_in[4], (const u16*)d_in[5], (const u16*)d_in[6],
                                                 (const u16*)d_in[7], (u16*)d_out, (char*)d_ws);
}
```
